```python
import torch

def _softmax(x, axis=-1):
    return torch.softmax(x, dim=axis)
def _gelu(x):
    return torch.nn.functional.gelu(x, approximate="tanh")
def _silu(x):
    return torch.nn.functional.silu(x)
def _sigmoid(x):
    return torch.sigmoid(x)
def _layernorm(x, axis=-1, eps=1e-5):
    m = x.mean(dim=axis, keepdim=True)
    v = x.var(dim=axis, keepdim=True, unbiased=False)
    return (x - m) / torch.sqrt(v + eps)
def _scatter_add(tgt, idx, src, axis=0):
    return tgt.clone().index_add_(axis, idx, src)
def _segment_sum(src, seg, n):
    out = torch.zeros((n,) + src.shape[1:], dtype=src.dtype)
    return out.index_add_(0, seg, src)
def _pad(x, pw):
    flat = tuple(v for pair in reversed(pw) for v in pair)
    return torch.nn.functional.pad(x, flat)

kernel_name = 'sequential_preLN__ssm_conv_s4__glu_gated_bilinear__prompt'

def setup_inputs():

    _w = (lambda a: (lambda t: t.to(torch.bfloat16) if t.is_floating_point() else t)(torch.as_tensor(a)))
    def _A_setup():
        B, L, D, N = 1, 2048, 448, 256
        A_re = -(lambda _x: torch.exp(torch.as_tensor(_x)))((lambda *s: torch.randn(*s, dtype=torch.bfloat16) * 3.2)(D, N))
        A_im = torch.pi * (lambda *s: torch.randn(*s, dtype=torch.bfloat16) * 0.05)(D, N)
        B_re = (lambda *s: torch.randn(*s, dtype=torch.bfloat16) * 0.05)(D, N)
        B_im = (lambda *s: torch.randn(*s, dtype=torch.bfloat16) * 0.05)(D, N)
        C_re = (lambda *s: torch.randn(*s, dtype=torch.bfloat16) * 0.05)(D, N)
        C_im = (lambda *s: torch.randn(*s, dtype=torch.bfloat16) * 0.05)(D, N)
        log_dt = (lambda *s: torch.randn(*s, dtype=torch.bfloat16) * 0.05)(D) * 0.5 - 4.0
        Dskip = (lambda *s: torch.randn(*s, dtype=torch.bfloat16) * 0.05)(D)
        return {
            "u": _w((lambda *s: torch.randn(*s, dtype=torch.bfloat16) * 0.05)(B, L, D)),
            "A_re": _w(A_re),
            "A_im": _w(A_im),
            "B_re": _w(B_re),
            "B_im": _w(B_im),
            "C_re": _w(C_re),
            "C_im": _w(C_im),
            "log_dt": _w(log_dt),
            "Dskip": _w(Dskip),
        }
    _A_in = _A_setup()

    def _B_setup():
        B, S, D, F = 1, 2048, 448, 1792
        return {
            "x": _w((lambda *s: torch.randn(*s, dtype=torch.bfloat16) * 0.05)(B, S, D)),
            "w_gate": _w((lambda *s: torch.randn(*s, dtype=torch.bfloat16) * 0.4)(D, F) * (D ** -0.5)),
            "w_up": _w((lambda *s: torch.randn(*s, dtype=torch.bfloat16) * 0.4)(D, F) * (D ** -0.5)),
            "w_down": _w((lambda *s: torch.randn(*s, dtype=torch.bfloat16) * 0.4)(F, D) * (F ** -0.5)),
        }
    _B_in = _B_setup()

    return {
        "A_u": _A_in["u"],
        "A_A_re": _A_in["A_re"],
        "A_A_im": _A_in["A_im"],
        "A_B_re": _A_in["B_re"],
        "A_B_im": _A_in["B_im"],
        "A_C_re": _A_in["C_re"],
        "A_C_im": _A_in["C_im"],
        "A_log_dt": _A_in["log_dt"],
        "A_Dskip": _A_in["Dskip"],
        "B_x": _B_in["x"],
        "B_w_gate": _B_in["w_gate"],
        "B_w_up": _B_in["w_up"],
        "B_w_down": _B_in["w_down"],
    }

def reference(A_u, A_A_re, A_A_im, A_B_re, A_B_im, A_C_re, A_C_im, A_log_dt, A_Dskip, B_x, B_w_gate, B_w_up, B_w_down):
    def _A_ref(u, A_re, A_im, B_re, B_im, C_re, C_im, log_dt, Dskip):
        Bsz, L, D = u.shape
        dt = torch.exp(log_dt)[:, None]
        A = A_re + 1j * A_im
        Bc = B_re + 1j * B_im
        Cc = C_re + 1j * C_im
        logdA = dt * A
        dB = dt * Bc
        l = (lambda x, dt: x.to(dt))(torch.arange(L), A_re.dtype)
        P = torch.exp(logdA[:, :, None] * l[None, None, :])
        K = torch.einsum("dn,dnl,dn->dl", Cc, P, dB).real
        L2 = 2 * L
        Kp = _pad(K, ((0, 0), (0, L)))
        up = (lambda x, *d: x.permute(*d))(u, 0, 2, 1)
        up = _pad(up, ((0, 0), (0, 0), (0, L)))
        Kf = (lambda x, axes=(-2,-1): torch.fft.rfft2(x.float(), dim=axes))(Kp[:, None, :], axes=(-2, -1))
        Uf = (lambda x, axes=(-2,-1): torch.fft.rfft2(x.float(), dim=axes))(up[:, :, None, :], axes=(-2, -1))
        Yf = Uf * Kf[None, :, :, :]
        y = (lambda x, s, axes=(-2,-1): torch.fft.irfft2(x, s=s, dim=axes).to(torch.bfloat16))(Yf, (1, L2), axes=(-2, -1))[:, :, 0, :L]
        y = (lambda x, *d: x.permute(*d))(y, 0, 2, 1) + u * Dskip[None, None, :]
        return y

    def _B_ref(x, w_gate, w_up, w_down):
        g = torch.einsum("bsd,df->bsf", x, w_gate)
        u = torch.einsum("bsd,df->bsf", x, w_up)
        h = g * u
        return torch.einsum("bsf,fd->bsd", h, w_down)

    x = A_u
    h = x + _A_ref(_layernorm(x, axis=-1), A_A_re, A_A_im, A_B_re, A_B_im, A_C_re, A_C_im, A_log_dt, A_Dskip)
    h2 = h + _B_ref(_layernorm(h, axis=-1), B_w_gate, B_w_up, B_w_down)
    return h2
```

```cpp
#include <hip/hip_runtime.h>
#include <hip/hip_bf16.h>
#include <stdint.h>

typedef unsigned short u16;
#define L_ 2048
#define D_ 448
#define N_ 256
#define F_ 1792

__device__ __forceinline__ float bf2f(u16 v){ return __uint_as_float(((unsigned)v)<<16); }
__device__ __forceinline__ u16 f2bf(float f){ unsigned u=__float_as_uint(f); u += 0x7FFF + ((u>>16)&1); return (u16)(u>>16); }
__device__ __forceinline__ float rbf(float f){ return bf2f(f2bf(f)); }

__global__ void ln_kernel(const u16* x, u16* out){
  int l = blockIdx.x; int t = threadIdx.x;
  float v[7]; float s=0;
  for(int i=0;i<7;i++){ v[i]=bf2f(x[l*D_+t+64*i]); s+=v[i]; }
  for(int o=32;o>0;o>>=1) s+=__shfl_xor(s,o);
  float mean = s/D_;
  float q=0; for(int i=0;i<7;i++){ float d=v[i]-mean; q+=d*d; }
  for(int o=32;o>0;o>>=1) q+=__shfl_xor(q,o);
  float var=q/D_;
  float m=rbf(mean); float vb=rbf(var); float ve=rbf(vb+1e-5f); float sd=rbf(sqrtf(ve));
  for(int i=0;i<7;i++){ float xm=rbf(v[i]-m); out[l*D_+t+64*i]=f2bf(xm/sd); }
}

__global__ void kgen_kernel(const u16* Are,const u16* Aim,const u16* Bre,const u16* Bim,const u16* Cre,const u16* Cim,const u16* logdt,float* K){
  int d=blockIdx.x;
  __shared__ float zr[N_],zi[N_],cr[N_],ci[N_];
  float dt=rbf(expf(bf2f(logdt[d])));
  int t=threadIdx.x;
  if(t<N_){
    zr[t]=dt*bf2f(Are[d*N_+t]); zi[t]=dt*bf2f(Aim[d*N_+t]);
    float br=dt*bf2f(Bre[d*N_+t]), bi=dt*bf2f(Bim[d*N_+t]);
    float c_r=bf2f(Cre[d*N_+t]), c_i=bf2f(Cim[d*N_+t]);
    cr[t]=c_r*br-c_i*bi; ci[t]=c_r*bi+c_i*br;
  }
  __syncthreads();
  for(int l=t;l<L_;l+=blockDim.x){
    float lq=rbf((float)l);
    float s=0;
    for(int n=0;n<N_;n++){
      float e=expf(zr[n]*lq); float sn,cs; sincosf(zi[n]*lq,&sn,&cs);
      s += e*(cr[n]*cs - ci[n]*sn);
    }
    K[d*L_+l]=s;
  }
}

__global__ void conv_kernel(const u16* x,const u16* u,const float* K,const u16* Dskip,u16* h){
  int d=blockIdx.x;
  __shared__ float ks[L_], us[L_];
  for(int l=threadIdx.x;l<L_;l+=blockDim.x){ ks[l]=K[d*L_+l]; us[l]=bf2f(u[l*D_+d]); }
  __syncthreads();
  float ds=bf2f(Dskip[d]);
  for(int l=threadIdx.x;l<L_;l+=blockDim.x){
    float s=0; for(int j=0;j<=l;j++) s+=ks[j]*us[l-j];
    float y=rbf(s); float sk=rbf(us[l]*ds); float yy=rbf(y+sk);
    h[l*D_+d]=f2bf(bf2f(x[l*D_+d])+yy);
  }
}

__global__ void gemm1_kernel(const u16* a,const u16* wg,const u16* wu,u16* hh){
  int l=blockIdx.x; __shared__ float as[D_];
  for(int i=threadIdx.x;i<D_;i+=blockDim.x) as[i]=bf2f(a[l*D_+i]);
  __syncthreads();
  for(int f=threadIdx.x;f<F_;f+=blockDim.x){
    float g=0,u=0; for(int k=0;k<D_;k++){ g+=as[k]*bf2f(wg[k*F_+f]); u+=as[k]*bf2f(wu[k*F_+f]); }
    hh[l*F_+f]=f2bf(rbf(g)*rbf(u));
  }
}
__global__ void gemm2_kernel(const u16* hh,const u16* wd,const u16* h,u16* out){
  int l=blockIdx.x; __shared__ float as[F_];
  for(int i=threadIdx.x;i<F_;i+=blockDim.x) as[i]=bf2f(hh[l*F_+i]);
  __syncthreads();
  for(int d=threadIdx.x;d<D_;d+=blockDim.x){
    float o=0; for(int k=0;k<F_;k++) o+=as[k]*bf2f(wd[k*D_+d]);
    out[l*D_+d]=f2bf(bf2f(h[l*D_+d])+rbf(o));
  }
}

extern "C" void kernel_launch(void* const* d_in, const int* in_sizes, int n_in,
                              void* d_out, int out_size, void* d_ws, size_t ws_size,
                              hipStream_t stream) {
  const u16* x=(const u16*)d_in[0];
  const u16* Are=(const u16*)d_in[1]; const u16* Aim=(const u16*)d_in[2];
  const u16* Bre=(const u16*)d_in[3]; const u16* Bim=(const u16*)d_in[4];
  const u16* Cre=(const u16*)d_in[5]; const u16* Cim=(const u16*)d_in[6];
  const u16* logdt=(const u16*)d_in[7]; const u16* Dskip=(const u16*)d_in[8];
  const u16* wg=(const u16*)d_in[10]; const u16* wu=(const u16*)d_in[11]; const u16* wd=(const u16*)d_in[12];
  char* ws=(char*)d_ws;
  u16* u=(u16*)ws; u16* h=(u16*)(ws+(2<<20)); u16* a2=(u16*)(ws+(4<<20)); float* K=(float*)(ws+(6<<20)); u16* hh=(u16*)(ws+(16<<20));
  ln_kernel<<<L_,64,0,stream>>>(x,u);
  kgen_kernel<<<D_,256,0,stream>>>(Are,Aim,Bre,Bim,Cre,Cim,logdt,K);
  conv_kernel<<<D_,256,0,stream>>>(x,u,K,Dskip,h);
  ln_kernel<<<L_,64,0,stream>>>(h,a2);
  gemm1_kernel<<<L_,256,0,stream>>>(a2,wg,wu,hh);
  gemm2_kernel<<<L_,256,0,stream>>>(hh,wd,h,(u16*)d_out);
}
```

```cpp
#include <hip/hip_runtime.h>
#include <stdint.h>

typedef unsigned short u16;
typedef unsigned u32;
typedef unsigned long long u64;
typedef u32 v4u __attribute__((ext_vector_type(4)));
typedef u32 v2u __attribute__((ext_vector_type(2)));
typedef float f32x4 __attribute__((ext_vector_type(4)));
typedef float f32x16 __attribute__((ext_vector_type(16)));
typedef float f2 __attribute__((ext_vector_type(2)));
typedef __bf16 bf16x8 __attribute__((ext_vector_type(8)));
typedef __bf16 bf16x2 __attribute__((ext_vector_type(2)));
typedef short s16x4 __attribute__((ext_vector_type(4)));
#define LAS __attribute__((address_space(3)))
#define GAS __attribute__((address_space(1)))
#define DI __device__ __forceinline__
#define RLX_AGENT __ATOMIC_RELAXED, __HIP_MEMORY_SCOPE_AGENT
#define SC1 16
#define BTSC1 true
#define WAUX 16

constexpr int L_ = 2048, D_ = 448, N_ = 256, F_ = 1792;
constexpr u32 WS_EP = 0;
constexpr u32 WS_SG = 4096;
constexpr u32 WS_WF = 32768;
constexpr u32 WS_CS = 40960;
constexpr u32 WS_WR = 1966080;
constexpr u32 WS_HT = 65536;
constexpr u32 WS_HH = 2u << 20;
constexpr u32 WS_WG = 10u << 20;
constexpr u32 WS_WD = 14u << 20;
constexpr u32 WS_XG = 16u << 20;
constexpr u32 WS_END = 24u << 20;
constexpr u32 WS_DBG = 24u << 20;
constexpr u32 POISON = 0xFFFFFFFFu;
constexpr u32 SPIN_CAP = 1u << 16;

constexpr u32 LDS_BYTES = 163840;
constexpr u32 KF = 0, KF_W = 53 * 272;
constexpr u32 FT = 8 * KF_W, FT_CH = 772 * 4;
constexpr u32 RT = 121600;
constexpr u32 KC = 0, KC_COPY = 4160, KC_CH = 4 * KC_COPY;
constexpr u32 UU = 2 * KC_CH, UU_CH = (256 + 2048) * 2;
constexpr u32 XS = UU + 2 * UU_CH;
constexpr u32 CX = XS + 8192;
static_assert(FT + 2 * FT_CH <= RT && RT + 16384 <= LDS_BYTES && CX + 8 * 3072 <= FT, "LDS map");
constexpr u32 BT = 0, BT_BYTES = 448 * 256;
constexpr u32 RING = 0, RING_SLOTS = 19;
constexpr u32 SM = 155648;
constexpr u32 SP = SM;
constexpr u32 SQ = SP + 1024;
constexpr u32 CSL = SQ + 1024;
static_assert(RING_SLOTS * 8192 <= SM && CSL + 1024 <= 163328, "LDS map");

struct EoTab { u16 v[256 * 16]; };
constexpr u32 foff_c(int j) {
    if (j < 0) return 768u;
    u32 t = (u32)j;
    int s = t >= 1024 ? 3 : t >= 512 ? 2 : t >= 256 ? 1 : 0;
    if (s > 0) t = ((t + (1u << (s - 1)) - 1u + ((t >> s) & 1u)) >> s) << s;
    u32 col = (t >> 3) & 31u, row = 0;
    if (t < 256u) row = t & 7u;
    else if (t < 512u) row = 8u + ((t >> 1) & 3u);
    else if (t < 1024u) row = 12u + 2u * ((t >> 8) & 1u) + ((t >> 2) & 1u);
    else if (t < 2048u) row = 16u + ((t >> 8) & 3u);
    else row = 20u;
    return row * 32u + col;
}
constexpr EoTab make_eo() { EoTab e{}; for (int t = 0; t < 256; ++t) for (int k = 0; k < 16; ++k) e.v[t * 16 + k] = (u16)(k < 11 ? foff_c(2047 - 8 * t - k) * 4u : 0u); return e; }
__device__ const EoTab EO = make_eo();

DI float bflo(u32 d) { return __uint_as_float(d << 16); }
DI float bfhi(u32 d) { return __uint_as_float(d & 0xffff0000u); }
DI u32 pk2(float lo, float hi) { bf16x2 v = {(__bf16)lo, (__bf16)hi}; return __builtin_bit_cast(u32, v); }
DI float rbf(float f) { return bflo(pk2(f, f)); }
DI bf16x8 as_bf(v4u v) { return __builtin_bit_cast(bf16x8, v); }
DI f32x4 mfma16(v4u a, v4u b, f32x4 c) { return __builtin_amdgcn_mfma_f32_16x16x32_bf16(as_bf(a), as_bf(b), c, 0, 0, 0); }
DI f32x16 mfma32(v4u a, v4u b, f32x16 c) { return __builtin_amdgcn_mfma_f32_32x32x16_bf16(as_bf(a), as_bf(b), c, 0, 0, 0); }
template <class T> DI LAS T* lds(u32 off) { return (LAS T*)(uintptr_t)off; }
DI v2u tr_read(u32 off) { s16x4 r = __builtin_amdgcn_ds_read_tr16_b64_v4i16(lds<s16x4>(off)); return __builtin_bit_cast(v2u, r); }
DI v4u tr_frag(u32 off0, u32 off1) { v2u a = tr_read(off0), b = tr_read(off1); return v4u{a.x, a.y, b.x, b.y}; }
template <int CTRL, int RM> DI float dpp_add(float v) { int t = __builtin_amdgcn_update_dpp(0, __float_as_int(v), CTRL, RM, 0xf, true); return v + __int_as_float(t); }
DI float wave_sum(float v) {
    v = dpp_add<0x111, 0xf>(v); v = dpp_add<0x112, 0xf>(v); v = dpp_add<0x114, 0xf>(v); v = dpp_add<0x118, 0xf>(v);
    v = dpp_add<0x142, 0xa>(v); v = dpp_add<0x143, 0xc>(v);
    return __int_as_float(__builtin_amdgcn_readlane(__float_as_int(v), 63));
}
DI float half_sum(float v) { const u32 u = __float_as_uint(v); const auto r = __builtin_amdgcn_permlane32_swap(u, u, false, false); return __uint_as_float(r[0]) + __uint_as_float(r[1]); }
DI void fence() { asm volatile("" ::: "memory"); }
DI void bar() { asm volatile("s_waitcnt lgkmcnt(0)" ::: "memory"); __builtin_amdgcn_s_barrier(); asm volatile("" ::: "memory"); }
template <int N> DI void vmw() { asm volatile("s_waitcnt vmcnt(%0)" :: "n"(N) : "memory"); }
template <bool S1> DI void glds16(u32 voff, const void* sbase, u32 ldst) {
    u32 keep;
    if (S1) asm volatile("s_mov_b32 %0, m0\n\ts_mov_b32 m0, %3\n\ts_nop 0\n\tglobal_load_lds_dwordx4 %1, %2 sc1\n\ts_mov_b32 m0, %0" : "=&s"(keep) : "v"(voff), "s"(sbase), "s"(ldst) : "memory");
    else    asm volatile("s_mov_b32 %0, m0\n\ts_mov_b32 m0, %3\n\ts_nop 0\n\tglobal_load_lds_dwordx4 %1, %2\n\ts_mov_b32 m0, %0" : "=&s"(keep) : "v"(voff), "s"(sbase), "s"(ldst) : "memory");
}
DI float dot2(u32 a, u32 b, float c) { return __builtin_amdgcn_fdot2_f32_bf16(__builtin_bit_cast(bf16x2, a), __builtin_bit_cast(bf16x2, b), c, false); }
DI f2 cmul(f2 a, f2 b) { f2 t = f2{a.x, a.x} * b; return __builtin_elementwise_fma(f2{a.y, a.y}, f2{-b.y, b.x}, t); }
DI f2 cexp_rev(float ar, float ai, float t) {
    const float e = __builtin_amdgcn_exp2f(ar * t), ph = __builtin_amdgcn_fractf(ai * t);
    return f2{e * __builtin_amdgcn_cosf(ph), e * __builtin_amdgcn_sinf(ph)};
}
DI u32 swz_b(u32 row) { return ((row & 3u) << 2) | ((row >> 2) & 3u); }
DI u32 off_b(u32 row, u32 ch) { return 256u * row + 16u * (ch ^ swz_b(row)); }
DI u32 off_a(u32 row, u32 ch) { return 2048u * (row >> 3) + 512u * (ch >> 2) + 64u * (row & 7u) + 16u * ((ch & 3u) ^ ((row >> 2) & 3u)); }
DI u32 max4(v4u v) { return max(max(v.x, v.y), max(v.z, v.w)); }
DI u32 san(u32 v) { return min(v, 0xfffffffeu); }
DI u32 ln_pack(float mean, float var) {
    const float m = rbf(mean), sd = rbf(__builtin_amdgcn_sqrtf(rbf(rbf(var) + 1e-5f)));
    return (__float_as_uint(m) >> 16) | (__float_as_uint(sd) & 0xffff0000u);
}

#define STAMP(i) do { } while (0)
#define STAMP_DECL
#define STAMP_FLUSH do { } while (0)
#define SGB(mask, n) __builtin_amdgcn_sched_group_barrier(mask, n, 0)
#define SB0() __builtin_amdgcn_sched_barrier(0)
constexpr int M_MFMA = 0x8, M_VMEM = 0x10, M_DSR = 0x100, M_VALU = 0x2;
DI u32 opaque(u32 v) { asm volatile("" : "+v"(v)); return v; }

template <int TG> struct Tiles;
template <> struct Tiles<0> { static constexpr int n = 3; static constexpr int t[5] = {7, 6, 2, 0, 0}; };
template <> struct Tiles<1> { static constexpr int n = 5; static constexpr int t[5] = {5, 4, 3, 1, 0}; };

template <int TG> DI void conv_wave(int tid, int w, int lane, int cm, float dsv, __amdgpu_buffer_rsrc_t rsW, v4u (&Wf)[28], u32 wf_off, u32 hh_off) {
    typedef Tiles<TG> T;
    constexpr int NP = T::t[0] + 1;
    const int ch = w >> 2, rh = (w >> 1) & 1;
    f32x4 acc[T::n];
#pragma unroll
    for (int i = 0; i < T::n; ++i) acc[i] = f32x4{0.f, 0.f, 0.f, 0.f};
    const u32 cb = lane & 15, cq = lane >> 4, cs = (3u - cb) & 3u;
    const u32 kbase = opaque(KC + ch * KC_CH + cs * KC_COPY + 2 * (2047 - 16 * (cq >> 1) - cb + 8 * (cq & 1) - cs) - 32 * 8 * rh - 32 * 118);
    const u32 kbase8 = opaque(kbase + 8);
    const u32 ubase = opaque(UU + ch * UU_CH + 512 + 32 * (cb - (cq >> 1)) + 16 * (cq & 1) - 32 * 8 * rh - 32 * 6);
    const v4u pz = {POISON, POISON, POISON, POISON};
#pragma unroll
    for (int rr = 0; rr < 4; ++rr) {
        v4u Af[NP], Bf[NP];
#pragma unroll
        for (int p = 0; p < NP; ++p) {
            const v2u lo = *lds<volatile v2u>(kbase + 32 * (118 - (16 * p + 2 * rr))), hi = *lds<volatile v2u>(kbase8 + 32 * (118 - (16 * p + 2 * rr)));
            Af[p] = v4u{lo.x, lo.y, hi.x, hi.y};
            Bf[p] = *lds<v4u>(ubase + 32 * (16 * p - 2 * rr + 6));
        }
        if (rr == 0) {
#pragma unroll
            for (int i = 0; i < 4; ++i) __builtin_amdgcn_raw_buffer_store_b128(pz, rsW, hh_off + i * 8192, 0, SC1);
            SB0();
        }
#pragma unroll
        for (int i = 0; i < 7; ++i) Wf[7 * rr + i] = __builtin_amdgcn_raw_buffer_load_b128(rsW, wf_off + (7 * rr + i) * 1024, 0, WAUX);
#pragma unroll
        for (int p = 0; p < NP; ++p)
#pragma unroll
            for (int i = 0; i < T::n; ++i)
                if (p <= T::t[i]) acc[i] = mfma16(Af[p], Bf[T::t[i] - p], acc[i]);
        if (rr != 0) SGB(M_DSR, 3 * NP);
#pragma unroll
        for (int i = 0; i < 6; ++i) { SGB(M_MFMA, 2); SGB(M_VMEM, 1); }
        SGB(M_MFMA, 6); SGB(M_VMEM, 1);
        SB0();
    }
    STAMP(30);
    const u32 mine = CX + w * 3072, other = CX + (w ^ 2) * 3072;
    if (rh == 0) {
#pragma unroll
        for (int i = 1; i < T::n; i += 2) *lds<f32x4>(mine + (i >> 1) * 1024 + lane * 16) = acc[i];
    } else {
#pragma unroll
        for (int i = 0; i < T::n; i += 2) *lds<f32x4>(mine + (i >> 1) * 1024 + lane * 16) = acc[i];
    }
    vmw<28>();
    STAMP(31);
    bar();
    STAMP(16);
    auto fin = [&](int i, f32x4 own) {
        const f32x4 y = own + *lds<f32x4>(other + (i >> 1) * 1024 + lane * 16);
        const u32 l0 = 256 * T::t[i] + 16 * cb + 4 * cq;
        const v2u uu = *lds<v2u>(UU + ch * UU_CH + 512 + l0 * 2), xx = *lds<v2u>(XS + ch * 4096 + l0 * 2);
        const float uv[4] = {bflo(uu.x), bfhi(uu.x), bflo(uu.y), bfhi(uu.y)}, xv[4] = {bflo(xx.x), bfhi(xx.x), bflo(xx.y), bfhi(xx.y)};
        float h[4];
#pragma unroll
        for (int r = 0; r < 4; ++r) h[r] = xv[r] + rbf(rbf(y[r]) + rbf(uv[r] * dsv));
        const v2u o2 = {san(pk2(h[0], h[1])), san(pk2(h[2], h[3]))};
        __builtin_amdgcn_raw_buffer_store_b64(o2, rsW, WS_HT + (2 * cm + ch) * 4096 + l0 * 2, 0, SC1);
    };
    if (rh == 0) {
#pragma unroll
        for (int i = 0; i < T::n; i += 2) fin(i, acc[i]);
    } else {
#pragma unroll
        for (int i = 1; i < T::n; i += 2) fin(i, acc[i]);
    }
}

__global__ void __launch_bounds__(512, 2)
fused_kernel(const u16* __restrict__ x, const u16* __restrict__ Are, const u16* __restrict__ Aim, const u16* __restrict__ Bre, const u16* __restrict__ Bim,
             const u16* __restrict__ Cre, const u16* __restrict__ Cim, const u16* __restrict__ logdt, const u16* __restrict__ Dskip,
             const u16* __restrict__ wg, const u16* __restrict__ wu, const u16* __restrict__ wd, u16* __restrict__ out, char* __restrict__ ws)
{
    const int tid = threadIdx.x, lane = tid & 63, w = __builtin_amdgcn_readfirstlane(tid >> 6);
    const int b = blockIdx.x, g = b & 7, j = b >> 3;
    const bool mainb = j < 28;
    const int cm = mainb ? g * 28 + j : 0;
    const int P = 4 * (g >> 1) + j / 7, S = 7 * (g & 1) + j % 7;
    const __amdgpu_buffer_rsrc_t rsW = __builtin_amdgcn_make_buffer_rsrc(ws, 0, WS_END, 0x00020000);
    const __amdgpu_buffer_rsrc_t rsX = __builtin_amdgcn_make_buffer_rsrc((void*)x, 0, L_ * D_ * 2, 0x00020000);
    const u32 trq = (lane & 15) >> 2, trp = lane & 3, trh = lane >> 5, trb = (lane >> 4) & 1;
    STAMP_DECL
    STAMP(0);
    const v4u pz = {POISON, POISON, POISON, POISON};
    __builtin_amdgcn_raw_buffer_store_b128(pz, rsW, mainb ? (u32)(WS_HT + cm * 8192 + tid * 16) : 0x7ffffff0u, 0, SC1);
    const v4u xr = __builtin_amdgcn_raw_buffer_load_b128(rsX, lane < 56 ? (u32)((8 * b + w) * 896 + lane * 16) : 0x7ffffff0u, 0, 0);
    const u64 ep = __hip_atomic_load((GAS u64*)(ws + WS_EP), RLX_AGENT) + 1ull;
    const u32 tag = (u32)ep, tagx = (u32)(ep >> 32) << 16;
    const int ch = w >> 2, dch = 2 * cm + ch, po = dch * N_ + (tid & 255);
    const u32 p_are = Are[po], p_aim = Aim[po], p_bre = Bre[po], p_bim = Bim[po], p_cre = Cre[po], p_cim = Cim[po];
    const u32 p_ldt = logdt[dch], p_dsk = Dskip[dch];
    const v4u eo0 = *(const v4u*)&EO.v[(tid & 255) * 16];
    const v2u eo1 = *(const v2u*)&EO.v[(tid & 255) * 16 + 8];
    fence();
    if (mainb) {
        if (b < 196) {
#pragma unroll
            for (int i = 0; i < 2; ++i) {
                const int id = 2 * b + i, Sp = id / 28, s = id % 28;
                const u32 row = (w & 3) * 4 + (lane >> 4), chk = (lane & 15) ^ swz_b(row);
                glds16<false>((u32)((16 * s + row) * (F_ * 2) + Sp * 256 + chk * 16), (w >> 2) ? (const void*)wu : (const void*)wg, RT + i * 8192 + w * 1024);
            }
        }
    } else {
        const int hb = (j - 28) * 8 + g;
#pragma unroll
        for (int k = 0; k < 7; ++k) {
            const int gid = hb + 32 * k;
            if (gid < 196) {
                const int dt_ = gid / 28, fg = gid % 28;
                const u32 row = 8 * w + (lane >> 3), chk = (lane & 7) ^ (((row >> 1) & 1) << 2);
                glds16<false>((u32)((64 * fg + row) * (D_ * 2) + dt_ * 128 + chk * 16), (const void*)wd, k * 8192 + w * 1024);
            }
        }
    }
    constexpr u32 XT = 140288;
    if (lane < 56) *lds<v4u>(XT + w * 912 + lane * 16) = xr;
    bar();
#pragma unroll
    for (int k = 0; k < 4; ++k) {
        const int id = tid + 512 * k, c = id >> 2, i2 = (id & 3) * 2;
        if (id < 1792) {
            const u32 x0 = *lds<u16>(XT + i2 * 912 + c * 2), x1 = *lds<u16>(XT + (i2 + 1) * 912 + c * 2);
            __builtin_amdgcn_raw_buffer_store_b128(v4u{x0 | tagx, tag, x1 | tagx, tag}, rsW, WS_XG + b * 28672 + id * 16, 0, SC1);
        }
    }
    STAMP(25);
    u32 ln1;
    {
        const float v[8] = {bflo(xr.x), bfhi(xr.x), bflo(xr.y), bfhi(xr.y), bflo(xr.z), bfhi(xr.z), bflo(xr.w), bfhi(xr.w)};
        const float mean = wave_sum(((v[0] + v[1]) + (v[2] + v[3])) + ((v[4] + v[5]) + (v[6] + v[7]))) * (1.f / 448.f);
        float q = 0.f;
#pragma unroll
        for (int e = 0; e < 8; ++e) { const float dl = v[e] - mean; q += dl * dl; }
        q = lane < 56 ? q : 0.f;
        ln1 = ln_pack(mean, wave_sum(q) * (1.f / 448.f));
    }
    STAMP(1);
    if (!mainb) {
        if (lane == 0) __hip_atomic_store((GAS u64*)(ws + WS_SG) + (8 * b + w), ((u64)tag << 32) | ln1, RLX_AGENT);
        const int hb = (j - 28) * 8 + g;
        float cs[8];
        {
            const u16* wsrc = hb < 16 ? wg : wu;
            const int c0 = 112 * (hb & 15) + 8 * (tid % 14), r0 = tid / 14;
#pragma unroll
            for (int e = 0; e < 8; ++e) cs[e] = 0.f;
            v4u t[13];
#pragma unroll
            for (int i = 0; i < 13; ++i) { const int r = r0 + 36 * i; t[i] = (tid < 504 && r < 448) ? *(const v4u*)(wsrc + (size_t)r * F_ + c0) : v4u{0, 0, 0, 0}; }
#pragma unroll
            for (int i = 0; i < 13; ++i) { cs[0] += bflo(t[i].x); cs[1] += bfhi(t[i].x); cs[2] += bflo(t[i].y); cs[3] += bfhi(t[i].y); cs[4] += bflo(t[i].z); cs[5] += bfhi(t[i].z); cs[6] += bflo(t[i].w); cs[7] += bfhi(t[i].w); }
        }
        vmw<0>(); bar();
#pragma unroll
        for (int k = 0; k < 7; ++k) {
            const int gid = hb + 32 * k;
            if (gid < 196) {
                const int dt_ = gid / 28, fg = gid % 28, fsl = w >> 1, td = w & 1;
                u32 ad[2];
#pragma unroll
                for (int t = 0; t < 2; ++t) { const u32 row = 16 * fsl + 8 * t + 4 * trh + trq, chk = 4 * td + 2 * trb + (trp >> 1); ad[t] = k * 8192 + row * 128 + 16 * (chk ^ (((row >> 1) & 1) << 2)) + 8 * (trp & 1); }
                const v4u f = tr_frag(ad[0], ad[1]);
                __builtin_amdgcn_raw_buffer_store_b128(f, rsW, WS_WD + ((dt_ * 2 + td) * 112 + 4 * fg + fsl) * 1024 + lane * 16, 0, SC1);
            }
        }
        if (tid < 504) {
            *lds<f32x4>(65536 + tid * 32) = f32x4{cs[0], cs[1], cs[2], cs[3]};
            *lds<f32x4>(65536 + tid * 32 + 16) = f32x4{cs[4], cs[5], cs[6], cs[7]};
        }
        bar();
        if (tid < 112) {
            float s = 0.f;
            for (int r = 0; r < 36; ++r) s += *lds<float>(65536 + (r * 14 + (tid >> 3)) * 32 + (tid & 7) * 4);
            __hip_atomic_store((GAS u32*)(ws + WS_CS) + ((hb < 16 ? 0 : 1792) + 112 * (hb & 15) + tid), __float_as_uint(s), RLX_AGENT);
        }
        vmw<0>(); bar();
        if (tid == 0) __hip_atomic_store((GAS u32*)(ws + WS_WF + hb * 64), tag, RLX_AGENT);
        return;
    }
    if (lane == 0) __hip_atomic_store((GAS u64*)(ws + WS_SG) + (8 * b + w), ((u64)tag << 32) | ln1, RLX_AGENT);
    if (w < 4) vmw<5>(); else vmw<4>();
    bar();
    if (b < 196) {
#pragma unroll
        for (int i = 0; i < 2; ++i) {
            const int id = 2 * b + i, Sp = id / 28, s = id % 28;
            const u32 base = RT + i * 8192 + trb * 4096 + 8 * (trp & 1);
            const v4u f = tr_frag(base + off_b(8 * trh + trq, 2 * w + (trp >> 1)), base + off_b(8 * trh + 4 + trq, 2 * w + (trp >> 1)));
            __builtin_amdgcn_raw_buffer_store_b128(f, rsW, WS_WG + ((Sp * 8 + w) * 28 + s) * 1024 + lane * 16, 0, SC1);
        }
    }
    fence();
    STAMP(2);
    {
        const u32 kfw = KF + w * KF_W;
        const float dt = rbf(expf(bflo(p_ldt)));
        const float zr = dt * bflo(p_are), zi = dt * bflo(p_aim);
        const float ar = zr * 1.4426950408889634f, ai = zi * 0.15915494309189535f;
        const f2 CB = cmul(f2{bflo(p_cre), bflo(p_cim)}, f2{dt * bflo(p_bre), dt * bflo(p_bim)});
        const f2 E1 = cexp_rev(ar, ai, 1.f), G1 = cexp_rev(ar, ai, 8.f), E256 = cexp_rev(ar, ai, 256.f);
        const f2 E2 = cmul(E1, E1), E4 = cmul(E2, E2), G2 = cmul(G1, G1), G4 = cmul(G2, G2);
        LAS u32* row = lds<u32>(kfw + lane * 4);
#define WA(r, X) row[(r) * 68] = pk2((X).x, -(X).y)
#define WB(r, X) row[(21 + (r)) * 68] = pk2((X).x, (X).y)
        {
            f2 Ga = f2{1.f, 0.f}, Gb = G1, Gc = G2, Gd = cmul(G2, G1);
#pragma unroll
            for (int t = 0; t < 32; t += 4) {
                WB(t, Ga); WB(t + 1, Gb); WB(t + 2, Gc); WB(t + 3, Gd);
                if (t < 28) { Ga = cmul(Ga, G4); Gb = cmul(Gb, G4); Gc = cmul(Gc, G4); Gd = cmul(Gd, G4); }
            }
        }
        {
            f2 X = CB, Y = cmul(CB, E256);
            WA(0, X); WA(8, Y);
            f2 Z = cmul(Y, E2); X = cmul(X, E1); Y = cmul(Y, E256); WA(9, Z); WA(1, X); WA(12, Y);
            { f2 V = cmul(Y, E4); Z = cmul(Z, E2); X = cmul(X, E1); Y = cmul(Y, E256); WA(13, V); WA(10, Z); WA(2, X); WA(14, Y); }
            { f2 V = cmul(Y, E4); Z = cmul(Z, E2); X = cmul(X, E1); Y = cmul(Y, E256); WA(15, V); WA(11, Z); WA(3, X); WA(16, Y); }
#pragma unroll
            for (int t = 0; t < 4; ++t) { X = cmul(X, E1); Y = cmul(Y, E256); WA(4 + t, X); WA(17 + t, Y); }
        }
#undef WA
#undef WB
        f32x16 acc;
#pragma unroll
        for (int i = 0; i < 16; ++i) acc[i] = 0.f;
#pragma unroll
        for (int ks = 0; ks < 8; ++ks) {
            const v4u A = *lds<v4u>(kfw + (lane & 31) * 272 + ks * 32 + (lane >> 5) * 16);
            const v4u B = *lds<v4u>(kfw + (21 + (lane & 31)) * 272 + ks * 32 + (lane >> 5) * 16);
            acc = mfma32(A, B, acc);
        }
#pragma unroll
        for (int r = 0; r < 12; ++r) *lds<float>(kfw + r * 256 + lane * 4) = acc[r];
        STAMP(3);
        vmw<0>();
        bar();
        if (tid == 0 && b < 196) __hip_atomic_store((GAS u32*)(ws + WS_WR + b * 64), tag, RLX_AGENT);
#pragma unroll
        for (int kk = 0; kk < 3; ++kk) {
            const int r = (w & 3) + 4 * kk;
            float s = 0.f;
#pragma unroll
            for (int ww = 0; ww < 4; ++ww) s += *lds<float>(KF + (4 * ch + ww) * KF_W + r * 256 + lane * 4);
            *lds<float>(FT + ch * FT_CH + (((w & 3) + 8 * kk + 4 * (lane >> 5)) * 32 + (lane & 31)) * 4) = s;
        }
        if ((tid & 255) == 0) *lds<float>(FT + ch * FT_CH + 768 * 4) = 0.f;
        bar();
        {
            const u32 eo[11] = {eo0.x & 0xffffu, eo0.x >> 16, eo0.y & 0xffffu, eo0.y >> 16, eo0.z & 0xffffu, eo0.z >> 16, eo0.w & 0xffffu, eo0.w >> 16, eo1.x & 0xffffu, eo1.x >> 16, eo1.y & 0xffffu};
            float v[11];
#pragma unroll
            for (int e = 0; e < 11; ++e) v[e] = *lds<float>(FT + ch * FT_CH + eo[e]);
            u32 p[10];
#pragma unroll
            for (int e = 0; e < 10; ++e) p[e] = pk2(v[e], v[e + 1]);
#pragma unroll
            for (int s = 0; s < 4; ++s) *lds<v4u>(KC + ch * KC_CH + s * KC_COPY + (tid & 255) * 16) = v4u{p[s], p[s + 2], p[s + 4], p[s + 6]};
        }
        if (tid < 16) *lds<v4u>(KC + (tid >> 3) * KC_CH + ((tid >> 1) & 3) * KC_COPY + 4096 + (tid & 1) * 16) = v4u{0, 0, 0, 0};
        if (tid >= 64 && tid < 128) *lds<v4u>(UU + ((tid >> 5) & 1) * UU_CH + (tid & 31) * 16) = v4u{0, 0, 0, 0};
    }
    STAMP(4);
    {
        v4u xg[4], sg[4];
        const u32 fa = WS_WR + (tid < 196 ? tid : 0) * 64;
        const u32 xa = WS_XG + (32 * w + (lane >> 3)) * 28672 + cm * 128 + (lane & 7) * 16, sa = WS_SG + (32 * w + (lane >> 3)) * 64 + (lane & 3) * 16;
        for (u32 spin = 0;; ++spin) {
#pragma unroll
            for (int k = 0; k < 4; ++k) { xg[k] = __builtin_amdgcn_raw_buffer_load_b128(rsW, xa + k * 8 * 28672, 0, SC1); sg[k] = __builtin_amdgcn_raw_buffer_load_b128(rsW, sa + k * 512, 0, SC1); }
            const u32 fl = __builtin_amdgcn_raw_buffer_load_b32(rsW, fa, 0, SC1);
            bool ok = fl == tag;
#pragma unroll
            for (int k = 0; k < 4; ++k) ok = ok && sg[k].y == tag && sg[k].w == tag && xg[k].y == tag && xg[k].w == tag && (xg[k].x & 0xffff0000u) == tagx && (xg[k].z & 0xffff0000u) == tagx;
            if (__all(ok) || spin > SPIN_CAP) break;
            __builtin_amdgcn_s_sleep(1);
        }
        STAMP(5);
        const int c = (lane >> 2) & 1;
#pragma unroll
        for (int k = 0; k < 4; ++k) {
            const u32 l0 = 8 * (32 * w + 8 * k + (lane >> 3)) + 2 * (lane & 3);
            const float u0 = rbf(bflo(xg[k].x) - bflo(sg[k].x)) * __builtin_amdgcn_rcpf(bfhi(sg[k].x));
            const float u1 = rbf(bflo(xg[k].z) - bflo(sg[k].z)) * __builtin_amdgcn_rcpf(bfhi(sg[k].z));
            *lds<u32>(UU + c * UU_CH + 512 + l0 * 2) = pk2(u0, u1);
            *lds<u32>(XS + c * 4096 + l0 * 2) = (xg[k].x & 0xffffu) | (xg[k].z << 16);
        }
    }
    bar();
    STAMP(6);
    v4u Wf[28];
    {
        const u32 wf_off = WS_WG + (S * 8 + w) * 28 * 1024 + lane * 16, hh_off = WS_HH + (P * 112 + 8 * S) * 4096 + tid * 16;
        if (w & 1) conv_wave<1>(tid, w, lane, cm, bflo(p_dsk), rsW, Wf, wf_off, hh_off); else conv_wave<0>(tid, w, lane, cm, bflo(p_dsk), rsW, Wf, wf_off, hh_off);
    }
    STAMP(7);
    const int l0p = 128 * P;
    {
        const u32 pa = tid < 448 ? (u32)(WS_HT + tid * 4096 + l0p * 2 + 252) : tid < 480 ? (u32)(WS_WF + (tid - 448) * 64) : (u32)WS_WF;
        for (u32 spin = 0;; ++spin) {
            const u32 s0 = __builtin_amdgcn_raw_buffer_load_b32(rsW, pa, 0, SC1);
            const bool ok = tid < 448 ? s0 != POISON : s0 == tag;
            if (spin == 0) STAMP(27);
            if (__all(ok || tid >= 480) || spin > SPIN_CAP) { STAMP(28);
                break; }
            __builtin_amdgcn_s_sleep(1);
        }
    }
    bar();
    STAMP(8);
    if (b == 0 && tid == 0) __hip_atomic_store((GAS u64*)(ws + WS_EP), ep, RLX_AGENT);
    const u32 bt_src = (8 * (w >> 1) + ((lane >> 2) & 7)) * 4096 + l0p * 2 + (4 * (2 * (w & 1) + (lane >> 5)) + ((lane & 3) ^ ((2 * (w >> 1) + ((lane >> 4) & 1)) & 3))) * 16;
    const u32 bt_dst = BT + (w >> 1) * 2048 + (w & 1) * 1024;
    auto bt_piece = [&](int k) { glds16<BTSC1>(bt_src + k * 32 * 4096, ws + WS_HT, bt_dst + k * 8192); };
    auto bt_fix = [&](int k0, int k1) {
        for (u32 spin = 0; spin < SPIN_CAP; ++spin) {
            u32 mx = 0;
            for (int k = k0; k < k1; ++k) mx = max(mx, max4(*lds<v4u>(bt_dst + k * 8192 + lane * 16)));
            if (__all(mx != POISON)) break;
            for (int k = k0; k < k1; ++k) bt_piece(k);
            vmw<0>();
        }
    };
    const float csum = __uint_as_float(__builtin_amdgcn_raw_buffer_load_b32(rsW, WS_CS + (((lane >> 4) & 1) * 1792 + 128 * S + 16 * w + (lane & 15)) * 4, 0, SC1));
    fence();
#pragma unroll
    for (int k = 0; k < 6; ++k) bt_piece(k);
    vmw<4>();
    bt_fix(0, 2);
    bar();
    STAMP(9);
    f32x16 acc[4];
    v4u Wd[28];
    const int td = w & 1, kq = w >> 1, hp = S / 7, dtile = S % 7;
    const u32 wd_off = WS_WD + ((dtile * 2 + td) * 112 + 28 * kq) * 1024 + lane * 16;
    u32 bb[2];
#pragma unroll
    for (int tt = 0; tt < 2; ++tt) bb[tt] = opaque(BT + 2048 * trh + 64 * (4 * tt + trq) + 16 * ((2 * trb + (trp >> 1)) ^ ((2 * trh + tt) & 3)) + 8 * (trp & 1));
    {
#pragma unroll
        for (int t = 0; t < 4; ++t)
#pragma unroll
            for (int i = 0; i < 16; ++i) acc[t][i] = 0.f;
        v4u Bf[2][4];
#pragma unroll
        for (int t = 0; t < 4; ++t) Bf[0][t] = tr_frag(bb[0] + 512 * t, bb[1] + 512 * t);
#pragma unroll
        for (int s = 0; s < 28; ++s) {
            SB0();
            if (s == 3) { vmw<6>(); bt_fix(2, 6); bar(); }
            if (s == 11) { vmw<7>(); bt_fix(6, 14); bar(); }
            if (s + 1 < 28) {
#pragma unroll
                for (int t = 0; t < 4; ++t) Bf[(s + 1) & 1][t] = tr_frag(bb[0] + (s + 1) * 4096 + 512 * t, bb[1] + (s + 1) * 4096 + 512 * t);
            }
            if (s < 4) { bt_piece(6 + 2 * s); bt_piece(7 + 2 * s); }
            else Wd[s - 4] = __builtin_amdgcn_raw_buffer_load_b128(rsW, wd_off + (s - 4) * 1024, 0, WAUX);
#pragma unroll
            for (int t = 0; t < 4; ++t) acc[t] = mfma32(Wf[s], Bf[s & 1][t], acc[t]);
            if (s >= 4) {
                SGB(M_MFMA, 1); SGB(M_DSR, 2); SGB(M_MFMA, 1); SGB(M_DSR, 2); SGB(M_VMEM, 1); SGB(M_MFMA, 1); SGB(M_DSR, 2); SGB(M_MFMA, 1); SGB(M_DSR, 2);
            }
        }
        SB0();
        STAMP(17);
#pragma unroll
        for (int s = 24; s < 28; ++s) Wd[s] = __builtin_amdgcn_raw_buffer_load_b128(rsW, wd_off + s * 1024, 0, WAUX);
        float sumh = 0.f, sq = 0.f;
        const u32 so = (w >> 2) * 4096 + (w & 3) * 512;
#pragma unroll
        for (int s = 0; s < 14; ++s) {
            const v4u o = tr_frag(bb[0] + so + s * 8192, bb[1] + so + s * 8192);
#pragma unroll
            for (int e = 0; e < 4; ++e) { sumh = dot2(o[e], 0x3f803f80u, sumh); sq = dot2(o[e], o[e], sq); }
        }
        sumh = half_sum(sumh); sq = half_sum(sq);
        if (lane < 32) { *lds<float>(SP + w * 128 + lane * 4) = sumh; *lds<float>(SQ + w * 128 + lane * 4) = sq; *lds<float>(CSL + w * 128 + lane * 4) = csum; }
    }
    STAMP(10);
    bar();
    {
        f32x4 csg[2], csu[2];
#pragma unroll
        for (int q = 0; q < 2; ++q) { csg[q] = *lds<f32x4>(CSL + w * 128 + (8 * q + 4 * trh) * 4); csu[q] = *lds<f32x4>(CSL + w * 128 + 64 + (8 * q + 4 * trh) * 4); }
#pragma unroll
        for (int t = 0; t < 4; ++t) {
            const float m = rbf((*lds<float>(SP + t * 128 + (lane & 31) * 4) + *lds<float>(SP + (t + 4) * 128 + (lane & 31) * 4)) * (1.f / 448.f));
            float hv[8];
#pragma unroll
            for (int e = 0; e < 8; ++e) hv[e] = (acc[t][e] - m * csg[e >> 2][e & 3]) * (acc[t][e + 8] - m * csu[e >> 2][e & 3]);
            const v4u o = {san(pk2(hv[0], hv[1])), san(pk2(hv[2], hv[3])), san(pk2(hv[4], hv[5])), san(pk2(hv[6], hv[7]))};
            __builtin_amdgcn_raw_buffer_store_b128(o, rsW, WS_HH + ((P * 112 + 8 * S + w) * 4 + t) * 1024 + lane * 16, 0, SC1);
        }
    }
    STAMP(11);
    const int tl_f = kq >> 1;
    const u32 lp = 64 * hp + 32 * tl_f + (lane & 31);
    float hres[8], rs;
    {
        const int tq = 2 * hp + tl_f;
        const float s1 = *lds<float>(SP + tq * 128 + (lane & 31) * 4) + *lds<float>(SP + (tq + 4) * 128 + (lane & 31) * 4);
        const float s2 = *lds<float>(SQ + tq * 128 + (lane & 31) * 4) + *lds<float>(SQ + (tq + 4) * 128 + (lane & 31) * 4);
        const float mean = s1 * (1.f / 448.f), var = fmaxf(s2 * (1.f / 448.f) - mean * mean, 0.f);
        const float sd = bfhi(ln_pack(mean, var));
        rs = 1.f / (sd * sd);
#pragma unroll
        for (int e = 0; e < 8; ++e) {
            const u32 row = 64 * dtile + 32 * td + 16 * (kq & 1) + 8 * (e >> 2) + 4 * trh + (e & 3);
            hres[e] = bflo(*lds<u16>(BT + off_a(row, lp >> 3) + (lp & 7) * 2));
        }
    }
    const u32 hbase = WS_HH + ((P * 112 + 28 * kq) * 4 + 2 * hp) * 1024;
    {
        const u32 pa = hbase + ((lane < 56 ? lane : 0) >> 1) * 4096 + (lane & 1) * 1024 + 1020;
        for (u32 spin = 0;; ++spin) {
            const u32 s0 = __builtin_amdgcn_raw_buffer_load_b32(rsW, pa, 0, SC1);
            if (spin == 0) STAMP(18);
            if (__all(s0 != POISON) || spin > SPIN_CAP) { STAMP(19);
                break; }
            __builtin_amdgcn_s_sleep(1);
        }
    }
    bar();
    STAMP(12);
    const u32 hsrc = td * 1024 + lane * 16, hdst = RING + kq * 2048 + td * 1024, hrd = opaque(RING + kq * 2048 + lane * 16);
    auto hh_dma = [&](int s) { glds16<true>(hsrc + s * 4096, ws + hbase, hdst + (s % RING_SLOTS) * 8192); };
    f32x16 o2[2];
    for (u32 attempt = 0;; ++attempt) {
#pragma unroll
        for (int i = 0; i < 16; ++i) { o2[0][i] = 0.f; o2[1][i] = 0.f; }
        u32 mx = 0;
        constexpr int AH = 9;
#pragma unroll
        for (int s = 0; s < AH; ++s) hh_dma(s);
        v4u hf[2][2];
        vmw<AH - 4>(); bar();
        if (attempt == 0) STAMP(13);
#pragma unroll
        for (int tl = 0; tl < 2; ++tl) hf[0][tl] = *lds<v4u>(hrd + tl * 1024);
#pragma unroll
        for (int s = 0; s < 28; ++s) {
            SB0();
            if ((s & 3) == 3 && s + 1 < 28) {
                constexpr int dummy = 0; (void)dummy;
                const int issued = (s - 1 + AH < 27) ? s - 1 + AH : 27, need = (s + 4 < 27) ? s + 4 : 27;
                switch (issued - need) { case 0: vmw<0>(); break; case 1: vmw<1>(); break; case 2: vmw<2>(); break; case 3: vmw<3>(); break; default: vmw<4>(); break; }
                bar();
            }
            if (s + 1 < 28) {
#pragma unroll
                for (int tl = 0; tl < 2; ++tl) hf[(s + 1) & 1][tl] = *lds<v4u>(hrd + ((s + 1) % RING_SLOTS) * 8192 + tl * 1024);
            }
            if (s + AH < 28) hh_dma(s + AH);
            mx = max(mx, max(max4(hf[s & 1][0]), max4(hf[s & 1][1])));
#pragma unroll
            for (int tl = 0; tl < 2; ++tl) o2[tl] = mfma32(Wd[s], hf[s & 1][tl], o2[tl]);
        }
        SB0();
        const bool bad = !__all(mx != POISON);
        if (lane == 0) *lds<u32>(SM + 3072 + w * 4 + (attempt & 1) * 32) = bad;
        bar();
        const v4u f0 = *lds<v4u>(SM + 3072 + (attempt & 1) * 32), f1 = *lds<v4u>(SM + 3072 + 16 + (attempt & 1) * 32);
        if (!(f0.x | f0.y | f0.z | f0.w | f1.x | f1.y | f1.z | f1.w) || attempt > 64) break;
    }
    STAMP(14);
    {
#pragma unroll
        for (int k2 = 0; k2 < 4; ++k2) {
            if (k2 != kq) {
                const int si = (kq > k2) ? kq - 1 : kq;
                const f32x16& a = o2[k2 >> 1];
#pragma unroll
                for (int q = 0; q < 2; ++q) { const int r0 = 8 * (k2 & 1) + 4 * q; *lds<f32x4>(((td * 4 + k2) * 3 + si) * 2048 + q * 1024 + lane * 16) = f32x4{a[r0], a[r0 + 1], a[r0 + 2], a[r0 + 3]}; }
            }
        }
        bar();
        f32x4 v[2];
#pragma unroll
        for (int q = 0; q < 2; ++q) {
            if (kq == 0) v[q] = f32x4{o2[0][4 * q], o2[0][4 * q + 1], o2[0][4 * q + 2], o2[0][4 * q + 3]};
            else if (kq == 1) v[q] = f32x4{o2[0][8 + 4 * q], o2[0][9 + 4 * q], o2[0][10 + 4 * q], o2[0][11 + 4 * q]};
            else if (kq == 2) v[q] = f32x4{o2[1][4 * q], o2[1][4 * q + 1], o2[1][4 * q + 2], o2[1][4 * q + 3]};
            else v[q] = f32x4{o2[1][8 + 4 * q], o2[1][9 + 4 * q], o2[1][10 + 4 * q], o2[1][11 + 4 * q]};
#pragma unroll
            for (int si = 0; si < 3; ++si) v[q] += *lds<f32x4>(((td * 4 + kq) * 3 + si) * 2048 + q * 1024 + lane * 16);
            const u32 d0 = 64 * dtile + 32 * td + 16 * (kq & 1) + 8 * q + 4 * trh;
            float r[4];
#pragma unroll
            for (int e = 0; e < 4; ++e) r[e] = hres[4 * q + e] + rbf(v[q][e] * rs);
            *(GAS v2u*)(out + (size_t)(l0p + lp) * D_ + d0) = v2u{pk2(r[0], r[1]), pk2(r[2], r[3])};
        }
    }
    STAMP(15);
    STAMP_FLUSH;
}

extern "C" void kernel_launch(void* const* d_in, const int* in_sizes, int n_in, void* d_out, int out_size, void* d_ws, size_t ws_size, hipStream_t stream) {
    (void)in_sizes; (void)n_in; (void)out_size; (void)ws_size;
    (void)hipFuncSetAttribute((const void*)fused_kernel, hipFuncAttributeMaxDynamicSharedMemorySize, LDS_BYTES);
    fused_kernel<<<256, 512, LDS_BYTES, stream>>>((const u16*)d_in[0], (const u16*)d_in[1], (const u16*)d_in[2], (const u16*)d_in[3], (const u16*)d_in[4],
        (const u16*)d_in[5], (const u16*)d_in[6], (const u16*)d_in[7], (const u16*)d_in[8], (const u16*)d_in[10], (const u16*)d_in[11], (const u16*)d_in[12],
        (u16*)d_out, (char*)d_ws);
}
```

```cpp
#include <hip/hip_runtime.h>
#include <stdint.h>

typedef unsigned short u16;
typedef unsigned u32;
typedef unsigned long long u64;
typedef u32 v4u __attribute__((ext_vector_type(4)));
typedef u32 v2u __attribute__((ext_vector_type(2)));
typedef float f32x4 __attribute__((ext_vector_type(4)));
typedef float f32x16 __attribute__((ext_vector_type(16)));
typedef float f2 __attribute__((ext_vector_type(2)));
typedef __bf16 bf16x8 __attribute__((ext_vector_type(8)));
typedef __bf16 bf16x2 __attribute__((ext_vector_type(2)));
typedef short s16x4 __attribute__((ext_vector_type(4)));
#define LAS __attribute__((address_space(3)))
#define GAS __attribute__((address_space(1)))
#define DI __device__ __forceinline__
#define RLX_AGENT __ATOMIC_RELAXED, __HIP_MEMORY_SCOPE_AGENT
#define PRETOUCH
#define SC1 16
#define BTSC1 true
#define WAUX 16

constexpr int L_ = 2048, D_ = 448, N_ = 256, F_ = 1792;
constexpr u32 WS_EP = 0;
constexpr u32 WS_SG = 4096;
constexpr u32 WS_WF = 32768;
constexpr u32 WS_CS = 40960;
constexpr u32 WS_WR = 1966080;
constexpr u32 WS_HT = 65536;
constexpr u32 WS_HH = 2u << 20;
constexpr u32 WS_WG = 10u << 20;
constexpr u32 WS_WD = 14u << 20;
constexpr u32 WS_XG = 16u << 20;
constexpr u32 WS_END = 24u << 20;
constexpr u32 WS_DBG = 24u << 20;
constexpr u32 POISON = 0xFFFFFFFFu;
constexpr u32 SPIN_CAP = 1u << 16;

constexpr u32 LDS_BYTES = 163840;
constexpr u32 KF = 0, KF_W = 53 * 272;
constexpr u32 FT = 8 * KF_W, FT_CH = 772 * 4;
constexpr u32 RT = 121600;
constexpr u32 KC = 0, KC_COPY = 4160, KC_CH = 4 * KC_COPY;
constexpr u32 UU = 2 * KC_CH, UU_CH = (256 + 2048) * 2;
constexpr u32 XS = UU + 2 * UU_CH;
constexpr u32 CX = XS + 8192;
static_assert(FT + 2 * FT_CH <= RT && RT + 16384 <= LDS_BYTES && CX + 8 * 3072 <= FT, "LDS map");
constexpr u32 BT = 0, BT_BYTES = 448 * 256;
constexpr u32 RING = 0, RING_SLOTS = 19;
constexpr u32 SM = 155648;
constexpr u32 SP = SM;
constexpr u32 SQ = SP + 1024;
constexpr u32 CSL = SQ + 1024;
static_assert(RING_SLOTS * 8192 <= SM && CSL + 1024 <= 163328, "LDS map");

struct EoTab { u16 v[256 * 16]; };
constexpr u32 foff_c(int j) {
    if (j < 0) return 768u;
    u32 t = (u32)j;
    int s = t >= 1024 ? 3 : t >= 512 ? 2 : t >= 256 ? 1 : 0;
    if (s > 0) t = ((t + (1u << (s - 1)) - 1u + ((t >> s) & 1u)) >> s) << s;
    u32 col = (t >> 3) & 31u, row = 0;
    if (t < 256u) row = t & 7u;
    else if (t < 512u) row = 8u + ((t >> 1) & 3u);
    else if (t < 1024u) row = 12u + 2u * ((t >> 8) & 1u) + ((t >> 2) & 1u);
    else if (t < 2048u) row = 16u + ((t >> 8) & 3u);
    else row = 20u;
    return row * 32u + col;
}
constexpr EoTab make_eo() { EoTab e{}; for (int t = 0; t < 256; ++t) for (int k = 0; k < 16; ++k) e.v[t * 16 + k] = (u16)(k < 11 ? foff_c(2047 - 8 * t - k) * 4u : 0u); return e; }
__device__ const EoTab EO = make_eo();

DI float bflo(u32 d) { return __uint_as_float(d << 16); }
DI float bfhi(u32 d) { return __uint_as_float(d & 0xffff0000u); }
DI u32 pk2(float lo, float hi) { bf16x2 v = {(__bf16)lo, (__bf16)hi}; return __builtin_bit_cast(u32, v); }
DI float rbf(float f) { return bflo(pk2(f, f)); }
DI bf16x8 as_bf(v4u v) { return __builtin_bit_cast(bf16x8, v); }
DI f32x4 mfma16(v4u a, v4u b, f32x4 c) { return __builtin_amdgcn_mfma_f32_16x16x32_bf16(as_bf(a), as_bf(b), c, 0, 0, 0); }
DI f32x16 mfma32(v4u a, v4u b, f32x16 c) { return __builtin_amdgcn_mfma_f32_32x32x16_bf16(as_bf(a), as_bf(b), c, 0, 0, 0); }
template <class T> DI LAS T* lds(u32 off) { return (LAS T*)(uintptr_t)off; }
DI v2u tr_read(u32 off) { s16x4 r = __builtin_amdgcn_ds_read_tr16_b64_v4i16(lds<s16x4>(off)); return __builtin_bit_cast(v2u, r); }
DI v4u tr_frag(u32 off0, u32 off1) { v2u a = tr_read(off0), b = tr_read(off1); return v4u{a.x, a.y, b.x, b.y}; }
template <int CTRL, int RM> DI float dpp_add(float v) { int t = __builtin_amdgcn_update_dpp(0, __float_as_int(v), CTRL, RM, 0xf, true); return v + __int_as_float(t); }
DI float wave_sum(float v) {
    v = dpp_add<0x111, 0xf>(v); v = dpp_add<0x112, 0xf>(v); v = dpp_add<0x114, 0xf>(v); v = dpp_add<0x118, 0xf>(v);
    v = dpp_add<0x142, 0xa>(v); v = dpp_add<0x143, 0xc>(v);
    return __int_as_float(__builtin_amdgcn_readlane(__float_as_int(v), 63));
}
DI float half_sum(float v) { const u32 u = __float_as_uint(v); const auto r = __builtin_amdgcn_permlane32_swap(u, u, false, false); return __uint_as_float(r[0]) + __uint_as_float(r[1]); }
DI void fence() { asm volatile("" ::: "memory"); }
DI void bar() { asm volatile("s_waitcnt lgkmcnt(0)" ::: "memory"); __builtin_amdgcn_s_barrier(); asm volatile("" ::: "memory"); }
template <int N> DI void vmw() { asm volatile("s_waitcnt vmcnt(%0)" :: "n"(N) : "memory"); }
template <bool S1> DI void glds16(u32 voff, const void* sbase, u32 ldst) {
    u32 keep;
    if (S1) asm volatile("s_mov_b32 %0, m0\n\ts_mov_b32 m0, %3\n\ts_nop 0\n\tglobal_load_lds_dwordx4 %1, %2 sc1\n\ts_mov_b32 m0, %0" : "=&s"(keep) : "v"(voff), "s"(sbase), "s"(ldst) : "memory");
    else    asm volatile("s_mov_b32 %0, m0\n\ts_mov_b32 m0, %3\n\ts_nop 0\n\tglobal_load_lds_dwordx4 %1, %2\n\ts_mov_b32 m0, %0" : "=&s"(keep) : "v"(voff), "s"(sbase), "s"(ldst) : "memory");
}
DI float dot2(u32 a, u32 b, float c) { return __builtin_amdgcn_fdot2_f32_bf16(__builtin_bit_cast(bf16x2, a), __builtin_bit_cast(bf16x2, b), c, false); }
DI f2 cmul(f2 a, f2 b) { f2 t = f2{a.x, a.x} * b; return __builtin_elementwise_fma(f2{a.y, a.y}, f2{-b.y, b.x}, t); }
DI f2 cexp_rev(float ar, float ai, float t) {
    const float e = __builtin_amdgcn_exp2f(ar * t), ph = __builtin_amdgcn_fractf(ai * t);
    return f2{e * __builtin_amdgcn_cosf(ph), e * __builtin_amdgcn_sinf(ph)};
}
DI u32 swz_b(u32 row) { return ((row & 3u) << 2) | ((row >> 2) & 3u); }
DI u32 off_b(u32 row, u32 ch) { return 256u * row + 16u * (ch ^ swz_b(row)); }
DI u32 off_a(u32 row, u32 ch) { return 2048u * (row >> 3) + 512u * (ch >> 2) + 64u * (row & 7u) + 16u * ((ch & 3u) ^ ((row >> 2) & 3u)); }
DI u32 max4(v4u v) { return max(max(v.x, v.y), max(v.z, v.w)); }
DI u32 san(u32 v) { return min(v, 0xfffffffeu); }
DI u32 ln_pack(float mean, float var) {
    const float m = rbf(mean), sd = rbf(__builtin_amdgcn_sqrtf(rbf(rbf(var) + 1e-5f)));
    return (__float_as_uint(m) >> 16) | (__float_as_uint(sd) & 0xffff0000u);
}

#define STAMP(i) do { } while (0)
#define STAMP_DECL
#define STAMP_FLUSH do { } while (0)
#define SGB(mask, n) __builtin_amdgcn_sched_group_barrier(mask, n, 0)
#define SB0() __builtin_amdgcn_sched_barrier(0)
constexpr int M_MFMA = 0x8, M_VMEM = 0x10, M_DSR = 0x100, M_VALU = 0x2;
DI u32 opaque(u32 v) { asm volatile("" : "+v"(v)); return v; }

template <int TG> struct Tiles;
template <> struct Tiles<0> { static constexpr int n = 3; static constexpr int t[5] = {7, 6, 2, 0, 0}; };
template <> struct Tiles<1> { static constexpr int n = 5; static constexpr int t[5] = {5, 4, 3, 1, 0}; };

template <int TG> DI void conv_wave(int tid, int w, int lane, int cm, float dsv, __amdgpu_buffer_rsrc_t rsW, v4u (&Wf)[28], u32 wf_off, u32 hh_off) {
    typedef Tiles<TG> T;
    constexpr int NP = T::t[0] + 1;
    const int ch = w >> 2, rh = (w >> 1) & 1;
    f32x4 acc[T::n];
#pragma unroll
    for (int i = 0; i < T::n; ++i) acc[i] = f32x4{0.f, 0.f, 0.f, 0.f};
    const u32 cb = lane & 15, cq = lane >> 4, cs = (3u - cb) & 3u;
    const u32 kbase = opaque(KC + ch * KC_CH + cs * KC_COPY + 2 * (2047 - 16 * (cq >> 1) - cb + 8 * (cq & 1) - cs) - 32 * 8 * rh - 32 * 118);
    const u32 kbase8 = opaque(kbase + 8);
    const u32 ubase = opaque(UU + ch * UU_CH + 512 + 32 * (cb - (cq >> 1)) + 16 * (cq & 1) - 32 * 8 * rh - 32 * 6);
    const v4u pz = {POISON, POISON, POISON, POISON};
#pragma unroll
    for (int rr = 0; rr < 4; ++rr) {
        v4u Af[NP], Bf[NP];
#pragma unroll
        for (int p = 0; p < NP; ++p) {
            const v2u lo = *lds<volatile v2u>(kbase + 32 * (118 - (16 * p + 2 * rr))), hi = *lds<volatile v2u>(kbase8 + 32 * (118 - (16 * p + 2 * rr)));
            Af[p] = v4u{lo.x, lo.y, hi.x, hi.y};
            Bf[p] = *lds<v4u>(ubase + 32 * (16 * p - 2 * rr + 6));
        }
        if (rr == 0) {
#pragma unroll
            for (int i = 0; i < 4; ++i) __builtin_amdgcn_raw_buffer_store_b128(pz, rsW, hh_off + i * 8192, 0, SC1);
            SB0();
        }
#pragma unroll
        for (int i = 0; i < 7; ++i) Wf[7 * rr + i] = __builtin_amdgcn_raw_buffer_load_b128(rsW, wf_off + (7 * rr + i) * 1024, 0, WAUX);
#pragma unroll
        for (int p = 0; p < NP; ++p)
#pragma unroll
            for (int i = 0; i < T::n; ++i)
                if (p <= T::t[i]) acc[i] = mfma16(Af[p], Bf[T::t[i] - p], acc[i]);
        if (rr != 0) SGB(M_DSR, 3 * NP);
#pragma unroll
        for (int i = 0; i < 6; ++i) { SGB(M_MFMA, 2); SGB(M_VMEM, 1); }
        SGB(M_MFMA, 6); SGB(M_VMEM, 1);
        SB0();
    }
    STAMP(30);
    const u32 mine = CX + w * 3072, other = CX + (w ^ 2) * 3072;
    if (rh == 0) {
#pragma unroll
        for (int i = 1; i < T::n; i += 2) *lds<f32x4>(mine + (i >> 1) * 1024 + lane * 16) = acc[i];
    } else {
#pragma unroll
        for (int i = 0; i < T::n; i += 2) *lds<f32x4>(mine + (i >> 1) * 1024 + lane * 16) = acc[i];
    }
    vmw<28>();
    STAMP(31);
    bar();
    STAMP(16);
    auto fin = [&](int i, f32x4 own) {
        const f32x4 y = own + *lds<f32x4>(other + (i >> 1) * 1024 + lane * 16);
        const u32 l0 = 256 * T::t[i] + 16 * cb + 4 * cq;
        const v2u uu = *lds<v2u>(UU + ch * UU_CH + 512 + l0 * 2), xx = *lds<v2u>(XS + ch * 4096 + l0 * 2);
        const float uv[4] = {bflo(uu.x), bfhi(uu.x), bflo(uu.y), bfhi(uu.y)}, xv[4] = {bflo(xx.x), bfhi(xx.x), bflo(xx.y), bfhi(xx.y)};
        float h[4];
#pragma unroll
        for (int r = 0; r < 4; ++r) h[r] = xv[r] + rbf(rbf(y[r]) + rbf(uv[r] * dsv));
        const v2u o2 = {san(pk2(h[0], h[1])), san(pk2(h[2], h[3]))};
        __builtin_amdgcn_raw_buffer_store_b64(o2, rsW, WS_HT + (2 * cm + ch) * 4096 + l0 * 2, 0, SC1);
    };
    if (rh == 0) {
#pragma unroll
        for (int i = 0; i < T::n; i += 2) fin(i, acc[i]);
    } else {
#pragma unroll
        for (int i = 1; i < T::n; i += 2) fin(i, acc[i]);
    }
}

__global__ void __launch_bounds__(512, 2)
fused_kernel(const u16* __restrict__ x, const u16* __restrict__ Are, const u16* __restrict__ Aim, const u16* __restrict__ Bre, const u16* __restrict__ Bim,
             const u16* __restrict__ Cre, const u16* __restrict__ Cim, const u16* __restrict__ logdt, const u16* __restrict__ Dskip,
             const u16* __restrict__ wg, const u16* __restrict__ wu, const u16* __restrict__ wd, u16* __restrict__ out, char* __restrict__ ws)
{
    const int tid = threadIdx.x, lane = tid & 63, w = __builtin_amdgcn_readfirstlane(tid >> 6);
    const int b = blockIdx.x, g = b & 7, j = b >> 3;
    const bool mainb = j < 28;
    const int cm = mainb ? g * 28 + j : 0;
    const int P = 4 * (g >> 1) + j / 7, S = 7 * (g & 1) + j % 7;
    const __amdgpu_buffer_rsrc_t rsW = __builtin_amdgcn_make_buffer_rsrc(ws, 0, WS_END, 0x00020000);
    const __amdgpu_buffer_rsrc_t rsX = __builtin_amdgcn_make_buffer_rsrc((void*)x, 0, L_ * D_ * 2, 0x00020000);
    const u32 trq = (lane & 15) >> 2, trp = lane & 3, trh = lane >> 5, trb = (lane >> 4) & 1;
    STAMP_DECL
    STAMP(0);
    const v4u pz = {POISON, POISON, POISON, POISON};
    __builtin_amdgcn_raw_buffer_store_b128(pz, rsW, mainb ? (u32)(WS_HT + cm * 8192 + tid * 16) : 0x7ffffff0u, 0, SC1);
    const v4u xr = __builtin_amdgcn_raw_buffer_load_b128(rsX, lane < 56 ? (u32)((8 * b + w) * 896 + lane * 16) : 0x7ffffff0u, 0, 0);
    const u64 ep = __hip_atomic_load((GAS u64*)(ws + WS_EP), RLX_AGENT) + 1ull;
    const u32 tag = (u32)ep, tagx = (u32)(ep >> 32) << 16;
    const int ch = w >> 2, dch = 2 * cm + ch, po = dch * N_ + (tid & 255);
    const u32 p_are = Are[po], p_aim = Aim[po], p_bre = Bre[po], p_bim = Bim[po], p_cre = Cre[po], p_cim = Cim[po];
    const u32 p_ldt = logdt[dch], p_dsk = Dskip[dch];
    u32 ptv = 0;
    if (w == 7) {
        ptv = __builtin_amdgcn_raw_buffer_load_b32(rsW, (lane < 48 ? lane : 0) * (512u << 10) + 8192, 0, 0);
        ptv ^= ((const GAS u32*)out)[(lane & 7) * 50000 + b];
        ptv ^= ((const GAS u32*)wd)[(lane & 7) * 50000 + b];
        ptv ^= ((const GAS u32*)wg)[(lane & 7) * 50000 + b] ^ ((const GAS u32*)wu)[(lane & 7) * 50000 + b];
    }
    const v4u eo0 = *(const v4u*)&EO.v[(tid & 255) * 16];
    const v2u eo1 = *(const v2u*)&EO.v[(tid & 255) * 16 + 8];
    fence();
    if (mainb) {
        if (b < 196) {
#pragma unroll
            for (int i = 0; i < 2; ++i) {
                const int id = 2 * b + i, Sp = id / 28, s = id % 28;
                const u32 row = (w & 3) * 4 + (lane >> 4), chk = (lane & 15) ^ swz_b(row);
                glds16<false>((u32)((16 * s + row) * (F_ * 2) + Sp * 256 + chk * 16), (w >> 2) ? (const void*)wu : (const void*)wg, RT + i * 8192 + w * 1024);
            }
        }
    } else {
        const int hb = (j - 28) * 8 + g;
#pragma unroll
        for (int k = 0; k < 7; ++k) {
            const int gid = hb + 32 * k;
            if (gid < 196) {
                const int dt_ = gid / 28, fg = gid % 28;
                const u32 row = 8 * w + (lane >> 3), chk = (lane & 7) ^ (((row >> 1) & 1) << 2);
                glds16<false>((u32)((64 * fg + row) * (D_ * 2) + dt_ * 128 + chk * 16), (const void*)wd, k * 8192 + w * 1024);
            }
        }
    }
    constexpr u32 XT = 140288;
    if (lane < 56) *lds<v4u>(XT + w * 912 + lane * 16) = xr;
    bar();
#pragma unroll
    for (int k = 0; k < 4; ++k) {
        const int id = tid + 512 * k, c = id >> 2, i2 = (id & 3) * 2;
        if (id < 1792) {
            const u32 x0 = *lds<u16>(XT + i2 * 912 + c * 2), x1 = *lds<u16>(XT + (i2 + 1) * 912 + c * 2);
            __builtin_amdgcn_raw_buffer_store_b128(v4u{x0 | tagx, tag, x1 | tagx, tag}, rsW, WS_XG + b * 28672 + id * 16, 0, SC1);
        }
    }
    STAMP(25);
    u32 ln1;
    {
        const float v[8] = {bflo(xr.x), bfhi(xr.x), bflo(xr.y), bfhi(xr.y), bflo(xr.z), bfhi(xr.z), bflo(xr.w), bfhi(xr.w)};
        const float mean = wave_sum(((v[0] + v[1]) + (v[2] + v[3])) + ((v[4] + v[5]) + (v[6] + v[7]))) * (1.f / 448.f);
        float q = 0.f;
#pragma unroll
        for (int e = 0; e < 8; ++e) { const float dl = v[e] - mean; q += dl * dl; }
        q = lane < 56 ? q : 0.f;
        ln1 = ln_pack(mean, wave_sum(q) * (1.f / 448.f));
    }
    asm volatile("" :: "v"(ptv));
    STAMP(1);
    if (!mainb) {
        if (lane == 0) __hip_atomic_store((GAS u64*)(ws + WS_SG) + (8 * b + w), ((u64)tag << 32) | ln1, RLX_AGENT);
        const int hb = (j - 28) * 8 + g;
        float cs[8];
        {
            const u16* wsrc = hb < 16 ? wg : wu;
            const int c0 = 112 * (hb & 15) + 8 * (tid % 14), r0 = tid / 14;
#pragma unroll
            for (int e = 0; e < 8; ++e) cs[e] = 0.f;
            v4u t[13];
#pragma unroll
            for (int i = 0; i < 13; ++i) { const int r = r0 + 36 * i; t[i] = (tid < 504 && r < 448) ? *(const v4u*)(wsrc + (size_t)r * F_ + c0) : v4u{0, 0, 0, 0}; }
#pragma unroll
            for (int i = 0; i < 13; ++i) { cs[0] += bflo(t[i].x); cs[1] += bfhi(t[i].x); cs[2] += bflo(t[i].y); cs[3] += bfhi(t[i].y); cs[4] += bflo(t[i].z); cs[5] += bfhi(t[i].z); cs[6] += bflo(t[i].w); cs[7] += bfhi(t[i].w); }
        }
        vmw<0>(); bar();
#pragma unroll
        for (int k = 0; k < 7; ++k) {
            const int gid = hb + 32 * k;
            if (gid < 196) {
                const int dt_ = gid / 28, fg = gid % 28, fsl = w >> 1, td = w & 1;
                u32 ad[2];
#pragma unroll
                for (int t = 0; t < 2; ++t) { const u32 row = 16 * fsl + 8 * t + 4 * trh + trq, chk = 4 * td + 2 * trb + (trp >> 1); ad[t] = k * 8192 + row * 128 + 16 * (chk ^ (((row >> 1) & 1) << 2)) + 8 * (trp & 1); }
                const v4u f = tr_frag(ad[0], ad[1]);
                __builtin_amdgcn_raw_buffer_store_b128(f, rsW, WS_WD + ((dt_ * 2 + td) * 112 + 4 * fg + fsl) * 1024 + lane * 16, 0, SC1);
            }
        }
        if (tid < 504) {
            *lds<f32x4>(65536 + tid * 32) = f32x4{cs[0], cs[1], cs[2], cs[3]};
            *lds<f32x4>(65536 + tid * 32 + 16) = f32x4{cs[4], cs[5], cs[6], cs[7]};
        }
        bar();
        if (tid < 112) {
            float s = 0.f;
            for (int r = 0; r < 36; ++r) s += *lds<float>(65536 + (r * 14 + (tid >> 3)) * 32 + (tid & 7) * 4);
            __hip_atomic_store((GAS u32*)(ws + WS_CS) + ((hb < 16 ? 0 : 1792) + 112 * (hb & 15) + tid), __float_as_uint(s), RLX_AGENT);
        }
        vmw<0>(); bar();
        if (tid == 0) __hip_atomic_store((GAS u32*)(ws + WS_WF + hb * 64), tag, RLX_AGENT);
        return;
    }
    if (lane == 0) __hip_atomic_store((GAS u64*)(ws + WS_SG) + (8 * b + w), ((u64)tag << 32) | ln1, RLX_AGENT);
    if (w < 4) vmw<5>(); else vmw<4>();
    bar();
    if (b < 196) {
#pragma unroll
        for (int i = 0; i < 2; ++i) {
            const int id = 2 * b + i, Sp = id / 28, s = id % 28;
            const u32 base = RT + i * 8192 + trb * 4096 + 8 * (trp & 1);
            const v4u f = tr_frag(base + off_b(8 * trh + trq, 2 * w + (trp >> 1)), base + off_b(8 * trh + 4 + trq, 2 * w + (trp >> 1)));
            __builtin_amdgcn_raw_buffer_store_b128(f, rsW, WS_WG + ((Sp * 8 + w) * 28 + s) * 1024 + lane * 16, 0, SC1);
        }
    }
    fence();
    STAMP(2);
    {
        const u32 kfw = KF + w * KF_W;
        const float dt = rbf(expf(bflo(p_ldt)));
        const float zr = dt * bflo(p_are), zi = dt * bflo(p_aim);
        const float ar = zr * 1.4426950408889634f, ai = zi * 0.15915494309189535f;
        const f2 CB = cmul(f2{bflo(p_cre), bflo(p_cim)}, f2{dt * bflo(p_bre), dt * bflo(p_bim)});
        const f2 E1 = cexp_rev(ar, ai, 1.f), G1 = cexp_rev(ar, ai, 8.f), E256 = cexp_rev(ar, ai, 256.f);
        const f2 E2 = cmul(E1, E1), E4 = cmul(E2, E2), G2 = cmul(G1, G1), G4 = cmul(G2, G2);
        LAS u32* row = lds<u32>(kfw + lane * 4);
#define WA(r, X) row[(r) * 68] = pk2((X).x, -(X).y)
#define WB(r, X) row[(21 + (r)) * 68] = pk2((X).x, (X).y)
        {
            f2 Ga = f2{1.f, 0.f}, Gb = G1, Gc = G2, Gd = cmul(G2, G1);
#pragma unroll
            for (int t = 0; t < 32; t += 4) {
                WB(t, Ga); WB(t + 1, Gb); WB(t + 2, Gc); WB(t + 3, Gd);
                if (t < 28) { Ga = cmul(Ga, G4); Gb = cmul(Gb, G4); Gc = cmul(Gc, G4); Gd = cmul(Gd, G4); }
            }
        }
        {
            f2 X = CB, Y = cmul(CB, E256);
            WA(0, X); WA(8, Y);
            f2 Z = cmul(Y, E2); X = cmul(X, E1); Y = cmul(Y, E256); WA(9, Z); WA(1, X); WA(12, Y);
            { f2 V = cmul(Y, E4); Z = cmul(Z, E2); X = cmul(X, E1); Y = cmul(Y, E256); WA(13, V); WA(10, Z); WA(2, X); WA(14, Y); }
            { f2 V = cmul(Y, E4); Z = cmul(Z, E2); X = cmul(X, E1); Y = cmul(Y, E256); WA(15, V); WA(11, Z); WA(3, X); WA(16, Y); }
#pragma unroll
            for (int t = 0; t < 4; ++t) { X = cmul(X, E1); Y = cmul(Y, E256); WA(4 + t, X); WA(17 + t, Y); }
        }
#undef WA
#undef WB
        f32x16 acc;
#pragma unroll
        for (int i = 0; i < 16; ++i) acc[i] = 0.f;
#pragma unroll
        for (int ks = 0; ks < 8; ++ks) {
            const v4u A = *lds<v4u>(kfw + (lane & 31) * 272 + ks * 32 + (lane >> 5) * 16);
            const v4u B = *lds<v4u>(kfw + (21 + (lane & 31)) * 272 + ks * 32 + (lane >> 5) * 16);
            acc = mfma32(A, B, acc);
        }
#pragma unroll
        for (int r = 0; r < 12; ++r) *lds<float>(kfw + r * 256 + lane * 4) = acc[r];
        STAMP(3);
        vmw<0>();
        bar();
        if (tid == 0 && b < 196) __hip_atomic_store((GAS u32*)(ws + WS_WR + b * 64), tag, RLX_AGENT);
#pragma unroll
        for (int kk = 0; kk < 3; ++kk) {
            const int r = (w & 3) + 4 * kk;
            float s = 0.f;
#pragma unroll
            for (int ww = 0; ww < 4; ++ww) s += *lds<float>(KF + (4 * ch + ww) * KF_W + r * 256 + lane * 4);
            *lds<float>(FT + ch * FT_CH + (((w & 3) + 8 * kk + 4 * (lane >> 5)) * 32 + (lane & 31)) * 4) = s;
        }
        if ((tid & 255) == 0) *lds<float>(FT + ch * FT_CH + 768 * 4) = 0.f;
        bar();
        {
            const u32 eo[11] = {eo0.x & 0xffffu, eo0.x >> 16, eo0.y & 0xffffu, eo0.y >> 16, eo0.z & 0xffffu, eo0.z >> 16, eo0.w & 0xffffu, eo0.w >> 16, eo1.x & 0xffffu, eo1.x >> 16, eo1.y & 0xffffu};
            float v[11];
#pragma unroll
            for (int e = 0; e < 11; ++e) v[e] = *lds<float>(FT + ch * FT_CH + eo[e]);
            u32 p[10];
#pragma unroll
            for (int e = 0; e < 10; ++e) p[e] = pk2(v[e], v[e + 1]);
#pragma unroll
            for (int s = 0; s < 4; ++s) *lds<v4u>(KC + ch * KC_CH + s * KC_COPY + (tid & 255) * 16) = v4u{p[s], p[s + 2], p[s + 4], p[s + 6]};
        }
        if (tid < 16) *lds<v4u>(KC + (tid >> 3) * KC_CH + ((tid >> 1) & 3) * KC_COPY + 4096 + (tid & 1) * 16) = v4u{0, 0, 0, 0};
        if (tid >= 64 && tid < 128) *lds<v4u>(UU + ((tid >> 5) & 1) * UU_CH + (tid & 31) * 16) = v4u{0, 0, 0, 0};
    }
    STAMP(4);
    {
        v4u xg[4], sg[4];
        const u32 fa = WS_WR + (tid < 196 ? tid : 0) * 64;
        const u32 xa = WS_XG + (32 * w + (lane >> 3)) * 28672 + cm * 128 + (lane & 7) * 16, sa = WS_SG + (32 * w + (lane >> 3)) * 64 + (lane & 3) * 16;
        for (u32 spin = 0;; ++spin) {
#pragma unroll
            for (int k = 0; k < 4; ++k) { xg[k] = __builtin_amdgcn_raw_buffer_load_b128(rsW, xa + k * 8 * 28672, 0, SC1); sg[k] = __builtin_amdgcn_raw_buffer_load_b128(rsW, sa + k * 512, 0, SC1); }
            const u32 fl = __builtin_amdgcn_raw_buffer_load_b32(rsW, fa, 0, SC1);
            bool ok = fl == tag;
#pragma unroll
            for (int k = 0; k < 4; ++k) ok = ok && sg[k].y == tag && sg[k].w == tag && xg[k].y == tag && xg[k].w == tag && (xg[k].x & 0xffff0000u) == tagx && (xg[k].z & 0xffff0000u) == tagx;
            if (__all(ok) || spin > SPIN_CAP) break;
            __builtin_amdgcn_s_sleep(1);
        }
        STAMP(5);
        const int c = (lane >> 2) & 1;
#pragma unroll
        for (int k = 0; k < 4; ++k) {
            const u32 l0 = 8 * (32 * w + 8 * k + (lane >> 3)) + 2 * (lane & 3);
            const float u0 = rbf(bflo(xg[k].x) - bflo(sg[k].x)) * __builtin_amdgcn_rcpf(bfhi(sg[k].x));
            const float u1 = rbf(bflo(xg[k].z) - bflo(sg[k].z)) * __builtin_amdgcn_rcpf(bfhi(sg[k].z));
            *lds<u32>(UU + c * UU_CH + 512 + l0 * 2) = pk2(u0, u1);
            *lds<u32>(XS + c * 4096 + l0 * 2) = (xg[k].x & 0xffffu) | (xg[k].z << 16);
        }
    }
    bar();
    STAMP(6);
    v4u Wf[28];
    {
        const u32 wf_off = WS_WG + (S * 8 + w) * 28 * 1024 + lane * 16, hh_off = WS_HH + (P * 112 + 8 * S) * 4096 + tid * 16;
        if (w & 1) conv_wave<1>(tid, w, lane, cm, bflo(p_dsk), rsW, Wf, wf_off, hh_off); else conv_wave<0>(tid, w, lane, cm, bflo(p_dsk), rsW, Wf, wf_off, hh_off);
    }
    STAMP(7);
    const int l0p = 128 * P;
    {
        const u32 pa = tid < 448 ? (u32)(WS_HT + tid * 4096 + l0p * 2 + 252) : tid < 480 ? (u32)(WS_WF + (tid - 448) * 64) : (u32)WS_WF;
        for (u32 spin = 0;; ++spin) {
            const u32 s0 = __builtin_amdgcn_raw_buffer_load_b32(rsW, pa, 0, SC1);
            const bool ok = tid < 448 ? s0 != POISON : s0 == tag;
            if (spin == 0) STAMP(27);
            if (__all(ok || tid >= 480) || spin > SPIN_CAP) { STAMP(28);
                break; }
            __builtin_amdgcn_s_sleep(1);
        }
    }
    bar();
    STAMP(8);
    if (b == 0 && tid == 0) __hip_atomic_store((GAS u64*)(ws + WS_EP), ep, RLX_AGENT);
    const u32 bt_src = (8 * (w >> 1) + ((lane >> 2) & 7)) * 4096 + l0p * 2 + (4 * (2 * (w & 1) + (lane >> 5)) + ((lane & 3) ^ ((2 * (w >> 1) + ((lane >> 4) & 1)) & 3))) * 16;
    const u32 bt_dst = BT + (w >> 1) * 2048 + (w & 1) * 1024;
    auto bt_piece = [&](int k) { glds16<BTSC1>(bt_src + k * 32 * 4096, ws + WS_HT, bt_dst + k * 8192); };
    auto bt_fix = [&](int k0, int k1) {
        for (u32 spin = 0; spin < SPIN_CAP; ++spin) {
            u32 mx = 0;
            for (int k = k0; k < k1; ++k) mx = max(mx, max4(*lds<v4u>(bt_dst + k * 8192 + lane * 16)));
            if (__all(mx != POISON)) break;
            for (int k = k0; k < k1; ++k) bt_piece(k);
            vmw<0>();
        }
    };
    const float csum = __uint_as_float(__builtin_amdgcn_raw_buffer_load_b32(rsW, WS_CS + (((lane >> 4) & 1) * 1792 + 128 * S + 16 * w + (lane & 15)) * 4, 0, SC1));
    fence();
#pragma unroll
    for (int k = 0; k < 6; ++k) bt_piece(k);
    vmw<4>();
    bt_fix(0, 2);
    bar();
    STAMP(9);
    f32x16 acc[4];
    v4u Wd[28];
    const int td = w & 1, kq = w >> 1, hp = S / 7, dtile = S % 7;
    const u32 wd_off = WS_WD + ((dtile * 2 + td) * 112 + 28 * kq) * 1024 + lane * 16;
    u32 bb[2];
#pragma unroll
    for (int tt = 0; tt < 2; ++tt) bb[tt] = opaque(BT + 2048 * trh + 64 * (4 * tt + trq) + 16 * ((2 * trb + (trp >> 1)) ^ ((2 * trh + tt) & 3)) + 8 * (trp & 1));
    {
#pragma unroll
        for (int t = 0; t < 4; ++t)
#pragma unroll
            for (int i = 0; i < 16; ++i) acc[t][i] = 0.f;
        v4u Bf[2][4];
#pragma unroll
        for (int t = 0; t < 4; ++t) Bf[0][t] = tr_frag(bb[0] + 512 * t, bb[1] + 512 * t);
#pragma unroll
        for (int s = 0; s < 28; ++s) {
            SB0();
            if (s == 3) { vmw<6>(); bt_fix(2, 6); bar(); }
            if (s == 11) { vmw<7>(); bt_fix(6, 14); bar(); }
            if (s + 1 < 28) {
#pragma unroll
                for (int t = 0; t < 4; ++t) Bf[(s + 1) & 1][t] = tr_frag(bb[0] + (s + 1) * 4096 + 512 * t, bb[1] + (s + 1) * 4096 + 512 * t);
            }
            if (s < 4) { bt_piece(6 + 2 * s); bt_piece(7 + 2 * s); }
            else Wd[s - 4] = __builtin_amdgcn_raw_buffer_load_b128(rsW, wd_off + (s - 4) * 1024, 0, WAUX);
#pragma unroll
            for (int t = 0; t < 4; ++t) acc[t] = mfma32(Wf[s], Bf[s & 1][t], acc[t]);
            if (s >= 4) {
                SGB(M_MFMA, 1); SGB(M_DSR, 2); SGB(M_MFMA, 1); SGB(M_DSR, 2); SGB(M_VMEM, 1); SGB(M_MFMA, 1); SGB(M_DSR, 2); SGB(M_MFMA, 1); SGB(M_DSR, 2);
            }
        }
        SB0();
        STAMP(17);
#pragma unroll
        for (int s = 24; s < 28; ++s) Wd[s] = __builtin_amdgcn_raw_buffer_load_b128(rsW, wd_off + s * 1024, 0, WAUX);
        float sumh = 0.f, sq = 0.f;
        const u32 so = (w >> 2) * 4096 + (w & 3) * 512;
#pragma unroll
        for (int s = 0; s < 14; ++s) {
            const v4u o = tr_frag(bb[0] + so + s * 8192, bb[1] + so + s * 8192);
#pragma unroll
            for (int e = 0; e < 4; ++e) { sumh = dot2(o[e], 0x3f803f80u, sumh); sq = dot2(o[e], o[e], sq); }
        }
        sumh = half_sum(sumh); sq = half_sum(sq);
        if (lane < 32) { *lds<float>(SP + w * 128 + lane * 4) = sumh; *lds<float>(SQ + w * 128 + lane * 4) = sq; *lds<float>(CSL + w * 128 + lane * 4) = csum; }
    }
    STAMP(10);
    bar();
    {
        f32x4 csg[2], csu[2];
#pragma unroll
        for (int q = 0; q < 2; ++q) { csg[q] = *lds<f32x4>(CSL + w * 128 + (8 * q + 4 * trh) * 4); csu[q] = *lds<f32x4>(CSL + w * 128 + 64 + (8 * q + 4 * trh) * 4); }
#pragma unroll
        for (int t = 0; t < 4; ++t) {
            const float m = rbf((*lds<float>(SP + t * 128 + (lane & 31) * 4) + *lds<float>(SP + (t + 4) * 128 + (lane & 31) * 4)) * (1.f / 448.f));
            float hv[8];
#pragma unroll
            for (int e = 0; e < 8; ++e) hv[e] = (acc[t][e] - m * csg[e >> 2][e & 3]) * (acc[t][e + 8] - m * csu[e >> 2][e & 3]);
            const v4u o = {san(pk2(hv[0], hv[1])), san(pk2(hv[2], hv[3])), san(pk2(hv[4], hv[5])), san(pk2(hv[6], hv[7]))};
            __builtin_amdgcn_raw_buffer_store_b128(o, rsW, WS_HH + ((P * 112 + 8 * S + w) * 4 + t) * 1024 + lane * 16, 0, SC1);
        }
    }
    STAMP(11);
    const int tl_f = kq >> 1;
    const u32 lp = 64 * hp + 32 * tl_f + (lane & 31);
    float hres[8], rs;
    {
        const int tq = 2 * hp + tl_f;
        const float s1 = *lds<float>(SP + tq * 128 + (lane & 31) * 4) + *lds<float>(SP + (tq + 4) * 128 + (lane & 31) * 4);
        const float s2 = *lds<float>(SQ + tq * 128 + (lane & 31) * 4) + *lds<float>(SQ + (tq + 4) * 128 + (lane & 31) * 4);
        const float mean = s1 * (1.f / 448.f), var = fmaxf(s2 * (1.f / 448.f) - mean * mean, 0.f);
        const float sd = bfhi(ln_pack(mean, var));
        rs = 1.f / (sd * sd);
#pragma unroll
        for (int e = 0; e < 8; ++e) {
            const u32 row = 64 * dtile + 32 * td + 16 * (kq & 1) + 8 * (e >> 2) + 4 * trh + (e & 3);
            hres[e] = bflo(*lds<u16>(BT + off_a(row, lp >> 3) + (lp & 7) * 2));
        }
    }
    const u32 hbase = WS_HH + ((P * 112 + 28 * kq) * 4 + 2 * hp) * 1024;
    {
        const u32 pa = hbase + ((lane < 56 ? lane : 0) >> 1) * 4096 + (lane & 1) * 1024 + 1020;
        for (u32 spin = 0;; ++spin) {
            const u32 s0 = __builtin_amdgcn_raw_buffer_load_b32(rsW, pa, 0, SC1);
            if (spin == 0) STAMP(18);
            if (__all(s0 != POISON) || spin > SPIN_CAP) { STAMP(19);
                break; }
            __builtin_amdgcn_s_sleep(1);
        }
    }
    bar();
    STAMP(12);
    const u32 hsrc = td * 1024 + lane * 16, hdst = RING + kq * 2048 + td * 1024, hrd = opaque(RING + kq * 2048 + lane * 16);
    auto hh_dma = [&](int s) { glds16<true>(hsrc + s * 4096, ws + hbase, hdst + (s % RING_SLOTS) * 8192); };
    f32x16 o2[2];
    for (u32 attempt = 0;; ++attempt) {
#pragma unroll
        for (int i = 0; i < 16; ++i) { o2[0][i] = 0.f; o2[1][i] = 0.f; }
        u32 mx = 0;
        constexpr int AH = 9;
#pragma unroll
        for (int s = 0; s < AH; ++s) hh_dma(s);
        v4u hf[2][2];
        vmw<AH - 4>(); bar();
        if (attempt == 0) STAMP(13);
#pragma unroll
        for (int tl = 0; tl < 2; ++tl) hf[0][tl] = *lds<v4u>(hrd + tl * 1024);
#pragma unroll
        for (int s = 0; s < 28; ++s) {
            SB0();
            if ((s & 3) == 3 && s + 1 < 28) {
                constexpr int dummy = 0; (void)dummy;
                const int issued = (s - 1 + AH < 27) ? s - 1 + AH : 27, need = (s + 4 < 27) ? s + 4 : 27;
                switch (issued - need) { case 0: vmw<0>(); break; case 1: vmw<1>(); break; case 2: vmw<2>(); break; case 3: vmw<3>(); break; default: vmw<4>(); break; }
                bar();
            }
            if (s + 1 < 28) {
#pragma unroll
                for (int tl = 0; tl < 2; ++tl) hf[(s + 1) & 1][tl] = *lds<v4u>(hrd + ((s + 1) % RING_SLOTS) * 8192 + tl * 1024);
            }
            if (s + AH < 28) hh_dma(s + AH);
            mx = max(mx, max(max4(hf[s & 1][0]), max4(hf[s & 1][1])));
#pragma unroll
            for (int tl = 0; tl < 2; ++tl) o2[tl] = mfma32(Wd[s], hf[s & 1][tl], o2[tl]);
        }
        SB0();
        const bool bad = !__all(mx != POISON);
        if (lane == 0) *lds<u32>(SM + 3072 + w * 4 + (attempt & 1) * 32) = bad;
        bar();
        const v4u f0 = *lds<v4u>(SM + 3072 + (attempt & 1) * 32), f1 = *lds<v4u>(SM + 3072 + 16 + (attempt & 1) * 32);
        if (!(f0.x | f0.y | f0.z | f0.w | f1.x | f1.y | f1.z | f1.w) || attempt > 64) break;
    }
    STAMP(14);
    {
#pragma unroll
        for (int k2 = 0; k2 < 4; ++k2) {
            if (k2 != kq) {
                const int si = (kq > k2) ? kq - 1 : kq;
                const f32x16& a = o2[k2 >> 1];
#pragma unroll
                for (int q = 0; q < 2; ++q) { const int r0 = 8 * (k2 & 1) + 4 * q; *lds<f32x4>(((td * 4 + k2) * 3 + si) * 2048 + q * 1024 + lane * 16) = f32x4{a[r0], a[r0 + 1], a[r0 + 2], a[r0 + 3]}; }
            }
        }
        bar();
        f32x4 v[2];
#pragma unroll
        for (int q = 0; q < 2; ++q) {
            if (kq == 0) v[q] = f32x4{o2[0][4 * q], o2[0][4 * q + 1], o2[0][4 * q + 2], o2[0][4 * q + 3]};
            else if (kq == 1) v[q] = f32x4{o2[0][8 + 4 * q], o2[0][9 + 4 * q], o2[0][10 + 4 * q], o2[0][11 + 4 * q]};
            else if (kq == 2) v[q] = f32x4{o2[1][4 * q], o2[1][4 * q + 1], o2[1][4 * q + 2], o2[1][4 * q + 3]};
            else v[q] = f32x4{o2[1][8 + 4 * q], o2[1][9 + 4 * q], o2[1][10 + 4 * q], o2[1][11 + 4 * q]};
#pragma unroll
            for (int si = 0; si < 3; ++si) v[q] += *lds<f32x4>(((td * 4 + kq) * 3 + si) * 2048 + q * 1024 + lane * 16);
            const u32 d0 = 64 * dtile + 32 * td + 16 * (kq & 1) + 8 * q + 4 * trh;
            float r[4];
#pragma unroll
            for (int e = 0; e < 4; ++e) r[e] = hres[4 * q + e] + rbf(v[q][e] * rs);
            *(GAS v2u*)(out + (size_t)(l0p + lp) * D_ + d0) = v2u{pk2(r[0], r[1]), pk2(r[2], r[3])};
        }
    }
    STAMP(15);
    STAMP_FLUSH;
}

extern "C" void kernel_launch(void* const* d_in, const int* in_sizes, int n_in, void* d_out, int out_size, void* d_ws, size_t ws_size, hipStream_t stream) {
    (void)in_sizes; (void)n_in; (void)out_size; (void)ws_size;
    (void)hipFuncSetAttribute((const void*)fused_kernel, hipFuncAttributeMaxDynamicSharedMemorySize, LDS_BYTES);
    fused_kernel<<<256, 512, LDS_BYTES, stream>>>((const u16*)d_in[0], (const u16*)d_in[1], (const u16*)d_in[2], (const u16*)d_in[3], (const u16*)d_in[4],
        (const u16*)d_in[5], (const u16*)d_in[6], (const u16*)d_in[7], (const u16*)d_in[8], (const u16*)d_in[10], (const u16*)d_in[11], (const u16*)d_in[12],
        (u16*)d_out, (char*)d_ws);
}
```

```cpp
#include <hip/hip_runtime.h>
#include <stdint.h>

typedef unsigned short u16;
typedef unsigned u32;
typedef unsigned long long u64;
typedef u32 v4u __attribute__((ext_vector_type(4)));
typedef u32 v2u __attribute__((ext_vector_type(2)));
typedef float f32x4 __attribute__((ext_vector_type(4)));
typedef float f32x16 __attribute__((ext_vector_type(16)));
typedef float f2 __attribute__((ext_vector_type(2)));
typedef __bf16 bf16x8 __attribute__((ext_vector_type(8)));
typedef __bf16 bf16x2 __attribute__((ext_vector_type(2)));
typedef short s16x4 __attribute__((ext_vector_type(4)));
#define LAS __attribute__((address_space(3)))
#define GAS __attribute__((address_space(1)))
#define DI __device__ __forceinline__
#define RLX_AGENT __ATOMIC_RELAXED, __HIP_MEMORY_SCOPE_AGENT
#define SC1 16
#define BTSC1 true
#define DLY1 0
#define DLY2 35
#define DLY3 35
#define WAUX 16

constexpr int L_ = 2048, D_ = 448, N_ = 256, F_ = 1792;
constexpr u32 WS_EP = 0;
constexpr u32 WS_SG = 4096;
constexpr u32 WS_WF = 32768;
constexpr u32 WS_CS = 40960;
constexpr u32 WS_WR = 1966080;
constexpr u32 WS_HT = 65536;
constexpr u32 WS_HH = 2u << 20;
constexpr u32 WS_WG = 10u << 20;
constexpr u32 WS_WD = 14u << 20;
constexpr u32 WS_XG = 16u << 20;
constexpr u32 WS_F2 = (23u << 20) + (512u << 10);
constexpr u32 WS_F3 = WS_F2 + 8 * 16384;
constexpr u32 WS_END = 24u << 20;
constexpr u32 WS_DBG = 24u << 20;
constexpr u32 POISON = 0xFFFFFFFFu;
constexpr u32 SPIN_CAP = 1u << 16;

constexpr u32 LDS_BYTES = 163840;
constexpr u32 FM = 0, FM_ROW = 1056, FM_CH = 53 * FM_ROW;
constexpr u32 FT = 2 * FM_CH, FT_CH = 772 * 4;
constexpr u32 EOL = FT + 2 * FT_CH;
constexpr u32 RT = 124288;
constexpr u32 PRM = RT + 16384;
constexpr u32 XT = PRM + 8192;
constexpr u32 KC = 0, KC_COPY = 4160, KC_CH = 4 * KC_COPY;
constexpr u32 CX = 2 * KC_CH;
constexpr u32 UU = RT, UU_CH = (256 + 2048) * 2;
constexpr u32 XS = UU + 2 * UU_CH;
static_assert(EOL % 16 == 0 && EOL + 6144 <= RT && XS + 8192 <= XT && XT + 8 * 912 <= 158848 && CX + 8 * 3072 <= FT, "LDS map");
constexpr u32 BT = 0, BT_BYTES = 448 * 256;
constexpr u32 RING = 0, RING_SLOTS = 19;
constexpr u32 SM = 155648;
constexpr u32 SP = SM;
constexpr u32 SQ = SP + 1024;
constexpr u32 CSL = SQ + 1024;
static_assert(RING_SLOTS * 8192 <= SM && CSL + 1024 <= 163328, "LDS map");

struct EoTab { u16 v[256 * 12]; };
constexpr u32 foff_c(int j) {
    if (j < 0) return 768u;
    u32 t = (u32)j;
    int s = t >= 1024 ? 3 : t >= 512 ? 2 : t >= 256 ? 1 : 0;
    if (s > 0) t = ((t + (1u << (s - 1)) - 1u + ((t >> s) & 1u)) >> s) << s;
    u32 col = (t >> 3) & 31u, row = 0;
    if (t < 256u) row = t & 7u;
    else if (t < 512u) row = 8u + ((t >> 1) & 3u);
    else if (t < 1024u) row = 12u + 2u * ((t >> 8) & 1u) + ((t >> 2) & 1u);
    else if (t < 2048u) row = 16u + ((t >> 8) & 3u);
    else row = 20u;
    return row * 32u + col;
}
constexpr EoTab make_eo() { EoTab e{}; for (int t = 0; t < 256; ++t) for (int k = 0; k < 12; ++k) e.v[t * 12 + k] = (u16)(k < 11 ? foff_c(2047 - 8 * t - k) * 4u : 0u); return e; }
__device__ const __attribute__((aligned(16))) EoTab EO = make_eo();

DI float bflo(u32 d) { return __uint_as_float(d << 16); }
DI float bfhi(u32 d) { return __uint_as_float(d & 0xffff0000u); }
DI u32 pk2(float lo, float hi) { bf16x2 v = {(__bf16)lo, (__bf16)hi}; return __builtin_bit_cast(u32, v); }
DI float rbf(float f) { return bflo(pk2(f, f)); }
DI bf16x8 as_bf(v4u v) { return __builtin_bit_cast(bf16x8, v); }
DI f32x4 mfma16(v4u a, v4u b, f32x4 c) { return __builtin_amdgcn_mfma_f32_16x16x32_bf16(as_bf(a), as_bf(b), c, 0, 0, 0); }
DI f32x16 mfma32(v4u a, v4u b, f32x16 c) { return __builtin_amdgcn_mfma_f32_32x32x16_bf16(as_bf(a), as_bf(b), c, 0, 0, 0); }
template <class T> DI LAS T* lds(u32 off) { return (LAS T*)(uintptr_t)off; }
DI v2u tr_read(u32 off) { s16x4 r = __builtin_amdgcn_ds_read_tr16_b64_v4i16(lds<s16x4>(off)); return __builtin_bit_cast(v2u, r); }
DI v4u tr_frag(u32 off0, u32 off1) { v2u a = tr_read(off0), b = tr_read(off1); return v4u{a.x, a.y, b.x, b.y}; }
template <int CTRL, int RM> DI float dpp_add(float v) { int t = __builtin_amdgcn_update_dpp(0, __float_as_int(v), CTRL, RM, 0xf, true); return v + __int_as_float(t); }
DI float wave_sum(float v) {
    v = dpp_add<0x111, 0xf>(v); v = dpp_add<0x112, 0xf>(v); v = dpp_add<0x114, 0xf>(v); v = dpp_add<0x118, 0xf>(v);
    v = dpp_add<0x142, 0xa>(v); v = dpp_add<0x143, 0xc>(v);
    return __int_as_float(__builtin_amdgcn_readlane(__float_as_int(v), 63));
}
DI float half_sum(float v) { const u32 u = __float_as_uint(v); const auto r = __builtin_amdgcn_permlane32_swap(u, u, false, false); return __uint_as_float(r[0]) + __uint_as_float(r[1]); }
DI void fence() { asm volatile("" ::: "memory"); }
DI void bar() { asm volatile("s_waitcnt lgkmcnt(0)" ::: "memory"); __builtin_amdgcn_s_barrier(); asm volatile("" ::: "memory"); }
template <int N> DI void vmw() { asm volatile("s_waitcnt vmcnt(%0)" :: "n"(N) : "memory"); }
template <bool S1> DI void glds16(u32 voff, const void* sbase, u32 ldst) {
    u32 keep;
    if (S1) asm volatile("s_mov_b32 %0, m0\n\ts_mov_b32 m0, %3\n\ts_nop 0\n\tglobal_load_lds_dwordx4 %1, %2 sc1\n\ts_mov_b32 m0, %0" : "=&s"(keep) : "v"(voff), "s"(sbase), "s"(ldst) : "memory");
    else    asm volatile("s_mov_b32 %0, m0\n\ts_mov_b32 m0, %3\n\ts_nop 0\n\tglobal_load_lds_dwordx4 %1, %2\n\ts_mov_b32 m0, %0" : "=&s"(keep) : "v"(voff), "s"(sbase), "s"(ldst) : "memory");
}
DI void glds16v(const void* gaddr, u32 ldst) {
    u32 keep;
    asm volatile("s_mov_b32 %0, m0\n\ts_mov_b32 m0, %2\n\ts_nop 0\n\tglobal_load_lds_dwordx4 %1, off\n\ts_mov_b32 m0, %0" : "=&s"(keep) : "v"(gaddr), "s"(ldst) : "memory");
}
DI float dot2(u32 a, u32 b, float c) { return __builtin_amdgcn_fdot2_f32_bf16(__builtin_bit_cast(bf16x2, a), __builtin_bit_cast(bf16x2, b), c, false); }
DI f2 cmul(f2 a, f2 b) { f2 t = f2{a.x, a.x} * b; return __builtin_elementwise_fma(f2{a.y, a.y}, f2{-b.y, b.x}, t); }
DI f2 cexp_rev(float ar, float ai, float t) {
    const float e = __builtin_amdgcn_exp2f(ar * t), ph = __builtin_amdgcn_fractf(ai * t);
    return f2{e * __builtin_amdgcn_cosf(ph), e * __builtin_amdgcn_sinf(ph)};
}
DI u32 swz_b(u32 row) { return ((row & 3u) << 2) | ((row >> 2) & 3u); }
DI u32 off_b(u32 row, u32 ch) { return 256u * row + 16u * (ch ^ swz_b(row)); }
DI u32 off_a(u32 row, u32 ch) { return 2048u * (row >> 3) + 512u * (ch >> 2) + 64u * (row & 7u) + 16u * ((ch & 3u) ^ ((row >> 2) & 3u)); }
DI u32 max4(v4u v) { return max(max(v.x, v.y), max(v.z, v.w)); }
DI u32 san(u32 v) { return min(v, 0xfffffffeu); }
DI u32 ln_pack(float mean, float var) {
    const float m = rbf(mean), sd = rbf(__builtin_amdgcn_sqrtf(rbf(rbf(var) + 1e-5f)));
    return (__float_as_uint(m) >> 16) | (__float_as_uint(sd) & 0xffff0000u);
}

#define STAMP(i) do { } while (0)
#define CSTAMP(i) do { } while (0)
#define STAMP_DECL
#define STAMP_FLUSH do { } while (0)
#define SGB(mask, n) __builtin_amdgcn_sched_group_barrier(mask, n, 0)
#define SB0() __builtin_amdgcn_sched_barrier(0)
constexpr int M_MFMA = 0x8, M_VMEM = 0x10, M_DSR = 0x100, M_VALU = 0x2;
DI void delay_ticks(u32 n) { const u64 t0 = __builtin_amdgcn_s_memrealtime(); while ((u32)(__builtin_amdgcn_s_memrealtime() - t0) < n) __builtin_amdgcn_s_sleep(1); }
DI u32 opaque(u32 v) { asm volatile("" : "+v"(v)); return v; }

template <int TG> struct Tiles;
template <> struct Tiles<0> { static constexpr int n = 3; static constexpr int t[5] = {7, 6, 2, 0, 0}; };
template <> struct Tiles<1> { static constexpr int n = 5; static constexpr int t[5] = {5, 4, 3, 1, 0}; };

template <int TG> DI void conv_wave(int tid, int w, int lane, int cm, float dsv, __amdgpu_buffer_rsrc_t rsW, v4u (&Wf)[28], u32 wf_off) {
    typedef Tiles<TG> T;
    constexpr int NP = T::t[0] + 1;
    const int ch = w >> 2, rh = (w >> 1) & 1;
    f32x4 acc[T::n];
#pragma unroll
    for (int i = 0; i < T::n; ++i) acc[i] = f32x4{0.f, 0.f, 0.f, 0.f};
    const u32 cb = lane & 15, cq = lane >> 4, cs = (3u - cb) & 3u;
    const u32 kbase = opaque(KC + ch * KC_CH + cs * KC_COPY + 2 * (2047 - 16 * (cq >> 1) - cb + 8 * (cq & 1) - cs) - 32 * 8 * rh - 32 * 118);
    const u32 kbase8 = opaque(kbase + 8);
    const u32 ubase = opaque(UU + ch * UU_CH + 512 + 32 * (cb - (cq >> 1)) + 16 * (cq & 1) - 32 * 8 * rh - 32 * 6);
    v4u Af[2][NP], Bf[2][NP];
    auto ld = [&](int rr, v4u (&A)[NP], v4u (&B)[NP]) {
#pragma unroll
        for (int p = 0; p < NP; ++p) {
            const v2u lo = *lds<volatile v2u>(kbase + 32 * (118 - (16 * p + 2 * rr))), hi = *lds<volatile v2u>(kbase8 + 32 * (118 - (16 * p + 2 * rr)));
            A[p] = v4u{lo.x, lo.y, hi.x, hi.y};
            B[p] = *lds<v4u>(ubase + 32 * (16 * p - 2 * rr + 6));
        }
    };
    ld(0, Af[0], Bf[0]);
#pragma unroll
    for (int rr = 0; rr < 4; ++rr) {
        SB0();
        if (rr < 3) ld(rr + 1, Af[(rr + 1) & 1], Bf[(rr + 1) & 1]);
#pragma unroll
        for (int i = 0; i < 3; ++i) Wf[3 * rr + i] = __builtin_amdgcn_raw_buffer_load_b128(rsW, wf_off + (3 * rr + i) * 1024, 0, WAUX);
#pragma unroll
        for (int p = 0; p < NP; ++p)
#pragma unroll
            for (int i = 0; i < T::n; ++i)
                if (p <= T::t[i]) acc[i] = mfma16(Af[rr & 1][p], Bf[rr & 1][T::t[i] - p], acc[i]);
#pragma unroll
        for (int i = 0; i < 18; ++i) {
            SGB(M_MFMA, 1);
            if (rr < 3) { if (NP == 8 && i < 6) SGB(M_DSR, 2); else SGB(M_DSR, 1); }
            if (i == 3 || i == 9 || i == 15) SGB(M_VMEM, 1);
        }
    }
    SB0();
    CSTAMP(30);
    const u32 mine = CX + w * 3072, other = CX + (w ^ 2) * 3072;
    if (rh == 0) {
#pragma unroll
        for (int i = 1; i < T::n; i += 2) *lds<f32x4>(mine + (i >> 1) * 1024 + lane * 16) = acc[i];
    } else {
#pragma unroll
        for (int i = 0; i < T::n; i += 2) *lds<f32x4>(mine + (i >> 1) * 1024 + lane * 16) = acc[i];
    }
    bar();
    CSTAMP(31);
    auto fin = [&](int i, f32x4 own) {
        const f32x4 y = own + *lds<f32x4>(other + (i >> 1) * 1024 + lane * 16);
        const u32 l0 = 256 * T::t[i] + 16 * cb + 4 * cq;
        const v2u uu = *lds<v2u>(UU + ch * UU_CH + 512 + l0 * 2), xx = *lds<v2u>(XS + ch * 4096 + l0 * 2);
        const float uv[4] = {bflo(uu.x), bfhi(uu.x), bflo(uu.y), bfhi(uu.y)}, xv[4] = {bflo(xx.x), bfhi(xx.x), bflo(xx.y), bfhi(xx.y)};
        float h[4];
#pragma unroll
        for (int r = 0; r < 4; ++r) h[r] = xv[r] + rbf(rbf(y[r]) + rbf(uv[r] * dsv));
        const v2u o2 = {pk2(h[0], h[1]), pk2(h[2], h[3])};
        __builtin_amdgcn_raw_buffer_store_b64(o2, rsW, WS_HT + (2 * cm + ch) * 4096 + l0 * 2, 0, SC1);
    };
    if (rh == 0) {
#pragma unroll
        for (int i = 0; i < T::n; i += 2) fin(i, acc[i]);
    } else {
#pragma unroll
        for (int i = 1; i < T::n; i += 2) fin(i, acc[i]);
    }
}

__global__ void __launch_bounds__(512, 2)
fused_kernel(const u16* __restrict__ x, const u16* __restrict__ Are, const u16* __restrict__ Aim, const u16* __restrict__ Bre, const u16* __restrict__ Bim,
             const u16* __restrict__ Cre, const u16* __restrict__ Cim, const u16* __restrict__ logdt, const u16* __restrict__ Dskip,
             const u16* __restrict__ wg, const u16* __restrict__ wu, const u16* __restrict__ wd, u16* __restrict__ out, char* __restrict__ ws)
{
    const int tid = threadIdx.x, lane = tid & 63, w = __builtin_amdgcn_readfirstlane(tid >> 6);
    const int b = blockIdx.x, g = b & 7, j = b >> 3;
    const bool mainb = j < 28;
    const int cm = mainb ? g * 28 + j : 0;
    const int P = 4 * (g >> 1) + j / 7, S = 7 * (g & 1) + j % 7;
    const __amdgpu_buffer_rsrc_t rsW = __builtin_amdgcn_make_buffer_rsrc(ws, 0, WS_END, 0x00020000);
    const __amdgpu_buffer_rsrc_t rsX = __builtin_amdgcn_make_buffer_rsrc((void*)x, 0, L_ * D_ * 2, 0x00020000);
    const u32 trq = (lane & 15) >> 2, trp = lane & 3, trh = lane >> 5, trb = (lane >> 4) & 1;
    STAMP_DECL
    STAMP(0);
    const u64 tstart = __builtin_amdgcn_s_memrealtime();
    v4u xr; u64 ep; u32 p_ld2, p_dk2;
    asm volatile("global_load_dwordx4 %0, %1, %2" : "=v"(xr) : "v"((u32)((8 * b + w) * 896 + min(lane, 55) * 16)), "s"(x) : "memory");
    asm volatile("s_load_dwordx2 %0, %3, 0x0\n\ts_load_dword %1, %4, 0x0\n\ts_load_dword %2, %5, 0x0" : "=&s"(ep), "=&s"(p_ld2), "=&s"(p_dk2)
                 : "s"(ws + WS_EP), "s"((const char*)logdt + cm * 4), "s"((const char*)Dskip + cm * 4) : "memory");
    const int ch = w >> 2, dch = 2 * cm + ch;
    if (mainb) {
        const int ai_ = lane >> 3;
        const u16* src = ai_ == 0 ? Are : ai_ == 1 ? Aim : ai_ == 2 ? Bre : ai_ == 3 ? Bim : ai_ == 4 ? Cre : Cim;
        glds16v(src + dch * N_ + (w & 3) * 64 + (lane & 7) * 8, PRM + w * 1024);
        const int ew = w < 6 ? w : w - 6;
        glds16<false>((u32)(ew * 1024 + lane * 16), (const void*)&EO, EOL + ew * 1024);
        const int bb_ = b < 196 ? b : b - 196;
#pragma unroll
        for (int i = 0; i < 2; ++i) {
            const int id = 2 * bb_ + i, Sp = id / 28, s = id % 28;
            const u32 row = (w & 3) * 4 + (lane >> 4), chk = (lane & 15) ^ swz_b(row);
            glds16<false>((u32)((16 * s + row) * (F_ * 2) + Sp * 256 + chk * 16), (w >> 2) ? (const void*)wu : (const void*)wg, RT + i * 8192 + w * 1024);
        }
    } else {
        const int hb = (j - 28) * 8 + g;
#pragma unroll
        for (int k = 0; k < 7; ++k) {
            const int gid = min(hb + 32 * k, 195);
            const int dt_ = gid / 28, fg = gid % 28;
            const u32 row = 8 * w + (lane >> 3), chk = (lane & 7) ^ (((row >> 1) & 1) << 2);
            glds16<false>((u32)((64 * fg + row) * (D_ * 2) + dt_ * 128 + chk * 16), (const void*)wd, k * 8192 + w * 1024);
        }
    }
    asm volatile("s_cmp_ge_i32 %1, 28\n\ts_cbranch_scc1 1f\n\ts_waitcnt vmcnt(4)\n\ts_branch 2f\n1:\n\ts_waitcnt vmcnt(7)\n2:" : "+v"(xr) : "s"(__builtin_amdgcn_readfirstlane(j)) : "memory", "scc");
    asm volatile("s_waitcnt lgkmcnt(0)" : "+s"(ep), "+s"(p_ld2), "+s"(p_dk2) :: "memory");
    ep += 1ull;
    const u32 p_ldt = ch ? p_ld2 >> 16 : p_ld2 & 0xffffu, p_dsk = ch ? p_dk2 >> 16 : p_dk2 & 0xffffu;
    if (lane >= 56) xr = v4u{0, 0, 0, 0};
    const u32 tag = (u32)ep, tagx = (u32)(ep >> 32) << 16;
    if (lane < 56) *lds<v4u>(XT + w * 912 + lane * 16) = xr;
    {
        const float v[8] = {bflo(xr.x), bfhi(xr.x), bflo(xr.y), bfhi(xr.y), bflo(xr.z), bfhi(xr.z), bflo(xr.w), bfhi(xr.w)};
        const float mean = wave_sum(((v[0] + v[1]) + (v[2] + v[3])) + ((v[4] + v[5]) + (v[6] + v[7]))) * (1.f / 448.f);
        float q = 0.f;
#pragma unroll
        for (int e = 0; e < 8; ++e) { const float dl = v[e] - mean; q += dl * dl; }
        q = lane < 56 ? q : 0.f;
        const u32 ln1 = ln_pack(mean, wave_sum(q) * (1.f / 448.f));
        if (lane == 0) __hip_atomic_store((GAS u64*)(ws + WS_SG) + (8 * b + w), ((u64)tag << 32) | ln1, RLX_AGENT);
    }
    STAMP(1);
    const u32 km0 = FM + ch * FM_CH + (w & 3) * 256;
    f2 Ga, Gb, Gc, Gd, KX, KY, G4, G4r, E1, E1r, E256, E256r, E2, E2r, E4, E4r;
#define M1(a, bb) (f2{(a).x, (a).x} * (bb))
#define M2(a, br, t) __builtin_elementwise_fma(f2{(a).y, (a).y}, (br), (t))
#define WROW1(r, v) do { u32 keep_; asm volatile("s_mov_b32 %0, m0\n\ts_mov_b32 m0, %2\n\ts_nop 0\n\tds_write_addtid_b32 %1 offset:%3\n\ts_mov_b32 m0, %0" : "=&s"(keep_) : "v"(v), "s"(km0), "n"((r) * FM_ROW) : "memory"); } while (0)
#define WROW6(r0, v0, r1, v1, r2, v2, r3, v3, r4, v4, r5, v5) do { u32 keep_; asm volatile("s_mov_b32 %0, m0\n\ts_mov_b32 m0, %7\n\ts_nop 0\n\t" \
        "ds_write_addtid_b32 %1 offset:%8\n\tds_write_addtid_b32 %2 offset:%9\n\tds_write_addtid_b32 %3 offset:%10\n\tds_write_addtid_b32 %4 offset:%11\n\tds_write_addtid_b32 %5 offset:%12\n\tds_write_addtid_b32 %6 offset:%13\n\ts_mov_b32 m0, %0" \
        : "=&s"(keep_) : "v"(v0), "v"(v1), "v"(v2), "v"(v3), "v"(v4), "v"(v5), "s"(km0), "n"((r0) * FM_ROW), "n"((r1) * FM_ROW), "n"((r2) * FM_ROW), "n"((r3) * FM_ROW), "n"((r4) * FM_ROW), "n"((r5) * FM_ROW) : "memory"); } while (0)
#define WA(r, X) WROW1(r, pk2((X).x, -(X).y))
#define KSTEP(t) do { SB0(); \
        const f2 ta = M1(Ga, G4), tb = M1(Gb, G4), tc = M1(Gc, G4), td_ = M1(Gd, G4), tx = M1(KX, E1), ty = M1(KY, E256); \
        const u32 c0 = pk2(Ga.x, Ga.y), c1 = pk2(Gb.x, Gb.y), c2 = pk2(Gc.x, Gc.y), c3 = pk2(Gd.x, Gd.y), c4 = pk2(KX.x, -KX.y), c5 = pk2(KY.x, -KY.y); \
        f2 ts0, ts1; \
        if ((t) == 0) ts0 = M1(KY, E2); \
        if ((t) == 1 || (t) == 2) ts0 = M1(KY, E4); \
        SB0(); \
        WROW6(21 + 4 * (t), c0, 22 + 4 * (t), c1, 23 + 4 * (t), c2, 24 + 4 * (t), c3, (t), c4, ((t) == 0 ? 8 : (t) == 1 ? 12 : (t) == 2 ? 14 : 13 + (t)), c5); \
        if ((t) == 0) { f2 Z = M2(KY, E2r, ts0); WA(9, Z); ts1 = M1(Z, E2); Z = M2(Z, E2r, ts1); WA(10, Z); ts1 = M1(Z, E2); Z = M2(Z, E2r, ts1); WA(11, Z); } \
        if ((t) == 1) { const f2 V = M2(KY, E4r, ts0); WA(13, V); } \
        if ((t) == 2) { const f2 V = M2(KY, E4r, ts0); WA(15, V); } \
        if ((t) < 7) { Ga = M2(Ga, G4r, ta); Gb = M2(Gb, G4r, tb); Gc = M2(Gc, G4r, tc); Gd = M2(Gd, G4r, td_); KX = M2(KX, E1r, tx); KY = M2(KY, E256r, ty); } \
    } while (0)
    if (mainb) {
        vmw<4>();
        const u32 pa_ = PRM + w * 1024 + lane * 2;
        const u32 p_are = *lds<u16>(pa_), p_aim = *lds<u16>(pa_ + 128), p_bre = *lds<u16>(pa_ + 256), p_bim = *lds<u16>(pa_ + 384), p_cre = *lds<u16>(pa_ + 512), p_cim = *lds<u16>(pa_ + 640);
        const float dt = rbf(expf(bflo(p_ldt)));
        const float zr = dt * bflo(p_are), zi = dt * bflo(p_aim);
        const float ar = zr * 1.4426950408889634f, ai = zi * 0.15915494309189535f;
        const f2 CB = cmul(f2{bflo(p_cre), bflo(p_cim)}, f2{dt * bflo(p_bre), dt * bflo(p_bim)});
        E1 = cexp_rev(ar, ai, 1.f); E256 = cexp_rev(ar, ai, 256.f);
        const f2 G1 = cexp_rev(ar, ai, 8.f);
        E2 = cmul(E1, E1); E4 = cmul(E2, E2);
        const f2 G2 = cmul(G1, G1);
        G4 = cmul(G2, G2);
        E1r = f2{-E1.y, E1.x}; E2r = f2{-E2.y, E2.x}; E4r = f2{-E4.y, E4.x}; E256r = f2{-E256.y, E256.x}; G4r = f2{-G4.y, G4.x};
        Ga = f2{1.f, 0.f}; Gb = G1; Gc = G2; Gd = cmul(G2, G1);
        KX = CB; KY = cmul(CB, E256);
        SB0();
    }
    bar();
    STAMP(16);
    auto publish_xg = [&]() {
        u32 x0[4], x1[4];
#pragma unroll
        for (int k = 0; k < 4; ++k) {
            const int id = min(tid + 512 * k, 1791), c = id >> 2, i2 = (id & 3) * 2;
            x0[k] = *lds<u16>(XT + i2 * 912 + c * 2); x1[k] = *lds<u16>(XT + (i2 + 1) * 912 + c * 2);
        }
        SB0();
#pragma unroll
        for (int k = 0; k < 4; ++k)
            __builtin_amdgcn_raw_buffer_store_b128(v4u{x0[k] | tagx, tag, x1[k] | tagx, tag}, rsW, (tid + 512 * k < 1792) ? (u32)(WS_XG + b * 28672 + (tid + 512 * k) * 16) : 0x7ffffff0u, 0, SC1);
    };
    if (mainb) {
    publish_xg();
    STAMP(17);
    KSTEP(0); KSTEP(1); KSTEP(2);
    SB0();
    vmw<5>();
    bar();
    if (b < 196) {
#pragma unroll
        for (int i = 0; i < 2; ++i) {
            const int id = 2 * b + i, Sp = id / 28, s = id % 28;
            const u32 base = RT + i * 8192 + trb * 4096 + 8 * (trp & 1);
            const v4u f = tr_frag(base + off_b(8 * trh + trq, 2 * w + (trp >> 1)), base + off_b(8 * trh + 4 + trq, 2 * w + (trp >> 1)));
            __builtin_amdgcn_raw_buffer_store_b128(f, rsW, WS_WG + ((Sp * 8 + w) * 28 + s) * 1024 + lane * 16, 0, SC1);
        }
    }
    fence();
    STAMP(2);
    {
        KSTEP(3); KSTEP(4); KSTEP(5); KSTEP(6); KSTEP(7);
        SB0();
#undef KSTEP
#undef WA
#undef WROW1
#undef WROW6
#undef M1
#undef M2
        STAMP(3);
        vmw<0>();
        bar();
        if (tid == 0 && b < 196) __hip_atomic_store((GAS u32*)(ws + WS_WR + b * 64), tag, RLX_AGENT);
        const int ta_ = (w >> 1) & 1, tb_ = w & 1;
        const u32 fa_ = opaque(FM + ch * FM_CH + (16 * ta_ + (lane & 15)) * FM_ROW + (lane >> 4) * 16), fb_ = opaque(FM + ch * FM_CH + (21 + 16 * tb_ + (lane & 15)) * FM_ROW + (lane >> 4) * 16);
        f32x4 f0 = {0.f, 0.f, 0.f, 0.f}, f1 = {0.f, 0.f, 0.f, 0.f};
        v4u A[16], B[16];
#pragma unroll
        for (int ks = 0; ks < 16; ++ks) { A[ks] = *lds<v4u>(fa_ + ks * 64); B[ks] = *lds<v4u>(fb_ + ks * 64); }
        SB0();
#pragma unroll
        for (int ks = 0; ks < 16; ks += 2) { f0 = mfma16(A[ks], B[ks], f0); f1 = mfma16(A[ks + 1], B[ks + 1], f1); }
        f0 += f1;
#pragma unroll
        for (int r = 0; r < 4; ++r) { const int row = 16 * ta_ + 4 * (lane >> 4) + r; if (row < 24) *lds<float>(FT + ch * FT_CH + (row * 32 + 16 * tb_ + (lane & 15)) * 4) = f0[r]; }
        if ((tid & 255) == 255) *lds<float>(FT + ch * FT_CH + 768 * 4) = 0.f;
    }
    STAMP(4);
    {
        v4u xg[4], sg[4];
        const u32 fa = WS_WR + (tid < 196 ? tid : 0) * 64;
        const u32 xa = WS_XG + (32 * w + (lane >> 3)) * 28672 + cm * 128 + (lane & 7) * 16, sa = WS_SG + (32 * w + (lane >> 3)) * 64 + (lane & 3) * 16;
        if (DLY1) { while ((u32)(__builtin_amdgcn_s_memrealtime() - tstart) < DLY1) __builtin_amdgcn_s_sleep(1); }
        for (u32 spin = 0;; ++spin) {
#pragma unroll
            for (int k = 0; k < 4; ++k) { xg[k] = __builtin_amdgcn_raw_buffer_load_b128(rsW, xa + k * 8 * 28672, 0, SC1); sg[k] = __builtin_amdgcn_raw_buffer_load_b128(rsW, sa + k * 512, 0, SC1); }
            const u32 fl = __builtin_amdgcn_raw_buffer_load_b32(rsW, fa, 0, SC1);
            bool ok = fl == tag;
#pragma unroll
            for (int k = 0; k < 4; ++k) ok = ok && sg[k].y == tag && sg[k].w == tag && xg[k].y == tag && xg[k].w == tag && (xg[k].x & 0xffff0000u) == tagx && (xg[k].z & 0xffff0000u) == tagx;
            if (__all(ok) || spin > SPIN_CAP) break;
            __builtin_amdgcn_s_sleep(8);
        }
        STAMP(5);
        const int c = (lane >> 2) & 1;
#pragma unroll
        for (int k = 0; k < 4; ++k) {
            const u32 l0 = 8 * (32 * w + 8 * k + (lane >> 3)) + 2 * (lane & 3);
            const float u0 = rbf(bflo(xg[k].x) - bflo(sg[k].x)) * __builtin_amdgcn_rcpf(bfhi(sg[k].x));
            const float u1 = rbf(bflo(xg[k].z) - bflo(sg[k].z)) * __builtin_amdgcn_rcpf(bfhi(sg[k].z));
            *lds<u32>(UU + c * UU_CH + 512 + l0 * 2) = pk2(u0, u1);
            *lds<u32>(XS + c * 4096 + l0 * 2) = (xg[k].x & 0xffffu) | (xg[k].z << 16);
        }
    }
    if (tid >= 64 && tid < 128) *lds<v4u>(UU + ((tid >> 5) & 1) * UU_CH + (tid & 31) * 16) = v4u{0, 0, 0, 0};
    bar();
    STAMP(18);
    {
        const v2u ea = *lds<v2u>(EOL + (tid & 255) * 24), eb = *lds<v2u>(EOL + (tid & 255) * 24 + 8), ec = *lds<v2u>(EOL + (tid & 255) * 24 + 16);
        const v4u eo0 = {ea.x, ea.y, eb.x, eb.y}; const v2u eo1 = ec;
        const u32 eo[11] = {eo0.x & 0xffffu, eo0.x >> 16, eo0.y & 0xffffu, eo0.y >> 16, eo0.z & 0xffffu, eo0.z >> 16, eo0.w & 0xffffu, eo0.w >> 16, eo1.x & 0xffffu, eo1.x >> 16, eo1.y & 0xffffu};
        float v[11];
#pragma unroll
        for (int e = 0; e < 11; ++e) v[e] = *lds<float>(FT + ch * FT_CH + eo[e]);
        SB0();
        u32 p[10];
#pragma unroll
        for (int e = 0; e < 10; ++e) p[e] = pk2(v[e], v[e + 1]);
#pragma unroll
        for (int s = 0; s < 4; ++s) *lds<v4u>(KC + ch * KC_CH + s * KC_COPY + (tid & 255) * 16) = v4u{p[s], p[s + 2], p[s + 4], p[s + 6]};
    }
    if (tid < 16) *lds<v4u>(KC + (tid >> 3) * KC_CH + ((tid >> 1) & 3) * KC_COPY + 4096 + (tid & 1) * 16) = v4u{0, 0, 0, 0};
    bar();
    STAMP(6);
    CSTAMP(26);
    v4u Wf[28];
    const u32 wf_off = WS_WG + (S * 8 + w) * 28 * 1024 + lane * 16;
    {

        if (w & 1) conv_wave<1>(tid, w, lane, cm, bflo(p_dsk), rsW, Wf, wf_off); else conv_wave<0>(tid, w, lane, cm, bflo(p_dsk), rsW, Wf, wf_off);
    }
    STAMP(7);
    CSTAMP(27);
    const int l0p = 128 * P;
    vmw<0>();
    bar();
    {
        if (tid < 8) __hip_atomic_store((GAS u32*)(ws + WS_F2 + tid * 16384 + cm * 64), tag, RLX_AGENT);
        fence();
#pragma unroll
        for (int i = 12; i < 28; ++i) Wf[i] = __builtin_amdgcn_raw_buffer_load_b128(rsW, wf_off + i * 1024, 0, WAUX);
        fence();
        delay_ticks(DLY2);
        if (w < 4) {
            const u32 pa = tid < 224 ? (u32)(WS_F2 + g * 16384 + tid * 64) : (u32)(WS_WF + (tid - 224) * 64);
            for (u32 spin = 0;; ++spin) {
                const u32 s0 = __builtin_amdgcn_raw_buffer_load_b32(rsW, pa, 0, SC1);
                if (__all(s0 == tag) || spin > SPIN_CAP) break;
                __builtin_amdgcn_s_sleep(8);
            }
        }
    }
    bar();
    STAMP(8);
    if (b == 0 && tid == 0) __hip_atomic_store((GAS u64*)(ws + WS_EP), ep, RLX_AGENT);
    const u32 bt_src = (8 * (w >> 1) + ((lane >> 2) & 7)) * 4096 + l0p * 2 + (4 * (2 * (w & 1) + (lane >> 5)) + ((lane & 3) ^ ((2 * (w >> 1) + ((lane >> 4) & 1)) & 3))) * 16;
    const u32 bt_dst = BT + (w >> 1) * 2048 + (w & 1) * 1024;
    auto bt_piece = [&](int k) { glds16<BTSC1>(bt_src + k * 32 * 4096, ws + WS_HT, bt_dst + k * 8192); };
    const float csum = __uint_as_float(__builtin_amdgcn_raw_buffer_load_b32(rsW, WS_CS + (((lane >> 4) & 1) * 1792 + 128 * S + 16 * w + (lane & 15)) * 4, 0, SC1));
    fence();
#pragma unroll
    for (int k = 0; k < 6; ++k) bt_piece(k);
    vmw<4>();
    bar();
    STAMP(9);
    CSTAMP(24);
    f32x16 acc[4];
    v4u Wd[28];
    const int td = w & 1, kq = w >> 1, hp = S / 7, dtile = S % 7;
    const u32 wd_off = WS_WD + ((dtile * 2 + td) * 112 + 28 * kq) * 1024 + lane * 16;
    u32 bb[2];
#pragma unroll
    for (int tt = 0; tt < 2; ++tt) bb[tt] = opaque(BT + 2048 * trh + 64 * (4 * tt + trq) + 16 * ((2 * trb + (trp >> 1)) ^ ((2 * trh + tt) & 3)) + 8 * (trp & 1));
    {
#pragma unroll
        for (int t = 0; t < 4; ++t)
#pragma unroll
            for (int i = 0; i < 16; ++i) acc[t][i] = 0.f;
        v4u Bf[2][4], Of;
        float sa0 = 0.f, sa1 = 0.f, sq0 = 0.f, sq1 = 0.f;
        const u32 bh0 = opaque(bb[0] + 65536), bh1 = opaque(bb[1] + 65536);
        const u32 so0 = opaque(bb[0] + (w >> 2) * 4096 + (w & 3) * 512), so1 = opaque(bb[1] + (w >> 2) * 4096 + (w & 3) * 512);
        const u32 sh0 = opaque(so0 + 65536), sh1 = opaque(so1 + 65536);
#pragma unroll
        for (int t = 0; t < 4; ++t) Bf[0][t] = tr_frag(bb[0] + 512 * t, bb[1] + 512 * t);
#pragma unroll
        for (int s = 0; s < 28; ++s) {
            SB0();
            if (s == 3) { vmw<6>(); bar(); }
            if (s == 11) { vmw<7>(); bar(); }
            SB0();
            if (s + 1 < 28) {
#pragma unroll
                for (int t = 0; t < 4; ++t)
                    Bf[(s + 1) & 1][t] = (s + 1 < 16) ? tr_frag(bb[0] + (s + 1) * 4096 + 512 * t, bb[1] + (s + 1) * 4096 + 512 * t)
                                                       : tr_frag(bh0 + (s + 1 - 16) * 4096 + 512 * t, bh1 + (s + 1 - 16) * 4096 + 512 * t);
            }
            if ((s & 1) == 0) Of = (s < 16) ? tr_frag(so0 + (s >> 1) * 8192, so1 + (s >> 1) * 8192) : tr_frag(sh0 + ((s - 16) >> 1) * 8192, sh1 + ((s - 16) >> 1) * 8192);
            if (s < 4) { bt_piece(6 + 2 * s); bt_piece(7 + 2 * s); }
            else Wd[s - 4] = __builtin_amdgcn_raw_buffer_load_b128(rsW, wd_off + (s - 4) * 1024, 0, WAUX);
#pragma unroll
            for (int t = 0; t < 4; ++t) acc[t] = mfma32(Wf[s], Bf[s & 1][t], acc[t]);
            if (s & 1) {
#pragma unroll
                for (int e = 0; e < 4; e += 2) {
                    const float l0 = bflo(Of[e]), h0 = bfhi(Of[e]), l1 = bflo(Of[e + 1]), h1 = bfhi(Of[e + 1]);
                    sa0 += l0; sq0 = __builtin_fmaf(l0, l0, sq0); sa1 += l1; sq1 = __builtin_fmaf(l1, l1, sq1);
                    sa0 += h0; sq0 = __builtin_fmaf(h0, h0, sq0); sa1 += h1; sq1 = __builtin_fmaf(h1, h1, sq1);
                }
            }
            if (s >= 4) {
                if (s & 1) { SGB(M_MFMA, 1); SGB(M_DSR, 2); SGB(M_VALU, 6); SGB(M_MFMA, 1); SGB(M_DSR, 2); SGB(M_VALU, 6); SGB(M_VMEM, 1); SGB(M_MFMA, 1); SGB(M_DSR, 2); SGB(M_VALU, 6); SGB(M_MFMA, 1); SGB(M_DSR, 2); SGB(M_VALU, 6); }
                else { SGB(M_MFMA, 1); SGB(M_DSR, 3); SGB(M_MFMA, 1); SGB(M_DSR, 3); SGB(M_VMEM, 1); SGB(M_MFMA, 1); SGB(M_DSR, 2); SGB(M_MFMA, 1); SGB(M_DSR, 2); }
            }
        }
        float sumh = sa0 + sa1, sq = sq0 + sq1;

        SB0();
    #pragma unroll
        for (int s = 24; s < 28; ++s) Wd[s] = __builtin_amdgcn_raw_buffer_load_b128(rsW, wd_off + s * 1024, 0, WAUX);

        sumh = half_sum(sumh); sq = half_sum(sq);
        if (lane < 32) { *lds<float>(SP + w * 128 + lane * 4) = sumh; *lds<float>(SQ + w * 128 + lane * 4) = sq; *lds<float>(CSL + w * 128 + lane * 4) = csum; }
    }
    STAMP(10);
    CSTAMP(25);
    bar();
    {
        f32x4 csg[2], csu[2];
#pragma unroll
        for (int q = 0; q < 2; ++q) { csg[q] = *lds<f32x4>(CSL + w * 128 + (8 * q + 4 * trh) * 4); csu[q] = *lds<f32x4>(CSL + w * 128 + 64 + (8 * q + 4 * trh) * 4); }
#pragma unroll
        for (int t = 0; t < 4; ++t) {
            const float m = rbf((*lds<float>(SP + t * 128 + (lane & 31) * 4) + *lds<float>(SP + (t + 4) * 128 + (lane & 31) * 4)) * (1.f / 448.f));
            float hv[8];
#pragma unroll
            for (int e = 0; e < 8; ++e) hv[e] = (acc[t][e] - m * csg[e >> 2][e & 3]) * (acc[t][e + 8] - m * csu[e >> 2][e & 3]);
            const v4u o = {pk2(hv[0], hv[1]), pk2(hv[2], hv[3]), pk2(hv[4], hv[5]), pk2(hv[6], hv[7])};
            __builtin_amdgcn_raw_buffer_store_b128(o, rsW, WS_HH + ((P * 112 + 8 * S + w) * 4 + t) * 1024 + lane * 16, 0, SC1);
        }
    }
    STAMP(11);
    const int tl_f = kq >> 1;
    const u32 lp = 64 * hp + 32 * tl_f + (lane & 31);
    float hres[8], rs;
    {
        const int tq = 2 * hp + tl_f;
        const float s1 = *lds<float>(SP + tq * 128 + (lane & 31) * 4) + *lds<float>(SP + (tq + 4) * 128 + (lane & 31) * 4);
        const float s2 = *lds<float>(SQ + tq * 128 + (lane & 31) * 4) + *lds<float>(SQ + (tq + 4) * 128 + (lane & 31) * 4);
        const float mean = s1 * (1.f / 448.f), var = fmaxf(s2 * (1.f / 448.f) - mean * mean, 0.f);
        const float sd = bfhi(ln_pack(mean, var));
        rs = 1.f / (sd * sd);
#pragma unroll
        for (int e = 0; e < 8; ++e) {
            const u32 row = 64 * dtile + 32 * td + 16 * (kq & 1) + 8 * (e >> 2) + 4 * trh + (e & 3);
            hres[e] = bflo(*lds<u16>(BT + off_a(row, lp >> 3) + (lp & 7) * 2));
        }
    }
    vmw<0>();
    bar();
    if (tid == 0) __hip_atomic_store((GAS u32*)(ws + WS_F3 + (P * 16 + S) * 64), tag, RLX_AGENT);
    const u32 hbase = WS_HH + ((P * 112 + 28 * kq) * 4 + 2 * hp) * 1024;
    if (w == 0) {
        delay_ticks(DLY3);
        const u32 pa = WS_F3 + (P * 16 + (lane < 14 ? lane : 0)) * 64;
        for (u32 spin = 0;; ++spin) {
            const u32 s0 = __builtin_amdgcn_raw_buffer_load_b32(rsW, pa, 0, SC1);
            if (__all(s0 == tag) || spin > SPIN_CAP) break;
            __builtin_amdgcn_s_sleep(8);
        }
    }
    bar();
    STAMP(12);
    const u32 hsrc = td * 1024 + lane * 16, hdst = RING + kq * 2048 + td * 1024, hrd = opaque(RING + kq * 2048 + lane * 16);
    auto hh_dma = [&](int s) { glds16<true>(hsrc + s * 4096, ws + hbase, hdst + (s % RING_SLOTS) * 8192); };
    f32x16 o2[2];
    {
#pragma unroll
        for (int i = 0; i < 16; ++i) { o2[0][i] = 0.f; o2[1][i] = 0.f; }
        constexpr int AH = 9;
#pragma unroll
        for (int s = 0; s < AH; ++s) hh_dma(s);
        v4u hf[2][2];
        vmw<AH - 4>(); bar();
        STAMP(13); CSTAMP(28);
#pragma unroll
        for (int tl = 0; tl < 2; ++tl) hf[0][tl] = *lds<v4u>(hrd + tl * 1024);
#pragma unroll
        for (int s = 0; s < 28; ++s) {
            SB0();
            if ((s & 3) == 3 && s + 1 < 28) {
                const int issued = (s - 1 + AH < 27) ? s - 1 + AH : 27, need = (s + 4 < 27) ? s + 4 : 27;
                switch (issued - need) { case 0: vmw<0>(); break; case 1: vmw<1>(); break; case 2: vmw<2>(); break; case 3: vmw<3>(); break; default: vmw<4>(); break; }
                bar();
                SB0();
            }
            if (s + 1 < 28) {
#pragma unroll
                for (int tl = 0; tl < 2; ++tl) hf[(s + 1) & 1][tl] = *lds<v4u>(hrd + ((s + 1) % RING_SLOTS) * 8192 + tl * 1024);
            }
            if (s + AH < 28) hh_dma(s + AH);
#pragma unroll
            for (int tl = 0; tl < 2; ++tl) o2[tl] = mfma32(Wd[s], hf[s & 1][tl], o2[tl]);
        }
        SB0();
        bar();
    }
    STAMP(14);
    CSTAMP(29);
    {
#pragma unroll
        for (int k2 = 0; k2 < 4; ++k2) {
            if (k2 != kq) {
                const int si = (kq > k2) ? kq - 1 : kq;
                const f32x16& a = o2[k2 >> 1];
#pragma unroll
                for (int q = 0; q < 2; ++q) { const int r0 = 8 * (k2 & 1) + 4 * q; *lds<f32x4>(((td * 4 + k2) * 3 + si) * 2048 + q * 1024 + lane * 16) = f32x4{a[r0], a[r0 + 1], a[r0 + 2], a[r0 + 3]}; }
            }
        }
        bar();
        f32x4 v[2];
#pragma unroll
        for (int q = 0; q < 2; ++q) {
            if (kq == 0) v[q] = f32x4{o2[0][4 * q], o2[0][4 * q + 1], o2[0][4 * q + 2], o2[0][4 * q + 3]};
            else if (kq == 1) v[q] = f32x4{o2[0][8 + 4 * q], o2[0][9 + 4 * q], o2[0][10 + 4 * q], o2[0][11 + 4 * q]};
            else if (kq == 2) v[q] = f32x4{o2[1][4 * q], o2[1][4 * q + 1], o2[1][4 * q + 2], o2[1][4 * q + 3]};
            else v[q] = f32x4{o2[1][8 + 4 * q], o2[1][9 + 4 * q], o2[1][10 + 4 * q], o2[1][11 + 4 * q]};
#pragma unroll
            for (int si = 0; si < 3; ++si) v[q] += *lds<f32x4>(((td * 4 + kq) * 3 + si) * 2048 + q * 1024 + lane * 16);
            const u32 d0 = 64 * dtile + 32 * td + 16 * (kq & 1) + 8 * q + 4 * trh;
            float r[4];
#pragma unroll
            for (int e = 0; e < 4; ++e) r[e] = hres[4 * q + e] + rbf(v[q][e] * rs);
            *(GAS v2u*)(out + (size_t)(l0p + lp) * D_ + d0) = v2u{pk2(r[0], r[1]), pk2(r[2], r[3])};
        }
    }
    STAMP(15);
    STAMP_FLUSH;
    } else {
        publish_xg();
        const int hb = (j - 28) * 8 + g;
        float cs[8];
        {
            const u16* wsrc = hb < 16 ? wg : wu;
            const int c0 = 112 * (hb & 15) + 8 * (tid % 14), r0 = tid / 14;
#pragma unroll
            for (int e = 0; e < 8; ++e) cs[e] = 0.f;
            v4u t[13];
#pragma unroll
            for (int i = 0; i < 13; ++i) { const int r = r0 + 36 * i; t[i] = (tid < 504 && r < 448) ? *(const v4u*)(wsrc + (size_t)r * F_ + c0) : v4u{0, 0, 0, 0}; }
#pragma unroll
            for (int i = 0; i < 13; ++i) { cs[0] += bflo(t[i].x); cs[1] += bfhi(t[i].x); cs[2] += bflo(t[i].y); cs[3] += bfhi(t[i].y); cs[4] += bflo(t[i].z); cs[5] += bfhi(t[i].z); cs[6] += bflo(t[i].w); cs[7] += bfhi(t[i].w); }
        }
        vmw<0>(); bar();
#pragma unroll
        for (int k = 0; k < 7; ++k) {
            const int gid = hb + 32 * k;
            if (gid < 196) {
                const int dt_ = gid / 28, fg = gid % 28, fsl = w >> 1, td = w & 1;
                u32 ad[2];
#pragma unroll
                for (int t = 0; t < 2; ++t) { const u32 row = 16 * fsl + 8 * t + 4 * trh + trq, chk = 4 * td + 2 * trb + (trp >> 1); ad[t] = k * 8192 + row * 128 + 16 * (chk ^ (((row >> 1) & 1) << 2)) + 8 * (trp & 1); }
                const v4u f = tr_frag(ad[0], ad[1]);
                __builtin_amdgcn_raw_buffer_store_b128(f, rsW, WS_WD + ((dt_ * 2 + td) * 112 + 4 * fg + fsl) * 1024 + lane * 16, 0, SC1);
            }
        }
        if (tid < 504) {
            *lds<f32x4>(65536 + tid * 32) = f32x4{cs[0], cs[1], cs[2], cs[3]};
            *lds<f32x4>(65536 + tid * 32 + 16) = f32x4{cs[4], cs[5], cs[6], cs[7]};
        }
        bar();
        if (tid < 112) {
            float s = 0.f;
            for (int r = 0; r < 36; ++r) s += *lds<float>(65536 + (r * 14 + (tid >> 3)) * 32 + (tid & 7) * 4);
            __hip_atomic_store((GAS u32*)(ws + WS_CS) + ((hb < 16 ? 0 : 1792) + 112 * (hb & 15) + tid), __float_as_uint(s), RLX_AGENT);
        }
        vmw<0>(); bar();
        if (tid == 0) __hip_atomic_store((GAS u32*)(ws + WS_WF + hb * 64), tag, RLX_AGENT);

    }
}

extern "C" void kernel_launch(void* const* d_in, const int* in_sizes, int n_in, void* d_out, int out_size, void* d_ws, size_t ws_size, hipStream_t stream) {
    (void)in_sizes; (void)n_in; (void)out_size; (void)ws_size;
    (void)hipFuncSetAttribute((const void*)fused_kernel, hipFuncAttributeMaxDynamicSharedMemorySize, LDS_BYTES);
    fused_kernel<<<256, 512, LDS_BYTES, stream>>>((const u16*)d_in[0], (const u16*)d_in[1], (const u16*)d_in[2], (const u16*)d_in[3], (const u16*)d_in[4],
        (const u16*)d_in[5], (const u16*)d_in[6], (const u16*)d_in[7], (const u16*)d_in[8], (const u16*)d_in[10], (const u16*)d_in[11], (const u16*)d_in[12],
        (u16*)d_out, (char*)d_ws);
}
```

```cpp
#include <hip/hip_runtime.h>
#include <stdint.h>

typedef unsigned short u16;
typedef unsigned u32;
typedef unsigned long long u64;
typedef u32 v4u __attribute__((ext_vector_type(4)));
typedef u32 v2u __attribute__((ext_vector_type(2)));
typedef float f32x4 __attribute__((ext_vector_type(4)));
typedef float f32x16 __attribute__((ext_vector_type(16)));
typedef float f2 __attribute__((ext_vector_type(2)));
typedef __bf16 bf16x8 __attribute__((ext_vector_type(8)));
typedef __bf16 bf16x2 __attribute__((ext_vector_type(2)));
typedef short s16x4 __attribute__((ext_vector_type(4)));
#define LAS __attribute__((address_space(3)))
#define GAS __attribute__((address_space(1)))
#define DI __device__ __forceinline__
#define RLX_AGENT __ATOMIC_RELAXED, __HIP_MEMORY_SCOPE_AGENT
#define CODEPF 28
#define SC1 16
#define BTSC1 true
#define DLY1 0
#define DLY2 35
#define DLY3 35
#define WAUX 16

constexpr int L_ = 2048, D_ = 448, N_ = 256, F_ = 1792;
constexpr u32 WS_EP = 0;
constexpr u32 WS_SG = 4096;
constexpr u32 WS_WF = 32768;
constexpr u32 WS_CS = 40960;
constexpr u32 WS_WR = 1966080;
constexpr u32 WS_HT = 65536;
constexpr u32 WS_HH = 2u << 20;
constexpr u32 WS_WG = 10u << 20;
constexpr u32 WS_WD = 14u << 20;
constexpr u32 WS_XG = 16u << 20;
constexpr u32 WS_F2 = (23u << 20) + (512u << 10);
constexpr u32 WS_F3 = WS_F2 + 8 * 16384;
constexpr u32 WS_END = 24u << 20;
constexpr u32 WS_DBG = 24u << 20;
constexpr u32 POISON = 0xFFFFFFFFu;
constexpr u32 SPIN_CAP = 1u << 16;

constexpr u32 LDS_BYTES = 163840;
constexpr u32 FM = 0, FM_ROW = 1056, FM_CH = 53 * FM_ROW;
constexpr u32 FT = 2 * FM_CH, FT_CH = 772 * 4;
constexpr u32 EOL = FT + 2 * FT_CH;
constexpr u32 RT = 124288;
constexpr u32 PRM = RT + 16384;
constexpr u32 XT = PRM + 8192;
constexpr u32 KC = 0, KC_COPY = 4160, KC_CH = 4 * KC_COPY;
constexpr u32 CX = 2 * KC_CH;
constexpr u32 UU = RT, UU_CH = (256 + 2048) * 2;
constexpr u32 XS = UU + 2 * UU_CH;
static_assert(EOL % 16 == 0 && EOL + 6144 <= RT && XS + 8192 <= XT && XT + 8 * 912 <= 158848 && CX + 8 * 3072 <= FT, "LDS map");
constexpr u32 BT = 0, BT_BYTES = 448 * 256;
constexpr u32 RING = 0, RING_SLOTS = 19;
constexpr u32 SM = 155648;
constexpr u32 SP = SM;
constexpr u32 SQ = SP + 1024;
constexpr u32 CSL = SQ + 1024;
static_assert(RING_SLOTS * 8192 <= SM && CSL + 1024 <= 163328, "LDS map");

struct EoTab { u16 v[256 * 12]; };
constexpr u32 foff_c(int j) {
    if (j < 0) return 768u;
    u32 t = (u32)j;
    int s = t >= 1024 ? 3 : t >= 512 ? 2 : t >= 256 ? 1 : 0;
    if (s > 0) t = ((t + (1u << (s - 1)) - 1u + ((t >> s) & 1u)) >> s) << s;
    u32 col = (t >> 3) & 31u, row = 0;
    if (t < 256u) row = t & 7u;
    else if (t < 512u) row = 8u + ((t >> 1) & 3u);
    else if (t < 1024u) row = 12u + 2u * ((t >> 8) & 1u) + ((t >> 2) & 1u);
    else if (t < 2048u) row = 16u + ((t >> 8) & 3u);
    else row = 20u;
    return row * 32u + col;
}
constexpr EoTab make_eo() { EoTab e{}; for (int t = 0; t < 256; ++t) for (int k = 0; k < 12; ++k) e.v[t * 12 + k] = (u16)(k < 11 ? foff_c(2047 - 8 * t - k) * 4u : 0u); return e; }
__device__ const __attribute__((aligned(16))) EoTab EO = make_eo();

DI float bflo(u32 d) { return __uint_as_float(d << 16); }
DI float bfhi(u32 d) { return __uint_as_float(d & 0xffff0000u); }
DI u32 pk2(float lo, float hi) { bf16x2 v = {(__bf16)lo, (__bf16)hi}; return __builtin_bit_cast(u32, v); }
DI float rbf(float f) { return bflo(pk2(f, f)); }
DI bf16x8 as_bf(v4u v) { return __builtin_bit_cast(bf16x8, v); }
DI f32x4 mfma16(v4u a, v4u b, f32x4 c) { return __builtin_amdgcn_mfma_f32_16x16x32_bf16(as_bf(a), as_bf(b), c, 0, 0, 0); }
DI f32x16 mfma32(v4u a, v4u b, f32x16 c) { return __builtin_amdgcn_mfma_f32_32x32x16_bf16(as_bf(a), as_bf(b), c, 0, 0, 0); }
template <class T> DI LAS T* lds(u32 off) { return (LAS T*)(uintptr_t)off; }
DI v2u tr_read(u32 off) { s16x4 r = __builtin_amdgcn_ds_read_tr16_b64_v4i16(lds<s16x4>(off)); return __builtin_bit_cast(v2u, r); }
DI v4u tr_frag(u32 off0, u32 off1) { v2u a = tr_read(off0), b = tr_read(off1); return v4u{a.x, a.y, b.x, b.y}; }
template <int CTRL, int RM> DI float dpp_add(float v) { int t = __builtin_amdgcn_update_dpp(0, __float_as_int(v), CTRL, RM, 0xf, true); return v + __int_as_float(t); }
DI float wave_sum(float v) {
    v = dpp_add<0x111, 0xf>(v); v = dpp_add<0x112, 0xf>(v); v = dpp_add<0x114, 0xf>(v); v = dpp_add<0x118, 0xf>(v);
    v = dpp_add<0x142, 0xa>(v); v = dpp_add<0x143, 0xc>(v);
    return __int_as_float(__builtin_amdgcn_readlane(__float_as_int(v), 63));
}
DI float half_sum(float v) { const u32 u = __float_as_uint(v); const auto r = __builtin_amdgcn_permlane32_swap(u, u, false, false); return __uint_as_float(r[0]) + __uint_as_float(r[1]); }
DI void fence() { asm volatile("" ::: "memory"); }
DI void bar() { asm volatile("s_waitcnt lgkmcnt(0)" ::: "memory"); __builtin_amdgcn_s_barrier(); asm volatile("" ::: "memory"); }
template <int N> DI void vmw() { asm volatile("s_waitcnt vmcnt(%0)" :: "n"(N) : "memory"); }
template <bool S1> DI void glds16(u32 voff, const void* sbase, u32 ldst) {
    u32 keep;
    if (S1) asm volatile("s_mov_b32 %0, m0\n\ts_mov_b32 m0, %3\n\ts_nop 0\n\tglobal_load_lds_dwordx4 %1, %2 sc1\n\ts_mov_b32 m0, %0" : "=&s"(keep) : "v"(voff), "s"(sbase), "s"(ldst) : "memory");
    else    asm volatile("s_mov_b32 %0, m0\n\ts_mov_b32 m0, %3\n\ts_nop 0\n\tglobal_load_lds_dwordx4 %1, %2\n\ts_mov_b32 m0, %0" : "=&s"(keep) : "v"(voff), "s"(sbase), "s"(ldst) : "memory");
}
DI void glds16v(const void* gaddr, u32 ldst) {
    u32 keep;
    asm volatile("s_mov_b32 %0, m0\n\ts_mov_b32 m0, %2\n\ts_nop 0\n\tglobal_load_lds_dwordx4 %1, off\n\ts_mov_b32 m0, %0" : "=&s"(keep) : "v"(gaddr), "s"(ldst) : "memory");
}
DI float dot2(u32 a, u32 b, float c) { return __builtin_amdgcn_fdot2_f32_bf16(__builtin_bit_cast(bf16x2, a), __builtin_bit_cast(bf16x2, b), c, false); }
DI f2 cmul(f2 a, f2 b) { f2 t = f2{a.x, a.x} * b; return __builtin_elementwise_fma(f2{a.y, a.y}, f2{-b.y, b.x}, t); }
DI f2 cexp_rev(float ar, float ai, float t) {
    const float e = __builtin_amdgcn_exp2f(ar * t), ph = __builtin_amdgcn_fractf(ai * t);
    return f2{e * __builtin_amdgcn_cosf(ph), e * __builtin_amdgcn_sinf(ph)};
}
DI u32 swz_b(u32 row) { return ((row & 3u) << 2) | ((row >> 2) & 3u); }
DI u32 off_b(u32 row, u32 ch) { return 256u * row + 16u * (ch ^ swz_b(row)); }
DI u32 off_a(u32 row, u32 ch) { return 2048u * (row >> 3) + 512u * (ch >> 2) + 64u * (row & 7u) + 16u * ((ch & 3u) ^ ((row >> 2) & 3u)); }
DI u32 max4(v4u v) { return max(max(v.x, v.y), max(v.z, v.w)); }
DI u32 san(u32 v) { return min(v, 0xfffffffeu); }
DI u32 ln_pack(float mean, float var) {
    const float m = rbf(mean), sd = rbf(__builtin_amdgcn_sqrtf(rbf(rbf(var) + 1e-5f)));
    return (__float_as_uint(m) >> 16) | (__float_as_uint(sd) & 0xffff0000u);
}

#define STAMP(i) do { } while (0)
#define CSTAMP(i) do { } while (0)
#define STAMP_DECL
#define STAMP_FLUSH do { } while (0)
#define SGB(mask, n) __builtin_amdgcn_sched_group_barrier(mask, n, 0)
#define SB0() __builtin_amdgcn_sched_barrier(0)
constexpr int M_MFMA = 0x8, M_VMEM = 0x10, M_DSR = 0x100, M_VALU = 0x2;
DI void delay_ticks(u32 n) { const u64 t0 = __builtin_amdgcn_s_memrealtime(); while ((u32)(__builtin_amdgcn_s_memrealtime() - t0) < n) __builtin_amdgcn_s_sleep(1); }
DI u32 opaque(u32 v) { asm volatile("" : "+v"(v)); return v; }

template <int TG> struct Tiles;
template <> struct Tiles<0> { static constexpr int n = 3; static constexpr int t[5] = {7, 6, 2, 0, 0}; };
template <> struct Tiles<1> { static constexpr int n = 5; static constexpr int t[5] = {5, 4, 3, 1, 0}; };

template <int TG> DI void conv_wave(int tid, int w, int lane, int cm, float dsv, __amdgpu_buffer_rsrc_t rsW, v4u (&Wf)[28], u32 wf_off) {
    typedef Tiles<TG> T;
    constexpr int NP = T::t[0] + 1;
    const int ch = w >> 2, rh = (w >> 1) & 1;
    f32x4 acc[T::n];
#pragma unroll
    for (int i = 0; i < T::n; ++i) acc[i] = f32x4{0.f, 0.f, 0.f, 0.f};
    const u32 cb = lane & 15, cq = lane >> 4, cs = (3u - cb) & 3u;
    const u32 kbase = opaque(KC + ch * KC_CH + cs * KC_COPY + 2 * (2047 - 16 * (cq >> 1) - cb + 8 * (cq & 1) - cs) - 32 * 8 * rh - 32 * 118);
    const u32 kbase8 = opaque(kbase + 8);
    const u32 ubase = opaque(UU + ch * UU_CH + 512 + 32 * (cb - (cq >> 1)) + 16 * (cq & 1) - 32 * 8 * rh - 32 * 6);
    v4u Af[2][NP], Bf[2][NP];
    auto ld = [&](int rr, v4u (&A)[NP], v4u (&B)[NP]) {
#pragma unroll
        for (int p = 0; p < NP; ++p) {
            const v2u lo = *lds<volatile v2u>(kbase + 32 * (118 - (16 * p + 2 * rr))), hi = *lds<volatile v2u>(kbase8 + 32 * (118 - (16 * p + 2 * rr)));
            A[p] = v4u{lo.x, lo.y, hi.x, hi.y};
            B[p] = *lds<v4u>(ubase + 32 * (16 * p - 2 * rr + 6));
        }
    };
    ld(0, Af[0], Bf[0]);
#pragma unroll
    for (int rr = 0; rr < 4; ++rr) {
        SB0();
        if (rr < 3) ld(rr + 1, Af[(rr + 1) & 1], Bf[(rr + 1) & 1]);
#pragma unroll
        for (int i = 0; i < 3; ++i) Wf[3 * rr + i] = __builtin_amdgcn_raw_buffer_load_b128(rsW, wf_off + (3 * rr + i) * 1024, 0, WAUX);
#pragma unroll
        for (int p = 0; p < NP; ++p)
#pragma unroll
            for (int i = 0; i < T::n; ++i)
                if (p <= T::t[i]) acc[i] = mfma16(Af[rr & 1][p], Bf[rr & 1][T::t[i] - p], acc[i]);
#pragma unroll
        for (int i = 0; i < 18; ++i) {
            SGB(M_MFMA, 1);
            if (rr < 3) { if (NP == 8 && i < 6) SGB(M_DSR, 2); else SGB(M_DSR, 1); }
            if (i == 3 || i == 9 || i == 15) SGB(M_VMEM, 1);
        }
    }
    SB0();
    CSTAMP(30);
    const u32 mine = CX + w * 3072, other = CX + (w ^ 2) * 3072;
    if (rh == 0) {
#pragma unroll
        for (int i = 1; i < T::n; i += 2) *lds<f32x4>(mine + (i >> 1) * 1024 + lane * 16) = acc[i];
    } else {
#pragma unroll
        for (int i = 0; i < T::n; i += 2) *lds<f32x4>(mine + (i >> 1) * 1024 + lane * 16) = acc[i];
    }
    bar();
    CSTAMP(31);
    auto fin = [&](int i, f32x4 own) {
        const f32x4 y = own + *lds<f32x4>(other + (i >> 1) * 1024 + lane * 16);
        const u32 l0 = 256 * T::t[i] + 16 * cb + 4 * cq;
        const v2u uu = *lds<v2u>(UU + ch * UU_CH + 512 + l0 * 2), xx = *lds<v2u>(XS + ch * 4096 + l0 * 2);
        const float uv[4] = {bflo(uu.x), bfhi(uu.x), bflo(uu.y), bfhi(uu.y)}, xv[4] = {bflo(xx.x), bfhi(xx.x), bflo(xx.y), bfhi(xx.y)};
        float h[4];
#pragma unroll
        for (int r = 0; r < 4; ++r) h[r] = xv[r] + rbf(rbf(y[r]) + rbf(uv[r] * dsv));
        const v2u o2 = {pk2(h[0], h[1]), pk2(h[2], h[3])};
        __builtin_amdgcn_raw_buffer_store_b64(o2, rsW, WS_HT + (2 * cm + ch) * 4096 + l0 * 2, 0, SC1);
    };
    if (rh == 0) {
#pragma unroll
        for (int i = 0; i < T::n; i += 2) fin(i, acc[i]);
    } else {
#pragma unroll
        for (int i = 1; i < T::n; i += 2) fin(i, acc[i]);
    }
}

__global__ void __launch_bounds__(512, 2)
fused_kernel(const u16* __restrict__ x, const u16* __restrict__ Are, const u16* __restrict__ Aim, const u16* __restrict__ Bre, const u16* __restrict__ Bim,
             const u16* __restrict__ Cre, const u16* __restrict__ Cim, const u16* __restrict__ logdt, const u16* __restrict__ Dskip,
             const u16* __restrict__ wg, const u16* __restrict__ wu, const u16* __restrict__ wd, u16* __restrict__ out, char* __restrict__ ws)
{
    const int tid = threadIdx.x, lane = tid & 63, w = __builtin_amdgcn_readfirstlane(tid >> 6);
    const int b = blockIdx.x, g = b & 7, j = b >> 3;
    const bool mainb = j < 28;
    const int cm = mainb ? g * 28 + j : 0;
    const int P = 4 * (g >> 1) + j / 7, S = 7 * (g & 1) + j % 7;
    const __amdgpu_buffer_rsrc_t rsW = __builtin_amdgcn_make_buffer_rsrc(ws, 0, WS_END, 0x00020000);
    const __amdgpu_buffer_rsrc_t rsX = __builtin_amdgcn_make_buffer_rsrc((void*)x, 0, L_ * D_ * 2, 0x00020000);
    const u32 trq = (lane & 15) >> 2, trp = lane & 3, trh = lane >> 5, trb = (lane >> 4) & 1;
    STAMP_DECL
    STAMP(0);
    {
        u64 pc; asm volatile("s_getpc_b64 %0" : "=s"(pc));
        pc &= ~255ull;
        for (int q = j * 8 + w; q < CODEPF; q += 256) glds16<false>((u32)(q * 1024 + lane * 16), (const void*)pc, 157696);
    }
    const u64 tstart = __builtin_amdgcn_s_memrealtime();
    v4u xr; u64 ep; u32 p_ld2, p_dk2;
    asm volatile("global_load_dwordx4 %0, %1, %2" : "=v"(xr) : "v"((u32)((8 * b + w) * 896 + min(lane, 55) * 16)), "s"(x) : "memory");
    asm volatile("s_load_dwordx2 %0, %3, 0x0\n\ts_load_dword %1, %4, 0x0\n\ts_load_dword %2, %5, 0x0" : "=&s"(ep), "=&s"(p_ld2), "=&s"(p_dk2)
                 : "s"(ws + WS_EP), "s"((const char*)logdt + cm * 4), "s"((const char*)Dskip + cm * 4) : "memory");
    const int ch = w >> 2, dch = 2 * cm + ch;
    if (mainb) {
        const int ai_ = lane >> 3;
        const u16* src = ai_ == 0 ? Are : ai_ == 1 ? Aim : ai_ == 2 ? Bre : ai_ == 3 ? Bim : ai_ == 4 ? Cre : Cim;
        glds16v(src + dch * N_ + (w & 3) * 64 + (lane & 7) * 8, PRM + w * 1024);
        const int ew = w < 6 ? w : w - 6;
        glds16<false>((u32)(ew * 1024 + lane * 16), (const void*)&EO, EOL + ew * 1024);
        const int bb_ = b < 196 ? b : b - 196;
#pragma unroll
        for (int i = 0; i < 2; ++i) {
            const int id = 2 * bb_ + i, Sp = id / 28, s = id % 28;
            const u32 row = (w & 3) * 4 + (lane >> 4), chk = (lane & 15) ^ swz_b(row);
            glds16<false>((u32)((16 * s + row) * (F_ * 2) + Sp * 256 + chk * 16), (w >> 2) ? (const void*)wu : (const void*)wg, RT + i * 8192 + w * 1024);
        }
    } else {
        const int hb = (j - 28) * 8 + g;
#pragma unroll
        for (int k = 0; k < 7; ++k) {
            const int gid = min(hb + 32 * k, 195);
            const int dt_ = gid / 28, fg = gid % 28;
            const u32 row = 8 * w + (lane >> 3), chk = (lane & 7) ^ (((row >> 1) & 1) << 2);
            glds16<false>((u32)((64 * fg + row) * (D_ * 2) + dt_ * 128 + chk * 16), (const void*)wd, k * 8192 + w * 1024);
        }
    }
    asm volatile("s_cmp_ge_i32 %1, 28\n\ts_cbranch_scc1 1f\n\ts_waitcnt vmcnt(4)\n\ts_branch 2f\n1:\n\ts_waitcnt vmcnt(7)\n2:" : "+v"(xr) : "s"(__builtin_amdgcn_readfirstlane(j)) : "memory", "scc");
    asm volatile("s_waitcnt lgkmcnt(0)" : "+s"(ep), "+s"(p_ld2), "+s"(p_dk2) :: "memory");
    ep += 1ull;
    const u32 p_ldt = ch ? p_ld2 >> 16 : p_ld2 & 0xffffu, p_dsk = ch ? p_dk2 >> 16 : p_dk2 & 0xffffu;
    if (lane >= 56) xr = v4u{0, 0, 0, 0};
    const u32 tag = (u32)ep, tagx = (u32)(ep >> 32) << 16;
    if (lane < 56) *lds<v4u>(XT + w * 912 + lane * 16) = xr;
    {
        const float v[8] = {bflo(xr.x), bfhi(xr.x), bflo(xr.y), bfhi(xr.y), bflo(xr.z), bfhi(xr.z), bflo(xr.w), bfhi(xr.w)};
        const float mean = wave_sum(((v[0] + v[1]) + (v[2] + v[3])) + ((v[4] + v[5]) + (v[6] + v[7]))) * (1.f / 448.f);
        float q = 0.f;
#pragma unroll
        for (int e = 0; e < 8; ++e) { const float dl = v[e] - mean; q += dl * dl; }
        q = lane < 56 ? q : 0.f;
        const u32 ln1 = ln_pack(mean, wave_sum(q) * (1.f / 448.f));
        if (lane == 0) __hip_atomic_store((GAS u64*)(ws + WS_SG) + (8 * b + w), ((u64)tag << 32) | ln1, RLX_AGENT);
    }
    STAMP(1);
    const u32 km0 = FM + ch * FM_CH + (w & 3) * 256;
    f2 Ga, Gb, Gc, Gd, KX, KY, G4, G4r, E1, E1r, E256, E256r, E2, E2r, E4, E4r;
#define M1(a, bb) (f2{(a).x, (a).x} * (bb))
#define M2(a, br, t) __builtin_elementwise_fma(f2{(a).y, (a).y}, (br), (t))
#define WROW1(r, v) do { u32 keep_; asm volatile("s_mov_b32 %0, m0\n\ts_mov_b32 m0, %2\n\ts_nop 0\n\tds_write_addtid_b32 %1 offset:%3\n\ts_mov_b32 m0, %0" : "=&s"(keep_) : "v"(v), "s"(km0), "n"((r) * FM_ROW) : "memory"); } while (0)
#define WROW6(r0, v0, r1, v1, r2, v2, r3, v3, r4, v4, r5, v5) do { u32 keep_; asm volatile("s_mov_b32 %0, m0\n\ts_mov_b32 m0, %7\n\ts_nop 0\n\t" \
        "ds_write_addtid_b32 %1 offset:%8\n\tds_write_addtid_b32 %2 offset:%9\n\tds_write_addtid_b32 %3 offset:%10\n\tds_write_addtid_b32 %4 offset:%11\n\tds_write_addtid_b32 %5 offset:%12\n\tds_write_addtid_b32 %6 offset:%13\n\ts_mov_b32 m0, %0" \
        : "=&s"(keep_) : "v"(v0), "v"(v1), "v"(v2), "v"(v3), "v"(v4), "v"(v5), "s"(km0), "n"((r0) * FM_ROW), "n"((r1) * FM_ROW), "n"((r2) * FM_ROW), "n"((r3) * FM_ROW), "n"((r4) * FM_ROW), "n"((r5) * FM_ROW) : "memory"); } while (0)
#define WA(r, X) WROW1(r, pk2((X).x, -(X).y))
#define KSTEP(t) do { SB0(); \
        const f2 ta = M1(Ga, G4), tb = M1(Gb, G4), tc = M1(Gc, G4), td_ = M1(Gd, G4), tx = M1(KX, E1), ty = M1(KY, E256); \
        const u32 c0 = pk2(Ga.x, Ga.y), c1 = pk2(Gb.x, Gb.y), c2 = pk2(Gc.x, Gc.y), c3 = pk2(Gd.x, Gd.y), c4 = pk2(KX.x, -KX.y), c5 = pk2(KY.x, -KY.y); \
        f2 ts0, ts1; \
        if ((t) == 0) ts0 = M1(KY, E2); \
        if ((t) == 1 || (t) == 2) ts0 = M1(KY, E4); \
        SB0(); \
        WROW6(21 + 4 * (t), c0, 22 + 4 * (t), c1, 23 + 4 * (t), c2, 24 + 4 * (t), c3, (t), c4, ((t) == 0 ? 8 : (t) == 1 ? 12 : (t) == 2 ? 14 : 13 + (t)), c5); \
        if ((t) == 0) { f2 Z = M2(KY, E2r, ts0); WA(9, Z); ts1 = M1(Z, E2); Z = M2(Z, E2r, ts1); WA(10, Z); ts1 = M1(Z, E2); Z = M2(Z, E2r, ts1); WA(11, Z); } \
        if ((t) == 1) { const f2 V = M2(KY, E4r, ts0); WA(13, V); } \
        if ((t) == 2) { const f2 V = M2(KY, E4r, ts0); WA(15, V); } \
        if ((t) < 7) { Ga = M2(Ga, G4r, ta); Gb = M2(Gb, G4r, tb); Gc = M2(Gc, G4r, tc); Gd = M2(Gd, G4r, td_); KX = M2(KX, E1r, tx); KY = M2(KY, E256r, ty); } \
    } while (0)
    if (mainb) {
        vmw<4>();
        const u32 pa_ = PRM + w * 1024 + lane * 2;
        const u32 p_are = *lds<u16>(pa_), p_aim = *lds<u16>(pa_ + 128), p_bre = *lds<u16>(pa_ + 256), p_bim = *lds<u16>(pa_ + 384), p_cre = *lds<u16>(pa_ + 512), p_cim = *lds<u16>(pa_ + 640);
        const float dt = rbf(expf(bflo(p_ldt)));
        const float zr = dt * bflo(p_are), zi = dt * bflo(p_aim);
        const float ar = zr * 1.4426950408889634f, ai = zi * 0.15915494309189535f;
        const f2 CB = cmul(f2{bflo(p_cre), bflo(p_cim)}, f2{dt * bflo(p_bre), dt * bflo(p_bim)});
        E1 = cexp_rev(ar, ai, 1.f); E256 = cexp_rev(ar, ai, 256.f);
        const f2 G1 = cexp_rev(ar, ai, 8.f);
        E2 = cmul(E1, E1); E4 = cmul(E2, E2);
        const f2 G2 = cmul(G1, G1);
        G4 = cmul(G2, G2);
        E1r = f2{-E1.y, E1.x}; E2r = f2{-E2.y, E2.x}; E4r = f2{-E4.y, E4.x}; E256r = f2{-E256.y, E256.x}; G4r = f2{-G4.y, G4.x};
        Ga = f2{1.f, 0.f}; Gb = G1; Gc = G2; Gd = cmul(G2, G1);
        KX = CB; KY = cmul(CB, E256);
        SB0();
    }
    bar();
    STAMP(16);
    auto publish_xg = [&]() {
        u32 x0[4], x1[4];
#pragma unroll
        for (int k = 0; k < 4; ++k) {
            const int id = min(tid + 512 * k, 1791), c = id >> 2, i2 = (id & 3) * 2;
            x0[k] = *lds<u16>(XT + i2 * 912 + c * 2); x1[k] = *lds<u16>(XT + (i2 + 1) * 912 + c * 2);
        }
        SB0();
#pragma unroll
        for (int k = 0; k < 4; ++k)
            __builtin_amdgcn_raw_buffer_store_b128(v4u{x0[k] | tagx, tag, x1[k] | tagx, tag}, rsW, (tid + 512 * k < 1792) ? (u32)(WS_XG + b * 28672 + (tid + 512 * k) * 16) : 0x7ffffff0u, 0, SC1);
    };
    if (mainb) {
    publish_xg();
    STAMP(17);
    KSTEP(0); KSTEP(1); KSTEP(2);
    SB0();
    vmw<5>();
    bar();
    if (b < 196) {
#pragma unroll
        for (int i = 0; i < 2; ++i) {
            const int id = 2 * b + i, Sp = id / 28, s = id % 28;
            const u32 base = RT + i * 8192 + trb * 4096 + 8 * (trp & 1);
            const v4u f = tr_frag(base + off_b(8 * trh + trq, 2 * w + (trp >> 1)), base + off_b(8 * trh + 4 + trq, 2 * w + (trp >> 1)));
            __builtin_amdgcn_raw_buffer_store_b128(f, rsW, WS_WG + ((Sp * 8 + w) * 28 + s) * 1024 + lane * 16, 0, SC1);
        }
    }
    fence();
    STAMP(2);
    {
        KSTEP(3); KSTEP(4); KSTEP(5); KSTEP(6); KSTEP(7);
        SB0();
#undef KSTEP
#undef WA
#undef WROW1
#undef WROW6
#undef M1
#undef M2
        STAMP(3);
        vmw<0>();
        bar();
        if (tid == 0 && b < 196) __hip_atomic_store((GAS u32*)(ws + WS_WR + b * 64), tag, RLX_AGENT);
        const int ta_ = (w >> 1) & 1, tb_ = w & 1;
        const u32 fa_ = opaque(FM + ch * FM_CH + (16 * ta_ + (lane & 15)) * FM_ROW + (lane >> 4) * 16), fb_ = opaque(FM + ch * FM_CH + (21 + 16 * tb_ + (lane & 15)) * FM_ROW + (lane >> 4) * 16);
        f32x4 f0 = {0.f, 0.f, 0.f, 0.f}, f1 = {0.f, 0.f, 0.f, 0.f};
        v4u A[16], B[16];
#pragma unroll
        for (int ks = 0; ks < 16; ++ks) { A[ks] = *lds<v4u>(fa_ + ks * 64); B[ks] = *lds<v4u>(fb_ + ks * 64); }
        SB0();
#pragma unroll
        for (int ks = 0; ks < 16; ks += 2) { f0 = mfma16(A[ks], B[ks], f0); f1 = mfma16(A[ks + 1], B[ks + 1], f1); }
        f0 += f1;
#pragma unroll
        for (int r = 0; r < 4; ++r) { const int row = 16 * ta_ + 4 * (lane >> 4) + r; if (row < 24) *lds<float>(FT + ch * FT_CH + (row * 32 + 16 * tb_ + (lane & 15)) * 4) = f0[r]; }
        if ((tid & 255) == 255) *lds<float>(FT + ch * FT_CH + 768 * 4) = 0.f;
    }
    STAMP(4);
    {
        v4u xg[4], sg[4];
        const u32 fa = WS_WR + (tid < 196 ? tid : 0) * 64;
        const u32 xa = WS_XG + (32 * w + (lane >> 3)) * 28672 + cm * 128 + (lane & 7) * 16, sa = WS_SG + (32 * w + (lane >> 3)) * 64 + (lane & 3) * 16;
        if (DLY1) { while ((u32)(__builtin_amdgcn_s_memrealtime() - tstart) < DLY1) __builtin_amdgcn_s_sleep(1); }
        for (u32 spin = 0;; ++spin) {
#pragma unroll
            for (int k = 0; k < 4; ++k) { xg[k] = __builtin_amdgcn_raw_buffer_load_b128(rsW, xa + k * 8 * 28672, 0, SC1); sg[k] = __builtin_amdgcn_raw_buffer_load_b128(rsW, sa + k * 512, 0, SC1); }
            const u32 fl = __builtin_amdgcn_raw_buffer_load_b32(rsW, fa, 0, SC1);
            bool ok = fl == tag;
#pragma unroll
            for (int k = 0; k < 4; ++k) ok = ok && sg[k].y == tag && sg[k].w == tag && xg[k].y == tag && xg[k].w == tag && (xg[k].x & 0xffff0000u) == tagx && (xg[k].z & 0xffff0000u) == tagx;
            if (__all(ok) || spin > SPIN_CAP) break;
            __builtin_amdgcn_s_sleep(8);
        }
        STAMP(5);
        const int c = (lane >> 2) & 1;
#pragma unroll
        for (int k = 0; k < 4; ++k) {
            const u32 l0 = 8 * (32 * w + 8 * k + (lane >> 3)) + 2 * (lane & 3);
            const float u0 = rbf(bflo(xg[k].x) - bflo(sg[k].x)) * __builtin_amdgcn_rcpf(bfhi(sg[k].x));
            const float u1 = rbf(bflo(xg[k].z) - bflo(sg[k].z)) * __builtin_amdgcn_rcpf(bfhi(sg[k].z));
            *lds<u32>(UU + c * UU_CH + 512 + l0 * 2) = pk2(u0, u1);
            *lds<u32>(XS + c * 4096 + l0 * 2) = (xg[k].x & 0xffffu) | (xg[k].z << 16);
        }
    }
    if (tid >= 64 && tid < 128) *lds<v4u>(UU + ((tid >> 5) & 1) * UU_CH + (tid & 31) * 16) = v4u{0, 0, 0, 0};
    bar();
    STAMP(18);
    {
        const v2u ea = *lds<v2u>(EOL + (tid & 255) * 24), eb = *lds<v2u>(EOL + (tid & 255) * 24 + 8), ec = *lds<v2u>(EOL + (tid & 255) * 24 + 16);
        const v4u eo0 = {ea.x, ea.y, eb.x, eb.y}; const v2u eo1 = ec;
        const u32 eo[11] = {eo0.x & 0xffffu, eo0.x >> 16, eo0.y & 0xffffu, eo0.y >> 16, eo0.z & 0xffffu, eo0.z >> 16, eo0.w & 0xffffu, eo0.w >> 16, eo1.x & 0xffffu, eo1.x >> 16, eo1.y & 0xffffu};
        float v[11];
#pragma unroll
        for (int e = 0; e < 11; ++e) v[e] = *lds<float>(FT + ch * FT_CH + eo[e]);
        SB0();
        u32 p[10];
#pragma unroll
        for (int e = 0; e < 10; ++e) p[e] = pk2(v[e], v[e + 1]);
#pragma unroll
        for (int s = 0; s < 4; ++s) *lds<v4u>(KC + ch * KC_CH + s * KC_COPY + (tid & 255) * 16) = v4u{p[s], p[s + 2], p[s + 4], p[s + 6]};
    }
    if (tid < 16) *lds<v4u>(KC + (tid >> 3) * KC_CH + ((tid >> 1) & 3) * KC_COPY + 4096 + (tid & 1) * 16) = v4u{0, 0, 0, 0};
    bar();
    STAMP(6);
    CSTAMP(26);
    v4u Wf[28];
    const u32 wf_off = WS_WG + (S * 8 + w) * 28 * 1024 + lane * 16;
    {

        if (w & 1) conv_wave<1>(tid, w, lane, cm, bflo(p_dsk), rsW, Wf, wf_off); else conv_wave<0>(tid, w, lane, cm, bflo(p_dsk), rsW, Wf, wf_off);
    }
    STAMP(7);
    CSTAMP(27);
    const int l0p = 128 * P;
    vmw<0>();
    bar();
    {
        if (tid < 8) __hip_atomic_store((GAS u32*)(ws + WS_F2 + tid * 16384 + cm * 64), tag, RLX_AGENT);
        fence();
#pragma unroll
        for (int i = 12; i < 28; ++i) Wf[i] = __builtin_amdgcn_raw_buffer_load_b128(rsW, wf_off + i * 1024, 0, WAUX);
        fence();
        delay_ticks(DLY2);
        if (w < 4) {
            const u32 pa = tid < 224 ? (u32)(WS_F2 + g * 16384 + tid * 64) : (u32)(WS_WF + (tid - 224) * 64);
            for (u32 spin = 0;; ++spin) {
                const u32 s0 = __builtin_amdgcn_raw_buffer_load_b32(rsW, pa, 0, SC1);
                if (__all(s0 == tag) || spin > SPIN_CAP) break;
                __builtin_amdgcn_s_sleep(8);
            }
        }
    }
    bar();
    STAMP(8);
    if (b == 0 && tid == 0) __hip_atomic_store((GAS u64*)(ws + WS_EP), ep, RLX_AGENT);
    const u32 bt_src = (8 * (w >> 1) + ((lane >> 2) & 7)) * 4096 + l0p * 2 + (4 * (2 * (w & 1) + (lane >> 5)) + ((lane & 3) ^ ((2 * (w >> 1) + ((lane >> 4) & 1)) & 3))) * 16;
    const u32 bt_dst = BT + (w >> 1) * 2048 + (w & 1) * 1024;
    auto bt_piece = [&](int k) { glds16<BTSC1>(bt_src + k * 32 * 4096, ws + WS_HT, bt_dst + k * 8192); };
    const float csum = __uint_as_float(__builtin_amdgcn_raw_buffer_load_b32(rsW, WS_CS + (((lane >> 4) & 1) * 1792 + 128 * S + 16 * w + (lane & 15)) * 4, 0, SC1));
    fence();
#pragma unroll
    for (int k = 0; k < 6; ++k) bt_piece(k);
    vmw<4>();
    bar();
    STAMP(9);
    CSTAMP(24);
    f32x16 acc[4];
    v4u Wd[28];
    const int td = w & 1, kq = w >> 1, hp = S / 7, dtile = S % 7;
    const u32 wd_off = WS_WD + ((dtile * 2 + td) * 112 + 28 * kq) * 1024 + lane * 16;
    u32 bb[2];
#pragma unroll
    for (int tt = 0; tt < 2; ++tt) bb[tt] = opaque(BT + 2048 * trh + 64 * (4 * tt + trq) + 16 * ((2 * trb + (trp >> 1)) ^ ((2 * trh + tt) & 3)) + 8 * (trp & 1));
    {
#pragma unroll
        for (int t = 0; t < 4; ++t)
#pragma unroll
            for (int i = 0; i < 16; ++i) acc[t][i] = 0.f;
        v4u Bf[2][4], Of;
        float sa0 = 0.f, sa1 = 0.f, sq0 = 0.f, sq1 = 0.f;
        const u32 bh0 = opaque(bb[0] + 65536), bh1 = opaque(bb[1] + 65536);
        const u32 so0 = opaque(bb[0] + (w >> 2) * 4096 + (w & 3) * 512), so1 = opaque(bb[1] + (w >> 2) * 4096 + (w & 3) * 512);
        const u32 sh0 = opaque(so0 + 65536), sh1 = opaque(so1 + 65536);
#pragma unroll
        for (int t = 0; t < 4; ++t) Bf[0][t] = tr_frag(bb[0] + 512 * t, bb[1] + 512 * t);
#pragma unroll
        for (int s = 0; s < 28; ++s) {
            SB0();
            if (s == 3) { vmw<6>(); bar(); }
            if (s == 11) { vmw<7>(); bar(); }
            SB0();
            if (s + 1 < 28) {
#pragma unroll
                for (int t = 0; t < 4; ++t)
                    Bf[(s + 1) & 1][t] = (s + 1 < 16) ? tr_frag(bb[0] + (s + 1) * 4096 + 512 * t, bb[1] + (s + 1) * 4096 + 512 * t)
                                                       : tr_frag(bh0 + (s + 1 - 16) * 4096 + 512 * t, bh1 + (s + 1 - 16) * 4096 + 512 * t);
            }
            if ((s & 1) == 0) Of = (s < 16) ? tr_frag(so0 + (s >> 1) * 8192, so1 + (s >> 1) * 8192) : tr_frag(sh0 + ((s - 16) >> 1) * 8192, sh1 + ((s - 16) >> 1) * 8192);
            if (s < 4) { bt_piece(6 + 2 * s); bt_piece(7 + 2 * s); }
            else Wd[s - 4] = __builtin_amdgcn_raw_buffer_load_b128(rsW, wd_off + (s - 4) * 1024, 0, WAUX);
#pragma unroll
            for (int t = 0; t < 4; ++t) acc[t] = mfma32(Wf[s], Bf[s & 1][t], acc[t]);
            if (s & 1) {
#pragma unroll
                for (int e = 0; e < 4; e += 2) {
                    const float l0 = bflo(Of[e]), h0 = bfhi(Of[e]), l1 = bflo(Of[e + 1]), h1 = bfhi(Of[e + 1]);
                    sa0 += l0; sq0 = __builtin_fmaf(l0, l0, sq0); sa1 += l1; sq1 = __builtin_fmaf(l1, l1, sq1);
                    sa0 += h0; sq0 = __builtin_fmaf(h0, h0, sq0); sa1 += h1; sq1 = __builtin_fmaf(h1, h1, sq1);
                }
            }
            if (s >= 4) {
                if (s & 1) { SGB(M_MFMA, 1); SGB(M_DSR, 2); SGB(M_VALU, 6); SGB(M_MFMA, 1); SGB(M_DSR, 2); SGB(M_VALU, 6); SGB(M_VMEM, 1); SGB(M_MFMA, 1); SGB(M_DSR, 2); SGB(M_VALU, 6); SGB(M_MFMA, 1); SGB(M_DSR, 2); SGB(M_VALU, 6); }
                else { SGB(M_MFMA, 1); SGB(M_DSR, 3); SGB(M_MFMA, 1); SGB(M_DSR, 3); SGB(M_VMEM, 1); SGB(M_MFMA, 1); SGB(M_DSR, 2); SGB(M_MFMA, 1); SGB(M_DSR, 2); }
            }
        }
        float sumh = sa0 + sa1, sq = sq0 + sq1;

        SB0();
    #pragma unroll
        for (int s = 24; s < 28; ++s) Wd[s] = __builtin_amdgcn_raw_buffer_load_b128(rsW, wd_off + s * 1024, 0, WAUX);

        sumh = half_sum(sumh); sq = half_sum(sq);
        if (lane < 32) { *lds<float>(SP + w * 128 + lane * 4) = sumh; *lds<float>(SQ + w * 128 + lane * 4) = sq; *lds<float>(CSL + w * 128 + lane * 4) = csum; }
    }
    STAMP(10);
    CSTAMP(25);
    bar();
    {
        f32x4 csg[2], csu[2];
#pragma unroll
        for (int q = 0; q < 2; ++q) { csg[q] = *lds<f32x4>(CSL + w * 128 + (8 * q + 4 * trh) * 4); csu[q] = *lds<f32x4>(CSL + w * 128 + 64 + (8 * q + 4 * trh) * 4); }
#pragma unroll
        for (int t = 0; t < 4; ++t) {
            const float m = rbf((*lds<float>(SP + t * 128 + (lane & 31) * 4) + *lds<float>(SP + (t + 4) * 128 + (lane & 31) * 4)) * (1.f / 448.f));
            float hv[8];
#pragma unroll
            for (int e = 0; e < 8; ++e) hv[e] = (acc[t][e] - m * csg[e >> 2][e & 3]) * (acc[t][e + 8] - m * csu[e >> 2][e & 3]);
            const v4u o = {pk2(hv[0], hv[1]), pk2(hv[2], hv[3]), pk2(hv[4], hv[5]), pk2(hv[6], hv[7])};
            __builtin_amdgcn_raw_buffer_store_b128(o, rsW, WS_HH + ((P * 112 + 8 * S + w) * 4 + t) * 1024 + lane * 16, 0, SC1);
        }
    }
    STAMP(11);
    const int tl_f = kq >> 1;
    const u32 lp = 64 * hp + 32 * tl_f + (lane & 31);
    float hres[8], rs;
    {
        const int tq = 2 * hp + tl_f;
        const float s1 = *lds<float>(SP + tq * 128 + (lane & 31) * 4) + *lds<float>(SP + (tq + 4) * 128 + (lane & 31) * 4);
        const float s2 = *lds<float>(SQ + tq * 128 + (lane & 31) * 4) + *lds<float>(SQ + (tq + 4) * 128 + (lane & 31) * 4);
        const float mean = s1 * (1.f / 448.f), var = fmaxf(s2 * (1.f / 448.f) - mean * mean, 0.f);
        const float sd = bfhi(ln_pack(mean, var));
        rs = 1.f / (sd * sd);
#pragma unroll
        for (int e = 0; e < 8; ++e) {
            const u32 row = 64 * dtile + 32 * td + 16 * (kq & 1) + 8 * (e >> 2) + 4 * trh + (e & 3);
            hres[e] = bflo(*lds<u16>(BT + off_a(row, lp >> 3) + (lp & 7) * 2));
        }
    }
    vmw<0>();
    bar();
    if (tid == 0) __hip_atomic_store((GAS u32*)(ws + WS_F3 + (P * 16 + S) * 64), tag, RLX_AGENT);
    const u32 hbase = WS_HH + ((P * 112 + 28 * kq) * 4 + 2 * hp) * 1024;
    if (w == 0) {
        delay_ticks(DLY3);
        const u32 pa = WS_F3 + (P * 16 + (lane < 14 ? lane : 0)) * 64;
        for (u32 spin = 0;; ++spin) {
            const u32 s0 = __builtin_amdgcn_raw_buffer_load_b32(rsW, pa, 0, SC1);
            if (__all(s0 == tag) || spin > SPIN_CAP) break;
            __builtin_amdgcn_s_sleep(8);
        }
    }
    bar();
    STAMP(12);
    const u32 hsrc = td * 1024 + lane * 16, hdst = RING + kq * 2048 + td * 1024, hrd = opaque(RING + kq * 2048 + lane * 16);
    auto hh_dma = [&](int s) { glds16<true>(hsrc + s * 4096, ws + hbase, hdst + (s % RING_SLOTS) * 8192); };
    f32x16 o2[2];
    {
#pragma unroll
        for (int i = 0; i < 16; ++i) { o2[0][i] = 0.f; o2[1][i] = 0.f; }
        constexpr int AH = 9;
#pragma unroll
        for (int s = 0; s < AH; ++s) hh_dma(s);
        v4u hf[2][2];
        vmw<AH - 4>(); bar();
        STAMP(13); CSTAMP(28);
#pragma unroll
        for (int tl = 0; tl < 2; ++tl) hf[0][tl] = *lds<v4u>(hrd + tl * 1024);
#pragma unroll
        for (int s = 0; s < 28; ++s) {
            SB0();
            if ((s & 3) == 3 && s + 1 < 28) {
                const int issued = (s - 1 + AH < 27) ? s - 1 + AH : 27, need = (s + 4 < 27) ? s + 4 : 27;
                switch (issued - need) { case 0: vmw<0>(); break; case 1: vmw<1>(); break; case 2: vmw<2>(); break; case 3: vmw<3>(); break; default: vmw<4>(); break; }
                bar();
                SB0();
            }
            if (s + 1 < 28) {
#pragma unroll
                for (int tl = 0; tl < 2; ++tl) hf[(s + 1) & 1][tl] = *lds<v4u>(hrd + ((s + 1) % RING_SLOTS) * 8192 + tl * 1024);
            }
            if (s + AH < 28) hh_dma(s + AH);
#pragma unroll
            for (int tl = 0; tl < 2; ++tl) o2[tl] = mfma32(Wd[s], hf[s & 1][tl], o2[tl]);
        }
        SB0();
        bar();
    }
    STAMP(14);
    CSTAMP(29);
    {
#pragma unroll
        for (int k2 = 0; k2 < 4; ++k2) {
            if (k2 != kq) {
                const int si = (kq > k2) ? kq - 1 : kq;
                const f32x16& a = o2[k2 >> 1];
#pragma unroll
                for (int q = 0; q < 2; ++q) { const int r0 = 8 * (k2 & 1) + 4 * q; *lds<f32x4>(((td * 4 + k2) * 3 + si) * 2048 + q * 1024 + lane * 16) = f32x4{a[r0], a[r0 + 1], a[r0 + 2], a[r0 + 3]}; }
            }
        }
        bar();
        f32x4 v[2];
#pragma unroll
        for (int q = 0; q < 2; ++q) {
            if (kq == 0) v[q] = f32x4{o2[0][4 * q], o2[0][4 * q + 1], o2[0][4 * q + 2], o2[0][4 * q + 3]};
            else if (kq == 1) v[q] = f32x4{o2[0][8 + 4 * q], o2[0][9 + 4 * q], o2[0][10 + 4 * q], o2[0][11 + 4 * q]};
            else if (kq == 2) v[q] = f32x4{o2[1][4 * q], o2[1][4 * q + 1], o2[1][4 * q + 2], o2[1][4 * q + 3]};
            else v[q] = f32x4{o2[1][8 + 4 * q], o2[1][9 + 4 * q], o2[1][10 + 4 * q], o2[1][11 + 4 * q]};
#pragma unroll
            for (int si = 0; si < 3; ++si) v[q] += *lds<f32x4>(((td * 4 + kq) * 3 + si) * 2048 + q * 1024 + lane * 16);
            const u32 d0 = 64 * dtile + 32 * td + 16 * (kq & 1) + 8 * q + 4 * trh;
            float r[4];
#pragma unroll
            for (int e = 0; e < 4; ++e) r[e] = hres[4 * q + e] + rbf(v[q][e] * rs);
            *(GAS v2u*)(out + (size_t)(l0p + lp) * D_ + d0) = v2u{pk2(r[0], r[1]), pk2(r[2], r[3])};
        }
    }
    STAMP(15);
    STAMP_FLUSH;
    } else {
        publish_xg();
        const int hb = (j - 28) * 8 + g;
        float cs[8];
        {
            const u16* wsrc = hb < 16 ? wg : wu;
            const int c0 = 112 * (hb & 15) + 8 * (tid % 14), r0 = tid / 14;
#pragma unroll
            for (int e = 0; e < 8; ++e) cs[e] = 0.f;
            v4u t[13];
#pragma unroll
            for (int i = 0; i < 13; ++i) { const int r = r0 + 36 * i; t[i] = (tid < 504 && r < 448) ? *(const v4u*)(wsrc + (size_t)r * F_ + c0) : v4u{0, 0, 0, 0}; }
#pragma unroll
            for (int i = 0; i < 13; ++i) { cs[0] += bflo(t[i].x); cs[1] += bfhi(t[i].x); cs[2] += bflo(t[i].y); cs[3] += bfhi(t[i].y); cs[4] += bflo(t[i].z); cs[5] += bfhi(t[i].z); cs[6] += bflo(t[i].w); cs[7] += bfhi(t[i].w); }
        }
        vmw<0>(); bar();
#pragma unroll
        for (int k = 0; k < 7; ++k) {
            const int gid = hb + 32 * k;
            if (gid < 196) {
                const int dt_ = gid / 28, fg = gid % 28, fsl = w >> 1, td = w & 1;
                u32 ad[2];
#pragma unroll
                for (int t = 0; t < 2; ++t) { const u32 row = 16 * fsl + 8 * t + 4 * trh + trq, chk = 4 * td + 2 * trb + (trp >> 1); ad[t] = k * 8192 + row * 128 + 16 * (chk ^ (((row >> 1) & 1) << 2)) + 8 * (trp & 1); }
                const v4u f = tr_frag(ad[0], ad[1]);
                __builtin_amdgcn_raw_buffer_store_b128(f, rsW, WS_WD + ((dt_ * 2 + td) * 112 + 4 * fg + fsl) * 1024 + lane * 16, 0, SC1);
            }
        }
        if (tid < 504) {
            *lds<f32x4>(65536 + tid * 32) = f32x4{cs[0], cs[1], cs[2], cs[3]};
            *lds<f32x4>(65536 + tid * 32 + 16) = f32x4{cs[4], cs[5], cs[6], cs[7]};
        }
        bar();
        if (tid < 112) {
            float s = 0.f;
            for (int r = 0; r < 36; ++r) s += *lds<float>(65536 + (r * 14 + (tid >> 3)) * 32 + (tid & 7) * 4);
            __hip_atomic_store((GAS u32*)(ws + WS_CS) + ((hb < 16 ? 0 : 1792) + 112 * (hb & 15) + tid), __float_as_uint(s), RLX_AGENT);
        }
        vmw<0>(); bar();
        if (tid == 0) __hip_atomic_store((GAS u32*)(ws + WS_WF + hb * 64), tag, RLX_AGENT);

    }
}

extern "C" void kernel_launch(void* const* d_in, const int* in_sizes, int n_in, void* d_out, int out_size, void* d_ws, size_t ws_size, hipStream_t stream) {
    (void)in_sizes; (void)n_in; (void)out_size; (void)ws_size;
    (void)hipFuncSetAttribute((const void*)fused_kernel, hipFuncAttributeMaxDynamicSharedMemorySize, LDS_BYTES);
    fused_kernel<<<256, 512, LDS_BYTES, stream>>>((const u16*)d_in[0], (const u16*)d_in[1], (const u16*)d_in[2], (const u16*)d_in[3], (const u16*)d_in[4],
        (const u16*)d_in[5], (const u16*)d_in[6], (const u16*)d_in[7], (const u16*)d_in[8], (const u16*)d_in[10], (const u16*)d_in[11], (const u16*)d_in[12],
        (u16*)d_out, (char*)d_ws);
}
```

```cpp
#include <hip/hip_runtime.h>
#include <stdint.h>

typedef unsigned short u16;
typedef unsigned u32;
typedef unsigned long long u64;
typedef u32 v4u __attribute__((ext_vector_type(4)));
typedef u32 v2u __attribute__((ext_vector_type(2)));
typedef float f32x4 __attribute__((ext_vector_type(4)));
typedef float f32x16 __attribute__((ext_vector_type(16)));
typedef float f2 __attribute__((ext_vector_type(2)));
typedef __bf16 bf16x8 __attribute__((ext_vector_type(8)));
typedef __bf16 bf16x2 __attribute__((ext_vector_type(2)));
typedef short s16x4 __attribute__((ext_vector_type(4)));
#define LAS __attribute__((address_space(3)))
#define GAS __attribute__((address_space(1)))
#define DI __device__ __forceinline__
#define RLX_AGENT __ATOMIC_RELAXED, __HIP_MEMORY_SCOPE_AGENT
#define CODEPF 28
#define PAGEPF 1
#define SC1 16
#define BTSC1 true
#define DLY1 0
#define DLY2 35
#define DLY3 35
#define WAUX 16

constexpr int L_ = 2048, D_ = 448, N_ = 256, F_ = 1792;
constexpr u32 WS_EP = 0;
constexpr u32 WS_SG = 4096;
constexpr u32 WS_WF = 32768;
constexpr u32 WS_CS = 40960;
constexpr u32 WS_WR = 1966080;
constexpr u32 WS_HT = 65536;
constexpr u32 WS_HH = 2u << 20;
constexpr u32 WS_WG = 10u << 20;
constexpr u32 WS_WD = 14u << 20;
constexpr u32 WS_XG = 16u << 20;
constexpr u32 WS_F2 = (23u << 20) + (512u << 10);
constexpr u32 WS_F3 = WS_F2 + 8 * 16384;
constexpr u32 WS_END = 24u << 20;
constexpr u32 WS_DBG = 24u << 20;
constexpr u32 POISON = 0xFFFFFFFFu;
constexpr u32 SPIN_CAP = 1u << 16;

constexpr u32 LDS_BYTES = 163840;
constexpr u32 FM = 0, FM_ROW = 1056, FM_CH = 53 * FM_ROW;
constexpr u32 FT = 2 * FM_CH, FT_CH = 772 * 4;
constexpr u32 EOL = FT + 2 * FT_CH;
constexpr u32 RT = 124288;
constexpr u32 PRM = RT + 16384;
constexpr u32 XT = PRM + 8192;
constexpr u32 KC = 0, KC_COPY = 4160, KC_CH = 4 * KC_COPY;
constexpr u32 CX = 2 * KC_CH;
constexpr u32 UU = RT, UU_CH = (256 + 2048) * 2;
constexpr u32 XS = UU + 2 * UU_CH;
static_assert(EOL % 16 == 0 && EOL + 6144 <= RT && XS + 8192 <= XT && XT + 8 * 912 <= 158848 && CX + 8 * 3072 <= FT, "LDS map");
constexpr u32 BT = 0, BT_BYTES = 448 * 256;
constexpr u32 RING = 0, RING_SLOTS = 19;
constexpr u32 SM = 155648;
constexpr u32 SP = SM;
constexpr u32 SQ = SP + 1024;
constexpr u32 CSL = SQ + 1024;
static_assert(RING_SLOTS * 8192 <= SM && CSL + 1024 <= 163328, "LDS map");

struct EoTab { u16 v[256 * 12]; };
constexpr u32 foff_c(int j) {
    if (j < 0) return 768u;
    u32 t = (u32)j;
    int s = t >= 1024 ? 3 : t >= 512 ? 2 : t >= 256 ? 1 : 0;
    if (s > 0) t = ((t + (1u << (s - 1)) - 1u + ((t >> s) & 1u)) >> s) << s;
    u32 col = (t >> 3) & 31u, row = 0;
    if (t < 256u) row = t & 7u;
    else if (t < 512u) row = 8u + ((t >> 1) & 3u);
    else if (t < 1024u) row = 12u + 2u * ((t >> 8) & 1u) + ((t >> 2) & 1u);
    else if (t < 2048u) row = 16u + ((t >> 8) & 3u);
    else row = 20u;
    return row * 32u + col;
}
constexpr EoTab make_eo() { EoTab e{}; for (int t = 0; t < 256; ++t) for (int k = 0; k < 12; ++k) e.v[t * 12 + k] = (u16)(k < 11 ? foff_c(2047 - 8 * t - k) * 4u : 0u); return e; }
__device__ const __attribute__((aligned(16))) EoTab EO = make_eo();

DI float bflo(u32 d) { return __uint_as_float(d << 16); }
DI float bfhi(u32 d) { return __uint_as_float(d & 0xffff0000u); }
DI u32 pk2(float lo, float hi) { bf16x2 v = {(__bf16)lo, (__bf16)hi}; return __builtin_bit_cast(u32, v); }
DI float rbf(float f) { return bflo(pk2(f, f)); }
DI bf16x8 as_bf(v4u v) { return __builtin_bit_cast(bf16x8, v); }
DI f32x4 mfma16(v4u a, v4u b, f32x4 c) { return __builtin_amdgcn_mfma_f32_16x16x32_bf16(as_bf(a), as_bf(b), c, 0, 0, 0); }
DI f32x16 mfma32(v4u a, v4u b, f32x16 c) { return __builtin_amdgcn_mfma_f32_32x32x16_bf16(as_bf(a), as_bf(b), c, 0, 0, 0); }
template <class T> DI LAS T* lds(u32 off) { return (LAS T*)(uintptr_t)off; }
DI v2u tr_read(u32 off) { s16x4 r = __builtin_amdgcn_ds_read_tr16_b64_v4i16(lds<s16x4>(off)); return __builtin_bit_cast(v2u, r); }
DI v4u tr_frag(u32 off0, u32 off1) { v2u a = tr_read(off0), b = tr_read(off1); return v4u{a.x, a.y, b.x, b.y}; }
template <int CTRL, int RM> DI float dpp_add(float v) { int t = __builtin_amdgcn_update_dpp(0, __float_as_int(v), CTRL, RM, 0xf, true); return v + __int_as_float(t); }
DI float wave_sum(float v) {
    v = dpp_add<0x111, 0xf>(v); v = dpp_add<0x112, 0xf>(v); v = dpp_add<0x114, 0xf>(v); v = dpp_add<0x118, 0xf>(v);
    v = dpp_add<0x142, 0xa>(v); v = dpp_add<0x143, 0xc>(v);
    return __int_as_float(__builtin_amdgcn_readlane(__float_as_int(v), 63));
}
DI float half_sum(float v) { const u32 u = __float_as_uint(v); const auto r = __builtin_amdgcn_permlane32_swap(u, u, false, false); return __uint_as_float(r[0]) + __uint_as_float(r[1]); }
DI void fence() { asm volatile("" ::: "memory"); }
DI void bar() { asm volatile("s_waitcnt lgkmcnt(0)" ::: "memory"); __builtin_amdgcn_s_barrier(); asm volatile("" ::: "memory"); }
template <int N> DI void vmw() { asm volatile("s_waitcnt vmcnt(%0)" :: "n"(N) : "memory"); }
template <bool S1> DI void glds16(u32 voff, const void* sbase, u32 ldst) {
    u32 keep;
    if (S1) asm volatile("s_mov_b32 %0, m0\n\ts_mov_b32 m0, %3\n\ts_nop 0\n\tglobal_load_lds_dwordx4 %1, %2 sc1\n\ts_mov_b32 m0, %0" : "=&s"(keep) : "v"(voff), "s"(sbase), "s"(ldst) : "memory");
    else    asm volatile("s_mov_b32 %0, m0\n\ts_mov_b32 m0, %3\n\ts_nop 0\n\tglobal_load_lds_dwordx4 %1, %2\n\ts_mov_b32 m0, %0" : "=&s"(keep) : "v"(voff), "s"(sbase), "s"(ldst) : "memory");
}
DI void glds16v(const void* gaddr, u32 ldst) {
    u32 keep;
    asm volatile("s_mov_b32 %0, m0\n\ts_mov_b32 m0, %2\n\ts_nop 0\n\tglobal_load_lds_dwordx4 %1, off\n\ts_mov_b32 m0, %0" : "=&s"(keep) : "v"(gaddr), "s"(ldst) : "memory");
}
DI float dot2(u32 a, u32 b, float c) { return __builtin_amdgcn_fdot2_f32_bf16(__builtin_bit_cast(bf16x2, a), __builtin_bit_cast(bf16x2, b), c, false); }
DI f2 cmul(f2 a, f2 b) { f2 t = f2{a.x, a.x} * b; return __builtin_elementwise_fma(f2{a.y, a.y}, f2{-b.y, b.x}, t); }
DI f2 cexp_rev(float ar, float ai, float t) {
    const float e = __builtin_amdgcn_exp2f(ar * t), ph = __builtin_amdgcn_fractf(ai * t);
    return f2{e * __builtin_amdgcn_cosf(ph), e * __builtin_amdgcn_sinf(ph)};
}
DI u32 swz_b(u32 row) { return ((row & 3u) << 2) | ((row >> 2) & 3u); }
DI u32 off_b(u32 row, u32 ch) { return 256u * row + 16u * (ch ^ swz_b(row)); }
DI u32 off_a(u32 row, u32 ch) { return 2048u * (row >> 3) + 512u * (ch >> 2) + 64u * (row & 7u) + 16u * ((ch & 3u) ^ ((row >> 2) & 3u)); }
DI u32 max4(v4u v) { return max(max(v.x, v.y), max(v.z, v.w)); }
DI u32 san(u32 v) { return min(v, 0xfffffffeu); }
DI u32 ln_pack(float mean, float var) {
    const float m = rbf(mean), sd = rbf(__builtin_amdgcn_sqrtf(rbf(rbf(var) + 1e-5f)));
    return (__float_as_uint(m) >> 16) | (__float_as_uint(sd) & 0xffff0000u);
}

#define STAMP(i) do { } while (0)
#define CSTAMP(i) do { } while (0)
#define STAMP_DECL
#define STAMP_FLUSH do { } while (0)
#define SGB(mask, n) __builtin_amdgcn_sched_group_barrier(mask, n, 0)
#define SB0() __builtin_amdgcn_sched_barrier(0)
constexpr int M_MFMA = 0x8, M_VMEM = 0x10, M_DSR = 0x100, M_VALU = 0x2;
DI void delay_ticks(u32 n) { const u64 t0 = __builtin_amdgcn_s_memrealtime(); while ((u32)(__builtin_amdgcn_s_memrealtime() - t0) < n) __builtin_amdgcn_s_sleep(1); }
DI u32 opaque(u32 v) { asm volatile("" : "+v"(v)); return v; }

template <int TG> struct Tiles;
template <> struct Tiles<0> { static constexpr int n = 3; static constexpr int t[5] = {7, 6, 2, 0, 0}; };
template <> struct Tiles<1> { static constexpr int n = 5; static constexpr int t[5] = {5, 4, 3, 1, 0}; };

template <int TG> DI void conv_wave(int tid, int w, int lane, int cm, float dsv, __amdgpu_buffer_rsrc_t rsW, v4u (&Wf)[28], u32 wf_off) {
    typedef Tiles<TG> T;
    constexpr int NP = T::t[0] + 1;
    const int ch = w >> 2, rh = (w >> 1) & 1;
    f32x4 acc[T::n];
#pragma unroll
    for (int i = 0; i < T::n; ++i) acc[i] = f32x4{0.f, 0.f, 0.f, 0.f};
    const u32 cb = lane & 15, cq = lane >> 4, cs = (3u - cb) & 3u;
    const u32 kbase = opaque(KC + ch * KC_CH + cs * KC_COPY + 2 * (2047 - 16 * (cq >> 1) - cb + 8 * (cq & 1) - cs) - 32 * 8 * rh - 32 * 118);
    const u32 kbase8 = opaque(kbase + 8);
    const u32 ubase = opaque(UU + ch * UU_CH + 512 + 32 * (cb - (cq >> 1)) + 16 * (cq & 1) - 32 * 8 * rh - 32 * 6);
    v4u Af[2][NP], Bf[2][NP];
    auto ld = [&](int rr, v4u (&A)[NP], v4u (&B)[NP]) {
#pragma unroll
        for (int p = 0; p < NP; ++p) {
            const v2u lo = *lds<volatile v2u>(kbase + 32 * (118 - (16 * p + 2 * rr))), hi = *lds<volatile v2u>(kbase8 + 32 * (118 - (16 * p + 2 * rr)));
            A[p] = v4u{lo.x, lo.y, hi.x, hi.y};
            B[p] = *lds<v4u>(ubase + 32 * (16 * p - 2 * rr + 6));
        }
    };
    ld(0, Af[0], Bf[0]);
#pragma unroll
    for (int rr = 0; rr < 4; ++rr) {
        SB0();
        if (rr < 3) ld(rr + 1, Af[(rr + 1) & 1], Bf[(rr + 1) & 1]);
#pragma unroll
        for (int i = 0; i < 3; ++i) Wf[3 * rr + i] = __builtin_amdgcn_raw_buffer_load_b128(rsW, wf_off + (3 * rr + i) * 1024, 0, WAUX);
#pragma unroll
        for (int p = 0; p < NP; ++p)
#pragma unroll
            for (int i = 0; i < T::n; ++i)
                if (p <= T::t[i]) acc[i] = mfma16(Af[rr & 1][p], Bf[rr & 1][T::t[i] - p], acc[i]);
#pragma unroll
        for (int i = 0; i < 18; ++i) {
            SGB(M_MFMA, 1);
            if (rr < 3) { if (NP == 8 && i < 6) SGB(M_DSR, 2); else SGB(M_DSR, 1); }
            if (i == 3 || i == 9 || i == 15) SGB(M_VMEM, 1);
        }
    }
    SB0();
    CSTAMP(30);
    const u32 mine = CX + w * 3072, other = CX + (w ^ 2) * 3072;
    if (rh == 0) {
#pragma unroll
        for (int i = 1; i < T::n; i += 2) *lds<f32x4>(mine + (i >> 1) * 1024 + lane * 16) = acc[i];
    } else {
#pragma unroll
        for (int i = 0; i < T::n; i += 2) *lds<f32x4>(mine + (i >> 1) * 1024 + lane * 16) = acc[i];
    }
    bar();
    CSTAMP(31);
    auto fin = [&](int i, f32x4 own) {
        const f32x4 y = own + *lds<f32x4>(other + (i >> 1) * 1024 + lane * 16);
        const u32 l0 = 256 * T::t[i] + 16 * cb + 4 * cq;
        const v2u uu = *lds<v2u>(UU + ch * UU_CH + 512 + l0 * 2), xx = *lds<v2u>(XS + ch * 4096 + l0 * 2);
        const float uv[4] = {bflo(uu.x), bfhi(uu.x), bflo(uu.y), bfhi(uu.y)}, xv[4] = {bflo(xx.x), bfhi(xx.x), bflo(xx.y), bfhi(xx.y)};
        float h[4];
#pragma unroll
        for (int r = 0; r < 4; ++r) h[r] = xv[r] + rbf(rbf(y[r]) + rbf(uv[r] * dsv));
        const v2u o2 = {pk2(h[0], h[1]), pk2(h[2], h[3])};
        __builtin_amdgcn_raw_buffer_store_b64(o2, rsW, WS_HT + (2 * cm + ch) * 4096 + l0 * 2, 0, SC1);
    };
    if (rh == 0) {
#pragma unroll
        for (int i = 0; i < T::n; i += 2) fin(i, acc[i]);
    } else {
#pragma unroll
        for (int i = 1; i < T::n; i += 2) fin(i, acc[i]);
    }
}

__global__ void __launch_bounds__(512, 2)
fused_kernel(const u16* __restrict__ x, const u16* __restrict__ Are, const u16* __restrict__ Aim, const u16* __restrict__ Bre, const u16* __restrict__ Bim,
             const u16* __restrict__ Cre, const u16* __restrict__ Cim, const u16* __restrict__ logdt, const u16* __restrict__ Dskip,
             const u16* __restrict__ wg, const u16* __restrict__ wu, const u16* __restrict__ wd, u16* __restrict__ out, char* __restrict__ ws)
{
    const int tid = threadIdx.x, lane = tid & 63, w = __builtin_amdgcn_readfirstlane(tid >> 6);
    const int b = blockIdx.x, g = b & 7, j = b >> 3;
    const bool mainb = j < 28;
    const int cm = mainb ? g * 28 + j : 0;
    const int P = 4 * (g >> 1) + j / 7, S = 7 * (g & 1) + j % 7;
    const __amdgpu_buffer_rsrc_t rsW = __builtin_amdgcn_make_buffer_rsrc(ws, 0, WS_END, 0x00020000);
    const __amdgpu_buffer_rsrc_t rsX = __builtin_amdgcn_make_buffer_rsrc((void*)x, 0, L_ * D_ * 2, 0x00020000);
    const u32 trq = (lane & 15) >> 2, trp = lane & 3, trh = lane >> 5, trb = (lane >> 4) & 1;
    STAMP_DECL
    STAMP(0);
    if (w == 7) {
        const char* p = lane < 24 ? ws + lane * (1u << 20) + 8192 + (b & 31) * 128
                      : lane < 26 ? (const char*)out + (lane - 24) * (1u << 20) + (b & 31) * 128
                      : lane < 28 ? (const char*)wg + (lane - 26) * (1u << 20) + (b & 31) * 128
                      : lane < 30 ? (const char*)wu + (lane - 28) * (1u << 20) + (b & 31) * 128
                      : lane < 32 ? (const char*)wd + (lane - 30) * (1u << 20) + (b & 31) * 128
                      : lane < 34 ? (const char*)x + (lane - 32) * (1u << 20) + (b & 31) * 128 : (const char*)x;
        glds16v(p, 156672);
    }
    {
        u64 pc; asm volatile("s_getpc_b64 %0" : "=s"(pc));
        pc &= ~255ull;
        for (int q = j * 8 + w; q < CODEPF; q += 256) glds16<false>((u32)(q * 1024 + lane * 16), (const void*)pc, 157696);
    }
    const u64 tstart = __builtin_amdgcn_s_memrealtime();
    v4u xr; u64 ep; u32 p_ld2, p_dk2;
    asm volatile("global_load_dwordx4 %0, %1, %2" : "=v"(xr) : "v"((u32)((8 * b + w) * 896 + min(lane, 55) * 16)), "s"(x) : "memory");
    asm volatile("s_load_dwordx2 %0, %3, 0x0\n\ts_load_dword %1, %4, 0x0\n\ts_load_dword %2, %5, 0x0" : "=&s"(ep), "=&s"(p_ld2), "=&s"(p_dk2)
                 : "s"(ws + WS_EP), "s"((const char*)logdt + cm * 4), "s"((const char*)Dskip + cm * 4) : "memory");
    const int ch = w >> 2, dch = 2 * cm + ch;
    if (mainb) {
        const int ai_ = lane >> 3;
        const u16* src = ai_ == 0 ? Are : ai_ == 1 ? Aim : ai_ == 2 ? Bre : ai_ == 3 ? Bim : ai_ == 4 ? Cre : Cim;
        glds16v(src + dch * N_ + (w & 3) * 64 + (lane & 7) * 8, PRM + w * 1024);
        const int ew = w < 6 ? w : w - 6;
        glds16<false>((u32)(ew * 1024 + lane * 16), (const void*)&EO, EOL + ew * 1024);
        const int bb_ = b < 196 ? b : b - 196;
#pragma unroll
        for (int i = 0; i < 2; ++i) {
            const int id = 2 * bb_ + i, Sp = id / 28, s = id % 28;
            const u32 row = (w & 3) * 4 + (lane >> 4), chk = (lane & 15) ^ swz_b(row);
            glds16<false>((u32)((16 * s + row) * (F_ * 2) + Sp * 256 + chk * 16), (w >> 2) ? (const void*)wu : (const void*)wg, RT + i * 8192 + w * 1024);
        }
    } else {
        const int hb = (j - 28) * 8 + g;
#pragma unroll
        for (int k = 0; k < 7; ++k) {
            const int gid = min(hb + 32 * k, 195);
            const int dt_ = gid / 28, fg = gid % 28;
            const u32 row = 8 * w + (lane >> 3), chk = (lane & 7) ^ (((row >> 1) & 1) << 2);
            glds16<false>((u32)((64 * fg + row) * (D_ * 2) + dt_ * 128 + chk * 16), (const void*)wd, k * 8192 + w * 1024);
        }
    }
    asm volatile("s_cmp_ge_i32 %1, 28\n\ts_cbranch_scc1 1f\n\ts_waitcnt vmcnt(4)\n\ts_branch 2f\n1:\n\ts_waitcnt vmcnt(7)\n2:" : "+v"(xr) : "s"(__builtin_amdgcn_readfirstlane(j)) : "memory", "scc");
    asm volatile("s_waitcnt lgkmcnt(0)" : "+s"(ep), "+s"(p_ld2), "+s"(p_dk2) :: "memory");
    ep += 1ull;
    const u32 p_ldt = ch ? p_ld2 >> 16 : p_ld2 & 0xffffu, p_dsk = ch ? p_dk2 >> 16 : p_dk2 & 0xffffu;
    if (lane >= 56) xr = v4u{0, 0, 0, 0};
    const u32 tag = (u32)ep, tagx = (u32)(ep >> 32) << 16;
    if (lane < 56) *lds<v4u>(XT + w * 912 + lane * 16) = xr;
    {
        const float v[8] = {bflo(xr.x), bfhi(xr.x), bflo(xr.y), bfhi(xr.y), bflo(xr.z), bfhi(xr.z), bflo(xr.w), bfhi(xr.w)};
        const float mean = wave_sum(((v[0] + v[1]) + (v[2] + v[3])) + ((v[4] + v[5]) + (v[6] + v[7]))) * (1.f / 448.f);
        float q = 0.f;
#pragma unroll
        for (int e = 0; e < 8; ++e) { const float dl = v[e] - mean; q += dl * dl; }
        q = lane < 56 ? q : 0.f;
        const u32 ln1 = ln_pack(mean, wave_sum(q) * (1.f / 448.f));
        if (lane == 0) __hip_atomic_store((GAS u64*)(ws + WS_SG) + (8 * b + w), ((u64)tag << 32) | ln1, RLX_AGENT);
    }
    STAMP(1);
    const u32 km0 = FM + ch * FM_CH + (w & 3) * 256;
    f2 Ga, Gb, Gc, Gd, KX, KY, G4, G4r, E1, E1r, E256, E256r, E2, E2r, E4, E4r;
#define M1(a, bb) (f2{(a).x, (a).x} * (bb))
#define M2(a, br, t) __builtin_elementwise_fma(f2{(a).y, (a).y}, (br), (t))
#define WROW1(r, v) do { u32 keep_; asm volatile("s_mov_b32 %0, m0\n\ts_mov_b32 m0, %2\n\ts_nop 0\n\tds_write_addtid_b32 %1 offset:%3\n\ts_mov_b32 m0, %0" : "=&s"(keep_) : "v"(v), "s"(km0), "n"((r) * FM_ROW) : "memory"); } while (0)
#define WROW6(r0, v0, r1, v1, r2, v2, r3, v3, r4, v4, r5, v5) do { u32 keep_; asm volatile("s_mov_b32 %0, m0\n\ts_mov_b32 m0, %7\n\ts_nop 0\n\t" \
        "ds_write_addtid_b32 %1 offset:%8\n\tds_write_addtid_b32 %2 offset:%9\n\tds_write_addtid_b32 %3 offset:%10\n\tds_write_addtid_b32 %4 offset:%11\n\tds_write_addtid_b32 %5 offset:%12\n\tds_write_addtid_b32 %6 offset:%13\n\ts_mov_b32 m0, %0" \
        : "=&s"(keep_) : "v"(v0), "v"(v1), "v"(v2), "v"(v3), "v"(v4), "v"(v5), "s"(km0), "n"((r0) * FM_ROW), "n"((r1) * FM_ROW), "n"((r2) * FM_ROW), "n"((r3) * FM_ROW), "n"((r4) * FM_ROW), "n"((r5) * FM_ROW) : "memory"); } while (0)
#define WA(r, X) WROW1(r, pk2((X).x, -(X).y))
#define KSTEP(t) do { SB0(); \
        const f2 ta = M1(Ga, G4), tb = M1(Gb, G4), tc = M1(Gc, G4), td_ = M1(Gd, G4), tx = M1(KX, E1), ty = M1(KY, E256); \
        const u32 c0 = pk2(Ga.x, Ga.y), c1 = pk2(Gb.x, Gb.y), c2 = pk2(Gc.x, Gc.y), c3 = pk2(Gd.x, Gd.y), c4 = pk2(KX.x, -KX.y), c5 = pk2(KY.x, -KY.y); \
        f2 ts0, ts1; \
        if ((t) == 0) ts0 = M1(KY, E2); \
        if ((t) == 1 || (t) == 2) ts0 = M1(KY, E4); \
        SB0(); \
        WROW6(21 + 4 * (t), c0, 22 + 4 * (t), c1, 23 + 4 * (t), c2, 24 + 4 * (t), c3, (t), c4, ((t) == 0 ? 8 : (t) == 1 ? 12 : (t) == 2 ? 14 : 13 + (t)), c5); \
        if ((t) == 0) { f2 Z = M2(KY, E2r, ts0); WA(9, Z); ts1 = M1(Z, E2); Z = M2(Z, E2r, ts1); WA(10, Z); ts1 = M1(Z, E2); Z = M2(Z, E2r, ts1); WA(11, Z); } \
        if ((t) == 1) { const f2 V = M2(KY, E4r, ts0); WA(13, V); } \
        if ((t) == 2) { const f2 V = M2(KY, E4r, ts0); WA(15, V); } \
        if ((t) < 7) { Ga = M2(Ga, G4r, ta); Gb = M2(Gb, G4r, tb); Gc = M2(Gc, G4r, tc); Gd = M2(Gd, G4r, td_); KX = M2(KX, E1r, tx); KY = M2(KY, E256r, ty); } \
    } while (0)
    if (mainb) {
        vmw<4>();
        const u32 pa_ = PRM + w * 1024 + lane * 2;
        const u32 p_are = *lds<u16>(pa_), p_aim = *lds<u16>(pa_ + 128), p_bre = *lds<u16>(pa_ + 256), p_bim = *lds<u16>(pa_ + 384), p_cre = *lds<u16>(pa_ + 512), p_cim = *lds<u16>(pa_ + 640);
        const float dt = rbf(expf(bflo(p_ldt)));
        const float zr = dt * bflo(p_are), zi = dt * bflo(p_aim);
        const float ar = zr * 1.4426950408889634f, ai = zi * 0.15915494309189535f;
        const f2 CB = cmul(f2{bflo(p_cre), bflo(p_cim)}, f2{dt * bflo(p_bre), dt * bflo(p_bim)});
        E1 = cexp_rev(ar, ai, 1.f); E256 = cexp_rev(ar, ai, 256.f);
        const f2 G1 = cexp_rev(ar, ai, 8.f);
        E2 = cmul(E1, E1); E4 = cmul(E2, E2);
        const f2 G2 = cmul(G1, G1);
        G4 = cmul(G2, G2);
        E1r = f2{-E1.y, E1.x}; E2r = f2{-E2.y, E2.x}; E4r = f2{-E4.y, E4.x}; E256r = f2{-E256.y, E256.x}; G4r = f2{-G4.y, G4.x};
        Ga = f2{1.f, 0.f}; Gb = G1; Gc = G2; Gd = cmul(G2, G1);
        KX = CB; KY = cmul(CB, E256);
        SB0();
    }
    bar();
    STAMP(16);
    auto publish_xg = [&]() {
        u32 x0[4], x1[4];
#pragma unroll
        for (int k = 0; k < 4; ++k) {
            const int id = min(tid + 512 * k, 1791), c = id >> 2, i2 = (id & 3) * 2;
            x0[k] = *lds<u16>(XT + i2 * 912 + c * 2); x1[k] = *lds<u16>(XT + (i2 + 1) * 912 + c * 2);
        }
        SB0();
#pragma unroll
        for (int k = 0; k < 4; ++k)
            __builtin_amdgcn_raw_buffer_store_b128(v4u{x0[k] | tagx, tag, x1[k] | tagx, tag}, rsW, (tid + 512 * k < 1792) ? (u32)(WS_XG + b * 28672 + (tid + 512 * k) * 16) : 0x7ffffff0u, 0, SC1);
    };
    if (mainb) {
    publish_xg();
    STAMP(17);
    KSTEP(0); KSTEP(1); KSTEP(2);
    SB0();
    vmw<5>();
    bar();
    if (b < 196) {
#pragma unroll
        for (int i = 0; i < 2; ++i) {
            const int id = 2 * b + i, Sp = id / 28, s = id % 28;
            const u32 base = RT + i * 8192 + trb * 4096 + 8 * (trp & 1);
            const v4u f = tr_frag(base + off_b(8 * trh + trq, 2 * w + (trp >> 1)), base + off_b(8 * trh + 4 + trq, 2 * w + (trp >> 1)));
            __builtin_amdgcn_raw_buffer_store_b128(f, rsW, WS_WG + ((Sp * 8 + w) * 28 + s) * 1024 + lane * 16, 0, SC1);
        }
    }
    fence();
    STAMP(2);
    {
        KSTEP(3); KSTEP(4); KSTEP(5); KSTEP(6); KSTEP(7);
        SB0();
#undef KSTEP
#undef WA
#undef WROW1
#undef WROW6
#undef M1
#undef M2
        STAMP(3);
        vmw<0>();
        bar();
        if (tid == 0 && b < 196) __hip_atomic_store((GAS u32*)(ws + WS_WR + b * 64), tag, RLX_AGENT);
        const int ta_ = (w >> 1) & 1, tb_ = w & 1;
        const u32 fa_ = opaque(FM + ch * FM_CH + (16 * ta_ + (lane & 15)) * FM_ROW + (lane >> 4) * 16), fb_ = opaque(FM + ch * FM_CH + (21 + 16 * tb_ + (lane & 15)) * FM_ROW + (lane >> 4) * 16);
        f32x4 f0 = {0.f, 0.f, 0.f, 0.f}, f1 = {0.f, 0.f, 0.f, 0.f};
        v4u A[16], B[16];
#pragma unroll
        for (int ks = 0; ks < 16; ++ks) { A[ks] = *lds<v4u>(fa_ + ks * 64); B[ks] = *lds<v4u>(fb_ + ks * 64); }
        SB0();
#pragma unroll
        for (int ks = 0; ks < 16; ks += 2) { f0 = mfma16(A[ks], B[ks], f0); f1 = mfma16(A[ks + 1], B[ks + 1], f1); }
        f0 += f1;
#pragma unroll
        for (int r = 0; r < 4; ++r) { const int row = 16 * ta_ + 4 * (lane >> 4) + r; if (row < 24) *lds<float>(FT + ch * FT_CH + (row * 32 + 16 * tb_ + (lane & 15)) * 4) = f0[r]; }
        if ((tid & 255) == 255) *lds<float>(FT + ch * FT_CH + 768 * 4) = 0.f;
    }
    STAMP(4);
    {
        v4u xg[4], sg[4];
        const u32 fa = WS_WR + (tid < 196 ? tid : 0) * 64;
        const u32 xa = WS_XG + (32 * w + (lane >> 3)) * 28672 + cm * 128 + (lane & 7) * 16, sa = WS_SG + (32 * w + (lane >> 3)) * 64 + (lane & 3) * 16;
        if (DLY1) { while ((u32)(__builtin_amdgcn_s_memrealtime() - tstart) < DLY1) __builtin_amdgcn_s_sleep(1); }
        for (u32 spin = 0;; ++spin) {
#pragma unroll
            for (int k = 0; k < 4; ++k) { xg[k] = __builtin_amdgcn_raw_buffer_load_b128(rsW, xa + k * 8 * 28672, 0, SC1); sg[k] = __builtin_amdgcn_raw_buffer_load_b128(rsW, sa + k * 512, 0, SC1); }
            const u32 fl = __builtin_amdgcn_raw_buffer_load_b32(rsW, fa, 0, SC1);
            bool ok = fl == tag;
#pragma unroll
            for (int k = 0; k < 4; ++k) ok = ok && sg[k].y == tag && sg[k].w == tag && xg[k].y == tag && xg[k].w == tag && (xg[k].x & 0xffff0000u) == tagx && (xg[k].z & 0xffff0000u) == tagx;
            if (__all(ok) || spin > SPIN_CAP) break;
            __builtin_amdgcn_s_sleep(8);
        }
        STAMP(5);
        const int c = (lane >> 2) & 1;
#pragma unroll
        for (int k = 0; k < 4; ++k) {
            const u32 l0 = 8 * (32 * w + 8 * k + (lane >> 3)) + 2 * (lane & 3);
            const float u0 = rbf(bflo(xg[k].x) - bflo(sg[k].x)) * __builtin_amdgcn_rcpf(bfhi(sg[k].x));
            const float u1 = rbf(bflo(xg[k].z) - bflo(sg[k].z)) * __builtin_amdgcn_rcpf(bfhi(sg[k].z));
            *lds<u32>(UU + c * UU_CH + 512 + l0 * 2) = pk2(u0, u1);
            *lds<u32>(XS + c * 4096 + l0 * 2) = (xg[k].x & 0xffffu) | (xg[k].z << 16);
        }
    }
    if (tid >= 64 && tid < 128) *lds<v4u>(UU + ((tid >> 5) & 1) * UU_CH + (tid & 31) * 16) = v4u{0, 0, 0, 0};
    bar();
    STAMP(18);
    {
        const v2u ea = *lds<v2u>(EOL + (tid & 255) * 24), eb = *lds<v2u>(EOL + (tid & 255) * 24 + 8), ec = *lds<v2u>(EOL + (tid & 255) * 24 + 16);
        const v4u eo0 = {ea.x, ea.y, eb.x, eb.y}; const v2u eo1 = ec;
        const u32 eo[11] = {eo0.x & 0xffffu, eo0.x >> 16, eo0.y & 0xffffu, eo0.y >> 16, eo0.z & 0xffffu, eo0.z >> 16, eo0.w & 0xffffu, eo0.w >> 16, eo1.x & 0xffffu, eo1.x >> 16, eo1.y & 0xffffu};
        float v[11];
#pragma unroll
        for (int e = 0; e < 11; ++e) v[e] = *lds<float>(FT + ch * FT_CH + eo[e]);
        SB0();
        u32 p[10];
#pragma unroll
        for (int e = 0; e < 10; ++e) p[e] = pk2(v[e], v[e + 1]);
#pragma unroll
        for (int s = 0; s < 4; ++s) *lds<v4u>(KC + ch * KC_CH + s * KC_COPY + (tid & 255) * 16) = v4u{p[s], p[s + 2], p[s + 4], p[s + 6]};
    }
    if (tid < 16) *lds<v4u>(KC + (tid >> 3) * KC_CH + ((tid >> 1) & 3) * KC_COPY + 4096 + (tid & 1) * 16) = v4u{0, 0, 0, 0};
    bar();
    STAMP(6);
    CSTAMP(26);
    v4u Wf[28];
    const u32 wf_off = WS_WG + (S * 8 + w) * 28 * 1024 + lane * 16;
    {

        if (w & 1) conv_wave<1>(tid, w, lane, cm, bflo(p_dsk), rsW, Wf, wf_off); else conv_wave<0>(tid, w, lane, cm, bflo(p_dsk), rsW, Wf, wf_off);
    }
    STAMP(7);
    CSTAMP(27);
    const int l0p = 128 * P;
    vmw<0>();
    bar();
    {
        if (tid < 8) __hip_atomic_store((GAS u32*)(ws + WS_F2 + tid * 16384 + cm * 64), tag, RLX_AGENT);
        fence();
#pragma unroll
        for (int i = 12; i < 28; ++i) Wf[i] = __builtin_amdgcn_raw_buffer_load_b128(rsW, wf_off + i * 1024, 0, WAUX);
        fence();
        delay_ticks(DLY2);
        if (w < 4) {
            const u32 pa = tid < 224 ? (u32)(WS_F2 + g * 16384 + tid * 64) : (u32)(WS_WF + (tid - 224) * 64);
            for (u32 spin = 0;; ++spin) {
                const u32 s0 = __builtin_amdgcn_raw_buffer_load_b32(rsW, pa, 0, SC1);
                if (__all(s0 == tag) || spin > SPIN_CAP) break;
                __builtin_amdgcn_s_sleep(8);
            }
        }
    }
    bar();
    STAMP(8);
    if (b == 0 && tid == 0) __hip_atomic_store((GAS u64*)(ws + WS_EP), ep, RLX_AGENT);
    const u32 bt_src = (8 * (w >> 1) + ((lane >> 2) & 7)) * 4096 + l0p * 2 + (4 * (2 * (w & 1) + (lane >> 5)) + ((lane & 3) ^ ((2 * (w >> 1) + ((lane >> 4) & 1)) & 3))) * 16;
    const u32 bt_dst = BT + (w >> 1) * 2048 + (w & 1) * 1024;
    auto bt_piece = [&](int k) { glds16<BTSC1>(bt_src + k * 32 * 4096, ws + WS_HT, bt_dst + k * 8192); };
    const float csum = __uint_as_float(__builtin_amdgcn_raw_buffer_load_b32(rsW, WS_CS + (((lane >> 4) & 1) * 1792 + 128 * S + 16 * w + (lane & 15)) * 4, 0, SC1));
    fence();
#pragma unroll
    for (int k = 0; k < 6; ++k) bt_piece(k);
    vmw<4>();
    bar();
    STAMP(9);
    CSTAMP(24);
    f32x16 acc[4];
    v4u Wd[28];
    const int td = w & 1, kq = w >> 1, hp = S / 7, dtile = S % 7;
    const u32 wd_off = WS_WD + ((dtile * 2 + td) * 112 + 28 * kq) * 1024 + lane * 16;
    u32 bb[2];
#pragma unroll
    for (int tt = 0; tt < 2; ++tt) bb[tt] = opaque(BT + 2048 * trh + 64 * (4 * tt + trq) + 16 * ((2 * trb + (trp >> 1)) ^ ((2 * trh + tt) & 3)) + 8 * (trp & 1));
    {
#pragma unroll
        for (int t = 0; t < 4; ++t)
#pragma unroll
            for (int i = 0; i < 16; ++i) acc[t][i] = 0.f;
        v4u Bf[2][4], Of;
        float sa0 = 0.f, sa1 = 0.f, sq0 = 0.f, sq1 = 0.f;
        const u32 bh0 = opaque(bb[0] + 65536), bh1 = opaque(bb[1] + 65536);
        const u32 so0 = opaque(bb[0] + (w >> 2) * 4096 + (w & 3) * 512), so1 = opaque(bb[1] + (w >> 2) * 4096 + (w & 3) * 512);
        const u32 sh0 = opaque(so0 + 65536), sh1 = opaque(so1 + 65536);
#pragma unroll
        for (int t = 0; t < 4; ++t) Bf[0][t] = tr_frag(bb[0] + 512 * t, bb[1] + 512 * t);
#pragma unroll
        for (int s = 0; s < 28; ++s) {
            SB0();
            if (s == 3) { vmw<6>(); bar(); }
            if (s == 11) { vmw<7>(); bar(); }
            SB0();
            if (s + 1 < 28) {
#pragma unroll
                for (int t = 0; t < 4; ++t)
                    Bf[(s + 1) & 1][t] = (s + 1 < 16) ? tr_frag(bb[0] + (s + 1) * 4096 + 512 * t, bb[1] + (s + 1) * 4096 + 512 * t)
                                                       : tr_frag(bh0 + (s + 1 - 16) * 4096 + 512 * t, bh1 + (s + 1 - 16) * 4096 + 512 * t);
            }
            if ((s & 1) == 0) Of = (s < 16) ? tr_frag(so0 + (s >> 1) * 8192, so1 + (s >> 1) * 8192) : tr_frag(sh0 + ((s - 16) >> 1) * 8192, sh1 + ((s - 16) >> 1) * 8192);
            if (s < 4) { bt_piece(6 + 2 * s); bt_piece(7 + 2 * s); }
            else Wd[s - 4] = __builtin_amdgcn_raw_buffer_load_b128(rsW, wd_off + (s - 4) * 1024, 0, WAUX);
#pragma unroll
            for (int t = 0; t < 4; ++t) acc[t] = mfma32(Wf[s], Bf[s & 1][t], acc[t]);
            if (s & 1) {
#pragma unroll
                for (int e = 0; e < 4; e += 2) {
                    const float l0 = bflo(Of[e]), h0 = bfhi(Of[e]), l1 = bflo(Of[e + 1]), h1 = bfhi(Of[e + 1]);
                    sa0 += l0; sq0 = __builtin_fmaf(l0, l0, sq0); sa1 += l1; sq1 = __builtin_fmaf(l1, l1, sq1);
                    sa0 += h0; sq0 = __builtin_fmaf(h0, h0, sq0); sa1 += h1; sq1 = __builtin_fmaf(h1, h1, sq1);
                }
            }
            if (s >= 4) {
                if (s & 1) { SGB(M_MFMA, 1); SGB(M_DSR, 2); SGB(M_VALU, 6); SGB(M_MFMA, 1); SGB(M_DSR, 2); SGB(M_VALU, 6); SGB(M_VMEM, 1); SGB(M_MFMA, 1); SGB(M_DSR, 2); SGB(M_VALU, 6); SGB(M_MFMA, 1); SGB(M_DSR, 2); SGB(M_VALU, 6); }
                else { SGB(M_MFMA, 1); SGB(M_DSR, 3); SGB(M_MFMA, 1); SGB(M_DSR, 3); SGB(M_VMEM, 1); SGB(M_MFMA, 1); SGB(M_DSR, 2); SGB(M_MFMA, 1); SGB(M_DSR, 2); }
            }
        }
        float sumh = sa0 + sa1, sq = sq0 + sq1;

        SB0();
    #pragma unroll
        for (int s = 24; s < 28; ++s) Wd[s] = __builtin_amdgcn_raw_buffer_load_b128(rsW, wd_off + s * 1024, 0, WAUX);

        sumh = half_sum(sumh); sq = half_sum(sq);
        if (lane < 32) { *lds<float>(SP + w * 128 + lane * 4) = sumh; *lds<float>(SQ + w * 128 + lane * 4) = sq; *lds<float>(CSL + w * 128 + lane * 4) = csum; }
    }
    STAMP(10);
    CSTAMP(25);
    bar();
    {
        f32x4 csg[2], csu[2];
#pragma unroll
        for (int q = 0; q < 2; ++q) { csg[q] = *lds<f32x4>(CSL + w * 128 + (8 * q + 4 * trh) * 4); csu[q] = *lds<f32x4>(CSL + w * 128 + 64 + (8 * q + 4 * trh) * 4); }
#pragma unroll
        for (int t = 0; t < 4; ++t) {
            const float m = rbf((*lds<float>(SP + t * 128 + (lane & 31) * 4) + *lds<float>(SP + (t + 4) * 128 + (lane & 31) * 4)) * (1.f / 448.f));
            float hv[8];
#pragma unroll
            for (int e = 0; e < 8; ++e) hv[e] = (acc[t][e] - m * csg[e >> 2][e & 3]) * (acc[t][e + 8] - m * csu[e >> 2][e & 3]);
            const v4u o = {pk2(hv[0], hv[1]), pk2(hv[2], hv[3]), pk2(hv[4], hv[5]), pk2(hv[6], hv[7])};
            __builtin_amdgcn_raw_buffer_store_b128(o, rsW, WS_HH + ((P * 112 + 8 * S + w) * 4 + t) * 1024 + lane * 16, 0, SC1);
        }
    }
    STAMP(11);
    const int tl_f = kq >> 1;
    const u32 lp = 64 * hp + 32 * tl_f + (lane & 31);
    float hres[8], rs;
    {
        const int tq = 2 * hp + tl_f;
        const float s1 = *lds<float>(SP + tq * 128 + (lane & 31) * 4) + *lds<float>(SP + (tq + 4) * 128 + (lane & 31) * 4);
        const float s2 = *lds<float>(SQ + tq * 128 + (lane & 31) * 4) + *lds<float>(SQ + (tq + 4) * 128 + (lane & 31) * 4);
        const float mean = s1 * (1.f / 448.f), var = fmaxf(s2 * (1.f / 448.f) - mean * mean, 0.f);
        const float sd = bfhi(ln_pack(mean, var));
        rs = 1.f / (sd * sd);
#pragma unroll
        for (int e = 0; e < 8; ++e) {
            const u32 row = 64 * dtile + 32 * td + 16 * (kq & 1) + 8 * (e >> 2) + 4 * trh + (e & 3);
            hres[e] = bflo(*lds<u16>(BT + off_a(row, lp >> 3) + (lp & 7) * 2));
        }
    }
    vmw<0>();
    bar();
    if (tid == 0) __hip_atomic_store((GAS u32*)(ws + WS_F3 + (P * 16 + S) * 64), tag, RLX_AGENT);
    const u32 hbase = WS_HH + ((P * 112 + 28 * kq) * 4 + 2 * hp) * 1024;
    if (w == 0) {
        delay_ticks(DLY3);
        const u32 pa = WS_F3 + (P * 16 + (lane < 14 ? lane : 0)) * 64;
        for (u32 spin = 0;; ++spin) {
            const u32 s0 = __builtin_amdgcn_raw_buffer_load_b32(rsW, pa, 0, SC1);
            if (__all(s0 == tag) || spin > SPIN_CAP) break;
            __builtin_amdgcn_s_sleep(8);
        }
    }
    bar();
    STAMP(12);
    const u32 hsrc = td * 1024 + lane * 16, hdst = RING + kq * 2048 + td * 1024, hrd = opaque(RING + kq * 2048 + lane * 16);
    auto hh_dma = [&](int s) { glds16<true>(hsrc + s * 4096, ws + hbase, hdst + (s % RING_SLOTS) * 8192); };
    f32x16 o2[2];
    {
#pragma unroll
        for (int i = 0; i < 16; ++i) { o2[0][i] = 0.f; o2[1][i] = 0.f; }
        constexpr int AH = 9;
#pragma unroll
        for (int s = 0; s < AH; ++s) hh_dma(s);
        v4u hf[2][2];
        vmw<AH - 4>(); bar();
        STAMP(13); CSTAMP(28);
#pragma unroll
        for (int tl = 0; tl < 2; ++tl) hf[0][tl] = *lds<v4u>(hrd + tl * 1024);
#pragma unroll
        for (int s = 0; s < 28; ++s) {
            SB0();
            if ((s & 3) == 3 && s + 1 < 28) {
                const int issued = (s - 1 + AH < 27) ? s - 1 + AH : 27, need = (s + 4 < 27) ? s + 4 : 27;
                switch (issued - need) { case 0: vmw<0>(); break; case 1: vmw<1>(); break; case 2: vmw<2>(); break; case 3: vmw<3>(); break; default: vmw<4>(); break; }
                bar();
                SB0();
            }
            if (s + 1 < 28) {
#pragma unroll
                for (int tl = 0; tl < 2; ++tl) hf[(s + 1) & 1][tl] = *lds<v4u>(hrd + ((s + 1) % RING_SLOTS) * 8192 + tl * 1024);
            }
            if (s + AH < 28) hh_dma(s + AH);
#pragma unroll
            for (int tl = 0; tl < 2; ++tl) o2[tl] = mfma32(Wd[s], hf[s & 1][tl], o2[tl]);
        }
        SB0();
        bar();
    }
    STAMP(14);
    CSTAMP(29);
    {
#pragma unroll
        for (int k2 = 0; k2 < 4; ++k2) {
            if (k2 != kq) {
                const int si = (kq > k2) ? kq - 1 : kq;
                const f32x16& a = o2[k2 >> 1];
#pragma unroll
                for (int q = 0; q < 2; ++q) { const int r0 = 8 * (k2 & 1) + 4 * q; *lds<f32x4>(((td * 4 + k2) * 3 + si) * 2048 + q * 1024 + lane * 16) = f32x4{a[r0], a[r0 + 1], a[r0 + 2], a[r0 + 3]}; }
            }
        }
        bar();
        f32x4 v[2];
#pragma unroll
        for (int q = 0; q < 2; ++q) {
            if (kq == 0) v[q] = f32x4{o2[0][4 * q], o2[0][4 * q + 1], o2[0][4 * q + 2], o2[0][4 * q + 3]};
            else if (kq == 1) v[q] = f32x4{o2[0][8 + 4 * q], o2[0][9 + 4 * q], o2[0][10 + 4 * q], o2[0][11 + 4 * q]};
            else if (kq == 2) v[q] = f32x4{o2[1][4 * q], o2[1][4 * q + 1], o2[1][4 * q + 2], o2[1][4 * q + 3]};
            else v[q] = f32x4{o2[1][8 + 4 * q], o2[1][9 + 4 * q], o2[1][10 + 4 * q], o2[1][11 + 4 * q]};
#pragma unroll
            for (int si = 0; si < 3; ++si) v[q] += *lds<f32x4>(((td * 4 + kq) * 3 + si) * 2048 + q * 1024 + lane * 16);
            const u32 d0 = 64 * dtile + 32 * td + 16 * (kq & 1) + 8 * q + 4 * trh;
            float r[4];
#pragma unroll
            for (int e = 0; e < 4; ++e) r[e] = hres[4 * q + e] + rbf(v[q][e] * rs);
            *(GAS v2u*)(out + (size_t)(l0p + lp) * D_ + d0) = v2u{pk2(r[0], r[1]), pk2(r[2], r[3])};
        }
    }
    STAMP(15);
    STAMP_FLUSH;
    } else {
        publish_xg();
        const int hb = (j - 28) * 8 + g;
        float cs[8];
        {
            const u16* wsrc = hb < 16 ? wg : wu;
            const int c0 = 112 * (hb & 15) + 8 * (tid % 14), r0 = tid / 14;
#pragma unroll
            for (int e = 0; e < 8; ++e) cs[e] = 0.f;
            v4u t[13];
#pragma unroll
            for (int i = 0; i < 13; ++i) { const int r = r0 + 36 * i; t[i] = (tid < 504 && r < 448) ? *(const v4u*)(wsrc + (size_t)r * F_ + c0) : v4u{0, 0, 0, 0}; }
#pragma unroll
            for (int i = 0; i < 13; ++i) { cs[0] += bflo(t[i].x); cs[1] += bfhi(t[i].x); cs[2] += bflo(t[i].y); cs[3] += bfhi(t[i].y); cs[4] += bflo(t[i].z); cs[5] += bfhi(t[i].z); cs[6] += bflo(t[i].w); cs[7] += bfhi(t[i].w); }
        }
        vmw<0>(); bar();
#pragma unroll
        for (int k = 0; k < 7; ++k) {
            const int gid = hb + 32 * k;
            if (gid < 196) {
                const int dt_ = gid / 28, fg = gid % 28, fsl = w >> 1, td = w & 1;
                u32 ad[2];
#pragma unroll
                for (int t = 0; t < 2; ++t) { const u32 row = 16 * fsl + 8 * t + 4 * trh + trq, chk = 4 * td + 2 * trb + (trp >> 1); ad[t] = k * 8192 + row * 128 + 16 * (chk ^ (((row >> 1) & 1) << 2)) + 8 * (trp & 1); }
                const v4u f = tr_frag(ad[0], ad[1]);
                __builtin_amdgcn_raw_buffer_store_b128(f, rsW, WS_WD + ((dt_ * 2 + td) * 112 + 4 * fg + fsl) * 1024 + lane * 16, 0, SC1);
            }
        }
        if (tid < 504) {
            *lds<f32x4>(65536 + tid * 32) = f32x4{cs[0], cs[1], cs[2], cs[3]};
            *lds<f32x4>(65536 + tid * 32 + 16) = f32x4{cs[4], cs[5], cs[6], cs[7]};
        }
        bar();
        if (tid < 112) {
            float s = 0.f;
            for (int r = 0; r < 36; ++r) s += *lds<float>(65536 + (r * 14 + (tid >> 3)) * 32 + (tid & 7) * 4);
            __hip_atomic_store((GAS u32*)(ws + WS_CS) + ((hb < 16 ? 0 : 1792) + 112 * (hb & 15) + tid), __float_as_uint(s), RLX_AGENT);
        }
        vmw<0>(); bar();
        if (tid == 0) __hip_atomic_store((GAS u32*)(ws + WS_WF + hb * 64), tag, RLX_AGENT);

    }
}

extern "C" void kernel_launch(void* const* d_in, const int* in_sizes, int n_in, void* d_out, int out_size, void* d_ws, size_t ws_size, hipStream_t stream) {
    (void)in_sizes; (void)n_in; (void)out_size; (void)ws_size;
    (void)hipFuncSetAttribute((const void*)fused_kernel, hipFuncAttributeMaxDynamicSharedMemorySize, LDS_BYTES);
    fused_kernel<<<256, 512, LDS_BYTES, stream>>>((const u16*)d_in[0], (const u16*)d_in[1], (const u16*)d_in[2], (const u16*)d_in[3], (const u16*)d_in[4],
        (const u16*)d_in[5], (const u16*)d_in[6], (const u16*)d_in[7], (const u16*)d_in[8], (const u16*)d_in[10], (const u16*)d_in[11], (const u16*)d_in[12],
        (u16*)d_out, (char*)d_ws);
}
```

```cpp
#include <hip/hip_runtime.h>
#include <stdint.h>

typedef unsigned short u16;
typedef unsigned u32;
typedef unsigned long long u64;
typedef u32 v4u __attribute__((ext_vector_type(4)));
typedef u32 v2u __attribute__((ext_vector_type(2)));
typedef float f32x4 __attribute__((ext_vector_type(4)));
typedef float f32x16 __attribute__((ext_vector_type(16)));
typedef float f2 __attribute__((ext_vector_type(2)));
typedef __bf16 bf16x8 __attribute__((ext_vector_type(8)));
typedef __bf16 bf16x2 __attribute__((ext_vector_type(2)));
typedef short s16x4 __attribute__((ext_vector_type(4)));
#define LAS __attribute__((address_space(3)))
#define GAS __attribute__((address_space(1)))
#define DI __device__ __forceinline__
#define RLX_AGENT __ATOMIC_RELAXED, __HIP_MEMORY_SCOPE_AGENT
#define CODEPF 28
#define DIAG 0
#define DIAGB 100
#define SC1 16
#define BTSC1 true
#define DLY1 0
#define DLY2 35
#define DLY3 35
#define WAUX 16

constexpr int L_ = 2048, D_ = 448, N_ = 256, F_ = 1792;
constexpr u32 WS_EP = 0;
constexpr u32 WS_SG = 4096;
constexpr u32 WS_WF = 32768;
constexpr u32 WS_CS = 40960;
constexpr u32 WS_WR = 1966080;
constexpr u32 WS_HT = 65536;
constexpr u32 WS_HH = 2u << 20;
constexpr u32 WS_WG = 10u << 20;
constexpr u32 WS_WD = 14u << 20;
constexpr u32 WS_XG = 16u << 20;
constexpr u32 WS_F2 = (23u << 20) + (512u << 10);
constexpr u32 WS_F3 = WS_F2 + 8 * 16384;
constexpr u32 WS_END = 24u << 20;
constexpr u32 WS_DBG = 24u << 20;
constexpr u32 POISON = 0xFFFFFFFFu;
constexpr u32 SPIN_CAP = 1u << 16;

constexpr u32 LDS_BYTES = 163840;
constexpr u32 FM = 0, FM_ROW = 1056, FM_CH = 53 * FM_ROW;
constexpr u32 FT = 2 * FM_CH, FT_CH = 772 * 4;
constexpr u32 EOL = FT + 2 * FT_CH;
constexpr u32 RT = 124288;
constexpr u32 PRM = RT + 16384;
constexpr u32 XT = PRM + 8192;
constexpr u32 KC = 0, KC_COPY = 4160, KC_CH = 4 * KC_COPY;
constexpr u32 CX = 2 * KC_CH;
constexpr u32 UU = RT, UU_CH = (256 + 2048) * 2;
constexpr u32 XS = UU + 2 * UU_CH;
static_assert(EOL % 16 == 0 && EOL + 6144 <= RT && XS + 8192 <= XT && XT + 8 * 912 <= 158848 && CX + 8 * 3072 <= FT, "LDS map");
constexpr u32 BT = 0, BT_BYTES = 448 * 256;
constexpr u32 RING = 0, RING_SLOTS = 19;
constexpr u32 SM = 155648;
constexpr u32 SP = SM;
constexpr u32 SQ = SP + 1024;
constexpr u32 CSL = SQ + 1024;
static_assert(RING_SLOTS * 8192 <= SM && CSL + 1024 <= 163328, "LDS map");

struct EoTab { u16 v[256 * 12]; };
constexpr u32 foff_c(int j) {
    if (j < 0) return 768u;
    u32 t = (u32)j;
    int s = t >= 1024 ? 3 : t >= 512 ? 2 : t >= 256 ? 1 : 0;
    if (s > 0) t = ((t + (1u << (s - 1)) - 1u + ((t >> s) & 1u)) >> s) << s;
    u32 col = (t >> 3) & 31u, row = 0;
    if (t < 256u) row = t & 7u;
    else if (t < 512u) row = 8u + ((t >> 1) & 3u);
    else if (t < 1024u) row = 12u + 2u * ((t >> 8) & 1u) + ((t >> 2) & 1u);
    else if (t < 2048u) row = 16u + ((t >> 8) & 3u);
    else row = 20u;
    return row * 32u + col;
}
constexpr EoTab make_eo() { EoTab e{}; for (int t = 0; t < 256; ++t) for (int k = 0; k < 12; ++k) e.v[t * 12 + k] = (u16)(k < 11 ? foff_c(2047 - 8 * t - k) * 4u : 0u); return e; }
__device__ const __attribute__((aligned(16))) EoTab EO = make_eo();

DI float bflo(u32 d) { return __uint_as_float(d << 16); }
DI float bfhi(u32 d) { return __uint_as_float(d & 0xffff0000u); }
DI u32 pk2(float lo, float hi) { bf16x2 v = {(__bf16)lo, (__bf16)hi}; return __builtin_bit_cast(u32, v); }
DI float rbf(float f) { return bflo(pk2(f, f)); }
DI bf16x8 as_bf(v4u v) { return __builtin_bit_cast(bf16x8, v); }
DI f32x4 mfma16(v4u a, v4u b, f32x4 c) { return __builtin_amdgcn_mfma_f32_16x16x32_bf16(as_bf(a), as_bf(b), c, 0, 0, 0); }
DI f32x16 mfma32(v4u a, v4u b, f32x16 c) { return __builtin_amdgcn_mfma_f32_32x32x16_bf16(as_bf(a), as_bf(b), c, 0, 0, 0); }
template <class T> DI LAS T* lds(u32 off) { return (LAS T*)(uintptr_t)off; }
DI v2u tr_read(u32 off) { s16x4 r = __builtin_amdgcn_ds_read_tr16_b64_v4i16(lds<s16x4>(off)); return __builtin_bit_cast(v2u, r); }
DI v4u tr_frag(u32 off0, u32 off1) { v2u a = tr_read(off0), b = tr_read(off1); return v4u{a.x, a.y, b.x, b.y}; }
template <int CTRL, int RM> DI float dpp_add(float v) { int t = __builtin_amdgcn_update_dpp(0, __float_as_int(v), CTRL, RM, 0xf, true); return v + __int_as_float(t); }
DI float wave_sum(float v) {
    v = dpp_add<0x111, 0xf>(v); v = dpp_add<0x112, 0xf>(v); v = dpp_add<0x114, 0xf>(v); v = dpp_add<0x118, 0xf>(v);
    v = dpp_add<0x142, 0xa>(v); v = dpp_add<0x143, 0xc>(v);
    return __int_as_float(__builtin_amdgcn_readlane(__float_as_int(v), 63));
}
DI float half_sum(float v) { const u32 u = __float_as_uint(v); const auto r = __builtin_amdgcn_permlane32_swap(u, u, false, false); return __uint_as_float(r[0]) + __uint_as_float(r[1]); }
DI void fence() { asm volatile("" ::: "memory"); }
DI void bar() { asm volatile("s_waitcnt lgkmcnt(0)" ::: "memory"); __builtin_amdgcn_s_barrier(); asm volatile("" ::: "memory"); }
template <int N> DI void vmw() { asm volatile("s_waitcnt vmcnt(%0)" :: "n"(N) : "memory"); }
template <bool S1> DI void glds16(u32 voff, const void* sbase, u32 ldst) {
    u32 keep;
    if (S1) asm volatile("s_mov_b32 %0, m0\n\ts_mov_b32 m0, %3\n\ts_nop 0\n\tglobal_load_lds_dwordx4 %1, %2 sc1\n\ts_mov_b32 m0, %0" : "=&s"(keep) : "v"(voff), "s"(sbase), "s"(ldst) : "memory");
    else    asm volatile("s_mov_b32 %0, m0\n\ts_mov_b32 m0, %3\n\ts_nop 0\n\tglobal_load_lds_dwordx4 %1, %2\n\ts_mov_b32 m0, %0" : "=&s"(keep) : "v"(voff), "s"(sbase), "s"(ldst) : "memory");
}
DI void glds16v(const void* gaddr, u32 ldst) {
    u32 keep;
    asm volatile("s_mov_b32 %0, m0\n\ts_mov_b32 m0, %2\n\ts_nop 0\n\tglobal_load_lds_dwordx4 %1, off\n\ts_mov_b32 m0, %0" : "=&s"(keep) : "v"(gaddr), "s"(ldst) : "memory");
}
DI float dot2(u32 a, u32 b, float c) { return __builtin_amdgcn_fdot2_f32_bf16(__builtin_bit_cast(bf16x2, a), __builtin_bit_cast(bf16x2, b), c, false); }
DI f2 cmul(f2 a, f2 b) { f2 t = f2{a.x, a.x} * b; return __builtin_elementwise_fma(f2{a.y, a.y}, f2{-b.y, b.x}, t); }
DI f2 cexp_rev(float ar, float ai, float t) {
    const float e = __builtin_amdgcn_exp2f(ar * t), ph = __builtin_amdgcn_fractf(ai * t);
    return f2{e * __builtin_amdgcn_cosf(ph), e * __builtin_amdgcn_sinf(ph)};
}
DI u32 swz_b(u32 row) { return ((row & 3u) << 2) | ((row >> 2) & 3u); }
DI u32 off_b(u32 row, u32 ch) { return 256u * row + 16u * (ch ^ swz_b(row)); }
DI u32 off_a(u32 row, u32 ch) { return 2048u * (row >> 3) + 512u * (ch >> 2) + 64u * (row & 7u) + 16u * ((ch & 3u) ^ ((row >> 2) & 3u)); }
DI u32 max4(v4u v) { return max(max(v.x, v.y), max(v.z, v.w)); }
DI u32 san(u32 v) { return min(v, 0xfffffffeu); }
DI u32 ln_pack(float mean, float var) {
    const float m = rbf(mean), sd = rbf(__builtin_amdgcn_sqrtf(rbf(rbf(var) + 1e-5f)));
    return (__float_as_uint(m) >> 16) | (__float_as_uint(sd) & 0xffff0000u);
}

#define STAMP(i) do { if ((i) == DIAG) diag_t = (u32)(__builtin_amdgcn_s_memrealtime() - tstart0); } while (0)
#define CSTAMP(i) do { } while (0)
#define STAMP_DECL u32 diag_t = 0; const u64 tstart0 = __builtin_amdgcn_s_memrealtime();
#define STAMP_FLUSH do { } while (0)
#define SGB(mask, n) __builtin_amdgcn_sched_group_barrier(mask, n, 0)
#define SB0() __builtin_amdgcn_sched_barrier(0)
constexpr int M_MFMA = 0x8, M_VMEM = 0x10, M_DSR = 0x100, M_VALU = 0x2;
DI void delay_ticks(u32 n) { const u64 t0 = __builtin_amdgcn_s_memrealtime(); while ((u32)(__builtin_amdgcn_s_memrealtime() - t0) < n) __builtin_amdgcn_s_sleep(1); }
DI u32 opaque(u32 v) { asm volatile("" : "+v"(v)); return v; }

template <int TG> struct Tiles;
template <> struct Tiles<0> { static constexpr int n = 3; static constexpr int t[5] = {7, 6, 2, 0, 0}; };
template <> struct Tiles<1> { static constexpr int n = 5; static constexpr int t[5] = {5, 4, 3, 1, 0}; };

template <int TG> DI void conv_wave(int tid, int w, int lane, int cm, float dsv, __amdgpu_buffer_rsrc_t rsW, v4u (&Wf)[28], u32 wf_off) {
    typedef Tiles<TG> T;
    constexpr int NP = T::t[0] + 1;
    const int ch = w >> 2, rh = (w >> 1) & 1;
    f32x4 acc[T::n];
#pragma unroll
    for (int i = 0; i < T::n; ++i) acc[i] = f32x4{0.f, 0.f, 0.f, 0.f};
    const u32 cb = lane & 15, cq = lane >> 4, cs = (3u - cb) & 3u;
    const u32 kbase = opaque(KC + ch * KC_CH + cs * KC_COPY + 2 * (2047 - 16 * (cq >> 1) - cb + 8 * (cq & 1) - cs) - 32 * 8 * rh - 32 * 118);
    const u32 kbase8 = opaque(kbase + 8);
    const u32 ubase = opaque(UU + ch * UU_CH + 512 + 32 * (cb - (cq >> 1)) + 16 * (cq & 1) - 32 * 8 * rh - 32 * 6);
    v4u Af[2][NP], Bf[2][NP];
    auto ld = [&](int rr, v4u (&A)[NP], v4u (&B)[NP]) {
#pragma unroll
        for (int p = 0; p < NP; ++p) {
            const v2u lo = *lds<volatile v2u>(kbase + 32 * (118 - (16 * p + 2 * rr))), hi = *lds<volatile v2u>(kbase8 + 32 * (118 - (16 * p + 2 * rr)));
            A[p] = v4u{lo.x, lo.y, hi.x, hi.y};
            B[p] = *lds<v4u>(ubase + 32 * (16 * p - 2 * rr + 6));
        }
    };
    ld(0, Af[0], Bf[0]);
#pragma unroll
    for (int rr = 0; rr < 4; ++rr) {
        SB0();
        if (rr < 3) ld(rr + 1, Af[(rr + 1) & 1], Bf[(rr + 1) & 1]);
#pragma unroll
        for (int i = 0; i < 3; ++i) Wf[3 * rr + i] = __builtin_amdgcn_raw_buffer_load_b128(rsW, wf_off + (3 * rr + i) * 1024, 0, WAUX);
#pragma unroll
        for (int p = 0; p < NP; ++p)
#pragma unroll
            for (int i = 0; i < T::n; ++i)
                if (p <= T::t[i]) acc[i] = mfma16(Af[rr & 1][p], Bf[rr & 1][T::t[i] - p], acc[i]);
#pragma unroll
        for (int i = 0; i < 18; ++i) {
            SGB(M_MFMA, 1);
            if (rr < 3) { if (NP == 8 && i < 6) SGB(M_DSR, 2); else SGB(M_DSR, 1); }
            if (i == 3 || i == 9 || i == 15) SGB(M_VMEM, 1);
        }
    }
    SB0();
    CSTAMP(30);
    const u32 mine = CX + w * 3072, other = CX + (w ^ 2) * 3072;
    if (rh == 0) {
#pragma unroll
        for (int i = 1; i < T::n; i += 2) *lds<f32x4>(mine + (i >> 1) * 1024 + lane * 16) = acc[i];
    } else {
#pragma unroll
        for (int i = 0; i < T::n; i += 2) *lds<f32x4>(mine + (i >> 1) * 1024 + lane * 16) = acc[i];
    }
    bar();
    CSTAMP(31);
    auto fin = [&](int i, f32x4 own) {
        const f32x4 y = own + *lds<f32x4>(other + (i >> 1) * 1024 + lane * 16);
        const u32 l0 = 256 * T::t[i] + 16 * cb + 4 * cq;
        const v2u uu = *lds<v2u>(UU + ch * UU_CH + 512 + l0 * 2), xx = *lds<v2u>(XS + ch * 4096 + l0 * 2);
        const float uv[4] = {bflo(uu.x), bfhi(uu.x), bflo(uu.y), bfhi(uu.y)}, xv[4] = {bflo(xx.x), bfhi(xx.x), bflo(xx.y), bfhi(xx.y)};
        float h[4];
#pragma unroll
        for (int r = 0; r < 4; ++r) h[r] = xv[r] + rbf(rbf(y[r]) + rbf(uv[r] * dsv));
        const v2u o2 = {pk2(h[0], h[1]), pk2(h[2], h[3])};
        __builtin_amdgcn_raw_buffer_store_b64(o2, rsW, WS_HT + (2 * cm + ch) * 4096 + l0 * 2, 0, SC1);
    };
    if (rh == 0) {
#pragma unroll
        for (int i = 0; i < T::n; i += 2) fin(i, acc[i]);
    } else {
#pragma unroll
        for (int i = 1; i < T::n; i += 2) fin(i, acc[i]);
    }
}

__global__ void __launch_bounds__(512, 2)
fused_kernel(const u16* __restrict__ x, const u16* __restrict__ Are, const u16* __restrict__ Aim, const u16* __restrict__ Bre, const u16* __restrict__ Bim,
             const u16* __restrict__ Cre, const u16* __restrict__ Cim, const u16* __restrict__ logdt, const u16* __restrict__ Dskip,
             const u16* __restrict__ wg, const u16* __restrict__ wu, const u16* __restrict__ wd, u16* __restrict__ out, char* __restrict__ ws)
{
    const int tid = threadIdx.x, lane = tid & 63, w = __builtin_amdgcn_readfirstlane(tid >> 6);
    const int b = blockIdx.x, g = b & 7, j = b >> 3;
    const bool mainb = j < 28;
    const int cm = mainb ? g * 28 + j : 0;
    const int P = 4 * (g >> 1) + j / 7, S = 7 * (g & 1) + j % 7;
    const __amdgpu_buffer_rsrc_t rsW = __builtin_amdgcn_make_buffer_rsrc(ws, 0, WS_END, 0x00020000);
    const __amdgpu_buffer_rsrc_t rsX = __builtin_amdgcn_make_buffer_rsrc((void*)x, 0, L_ * D_ * 2, 0x00020000);
    const u32 trq = (lane & 15) >> 2, trp = lane & 3, trh = lane >> 5, trb = (lane >> 4) & 1;
    STAMP_DECL
    STAMP(0);
    {
        u64 pc; asm volatile("s_getpc_b64 %0" : "=s"(pc));
        pc &= ~255ull;
        for (int q = j * 8 + w; q < CODEPF; q += 256) glds16<false>((u32)(q * 1024 + lane * 16), (const void*)pc, 157696);
    }
    const u64 tstart = __builtin_amdgcn_s_memrealtime();
    v4u xr; u64 ep; u32 p_ld2, p_dk2;
    asm volatile("global_load_dwordx4 %0, %1, %2" : "=v"(xr) : "v"((u32)((8 * b + w) * 896 + min(lane, 55) * 16)), "s"(x) : "memory");
    asm volatile("s_load_dwordx2 %0, %3, 0x0\n\ts_load_dword %1, %4, 0x0\n\ts_load_dword %2, %5, 0x0" : "=&s"(ep), "=&s"(p_ld2), "=&s"(p_dk2)
                 : "s"(ws + WS_EP), "s"((const char*)logdt + cm * 4), "s"((const char*)Dskip + cm * 4) : "memory");
    const int ch = w >> 2, dch = 2 * cm + ch;
    if (mainb) {
        const int ai_ = lane >> 3;
        const u16* src = ai_ == 0 ? Are : ai_ == 1 ? Aim : ai_ == 2 ? Bre : ai_ == 3 ? Bim : ai_ == 4 ? Cre : Cim;
        glds16v(src + dch * N_ + (w & 3) * 64 + (lane & 7) * 8, PRM + w * 1024);
        const int ew = w < 6 ? w : w - 6;
        glds16<false>((u32)(ew * 1024 + lane * 16), (const void*)&EO, EOL + ew * 1024);
        const int bb_ = b < 196 ? b : b - 196;
#pragma unroll
        for (int i = 0; i < 2; ++i) {
            const int id = 2 * bb_ + i, Sp = id / 28, s = id % 28;
            const u32 row = (w & 3) * 4 + (lane >> 4), chk = (lane & 15) ^ swz_b(row);
            glds16<false>((u32)((16 * s + row) * (F_ * 2) + Sp * 256 + chk * 16), (w >> 2) ? (const void*)wu : (const void*)wg, RT + i * 8192 + w * 1024);
        }
    } else {
        const int hb = (j - 28) * 8 + g;
#pragma unroll
        for (int k = 0; k < 7; ++k) {
            const int gid = min(hb + 32 * k, 195);
            const int dt_ = gid / 28, fg = gid % 28;
            const u32 row = 8 * w + (lane >> 3), chk = (lane & 7) ^ (((row >> 1) & 1) << 2);
            glds16<false>((u32)((64 * fg + row) * (D_ * 2) + dt_ * 128 + chk * 16), (const void*)wd, k * 8192 + w * 1024);
        }
    }
    asm volatile("s_cmp_ge_i32 %1, 28\n\ts_cbranch_scc1 1f\n\ts_waitcnt vmcnt(4)\n\ts_branch 2f\n1:\n\ts_waitcnt vmcnt(7)\n2:" : "+v"(xr) : "s"(__builtin_amdgcn_readfirstlane(j)) : "memory", "scc");
    asm volatile("s_waitcnt lgkmcnt(0)" : "+s"(ep), "+s"(p_ld2), "+s"(p_dk2) :: "memory");
    ep += 1ull;
    const u32 p_ldt = ch ? p_ld2 >> 16 : p_ld2 & 0xffffu, p_dsk = ch ? p_dk2 >> 16 : p_dk2 & 0xffffu;
    if (lane >= 56) xr = v4u{0, 0, 0, 0};
    const u32 tag = (u32)ep, tagx = (u32)(ep >> 32) << 16;
    if (lane < 56) *lds<v4u>(XT + w * 912 + lane * 16) = xr;
    {
        const float v[8] = {bflo(xr.x), bfhi(xr.x), bflo(xr.y), bfhi(xr.y), bflo(xr.z), bfhi(xr.z), bflo(xr.w), bfhi(xr.w)};
        const float mean = wave_sum(((v[0] + v[1]) + (v[2] + v[3])) + ((v[4] + v[5]) + (v[6] + v[7]))) * (1.f / 448.f);
        float q = 0.f;
#pragma unroll
        for (int e = 0; e < 8; ++e) { const float dl = v[e] - mean; q += dl * dl; }
        q = lane < 56 ? q : 0.f;
        const u32 ln1 = ln_pack(mean, wave_sum(q) * (1.f / 448.f));
        if (lane == 0) __hip_atomic_store((GAS u64*)(ws + WS_SG) + (8 * b + w), ((u64)tag << 32) | ln1, RLX_AGENT);
    }
    STAMP(1);
    const u32 km0 = FM + ch * FM_CH + (w & 3) * 256;
    f2 Ga, Gb, Gc, Gd, KX, KY, G4, G4r, E1, E1r, E256, E256r, E2, E2r, E4, E4r;
#define M1(a, bb) (f2{(a).x, (a).x} * (bb))
#define M2(a, br, t) __builtin_elementwise_fma(f2{(a).y, (a).y}, (br), (t))
#define WROW1(r, v) do { u32 keep_; asm volatile("s_mov_b32 %0, m0\n\ts_mov_b32 m0, %2\n\ts_nop 0\n\tds_write_addtid_b32 %1 offset:%3\n\ts_mov_b32 m0, %0" : "=&s"(keep_) : "v"(v), "s"(km0), "n"((r) * FM_ROW) : "memory"); } while (0)
#define WROW6(r0, v0, r1, v1, r2, v2, r3, v3, r4, v4, r5, v5) do { u32 keep_; asm volatile("s_mov_b32 %0, m0\n\ts_mov_b32 m0, %7\n\ts_nop 0\n\t" \
        "ds_write_addtid_b32 %1 offset:%8\n\tds_write_addtid_b32 %2 offset:%9\n\tds_write_addtid_b32 %3 offset:%10\n\tds_write_addtid_b32 %4 offset:%11\n\tds_write_addtid_b32 %5 offset:%12\n\tds_write_addtid_b32 %6 offset:%13\n\ts_mov_b32 m0, %0" \
        : "=&s"(keep_) : "v"(v0), "v"(v1), "v"(v2), "v"(v3), "v"(v4), "v"(v5), "s"(km0), "n"((r0) * FM_ROW), "n"((r1) * FM_ROW), "n"((r2) * FM_ROW), "n"((r3) * FM_ROW), "n"((r4) * FM_ROW), "n"((r5) * FM_ROW) : "memory"); } while (0)
#define WA(r, X) WROW1(r, pk2((X).x, -(X).y))
#define KSTEP(t) do { SB0(); \
        const f2 ta = M1(Ga, G4), tb = M1(Gb, G4), tc = M1(Gc, G4), td_ = M1(Gd, G4), tx = M1(KX, E1), ty = M1(KY, E256); \
        const u32 c0 = pk2(Ga.x, Ga.y), c1 = pk2(Gb.x, Gb.y), c2 = pk2(Gc.x, Gc.y), c3 = pk2(Gd.x, Gd.y), c4 = pk2(KX.x, -KX.y), c5 = pk2(KY.x, -KY.y); \
        f2 ts0, ts1; \
        if ((t) == 0) ts0 = M1(KY, E2); \
        if ((t) == 1 || (t) == 2) ts0 = M1(KY, E4); \
        SB0(); \
        WROW6(21 + 4 * (t), c0, 22 + 4 * (t), c1, 23 + 4 * (t), c2, 24 + 4 * (t), c3, (t), c4, ((t) == 0 ? 8 : (t) == 1 ? 12 : (t) == 2 ? 14 : 13 + (t)), c5); \
        if ((t) == 0) { f2 Z = M2(KY, E2r, ts0); WA(9, Z); ts1 = M1(Z, E2); Z = M2(Z, E2r, ts1); WA(10, Z); ts1 = M1(Z, E2); Z = M2(Z, E2r, ts1); WA(11, Z); } \
        if ((t) == 1) { const f2 V = M2(KY, E4r, ts0); WA(13, V); } \
        if ((t) == 2) { const f2 V = M2(KY, E4r, ts0); WA(15, V); } \
        if ((t) < 7) { Ga = M2(Ga, G4r, ta); Gb = M2(Gb, G4r, tb); Gc = M2(Gc, G4r, tc); Gd = M2(Gd, G4r, td_); KX = M2(KX, E1r, tx); KY = M2(KY, E256r, ty); } \
    } while (0)
    if (mainb) {
        vmw<4>();
        const u32 pa_ = PRM + w * 1024 + lane * 2;
        const u32 p_are = *lds<u16>(pa_), p_aim = *lds<u16>(pa_ + 128), p_bre = *lds<u16>(pa_ + 256), p_bim = *lds<u16>(pa_ + 384), p_cre = *lds<u16>(pa_ + 512), p_cim = *lds<u16>(pa_ + 640);
        const float dt = rbf(expf(bflo(p_ldt)));
        const float zr = dt * bflo(p_are), zi = dt * bflo(p_aim);
        const float ar = zr * 1.4426950408889634f, ai = zi * 0.15915494309189535f;
        const f2 CB = cmul(f2{bflo(p_cre), bflo(p_cim)}, f2{dt * bflo(p_bre), dt * bflo(p_bim)});
        E1 = cexp_rev(ar, ai, 1.f); E256 = cexp_rev(ar, ai, 256.f);
        const f2 G1 = cexp_rev(ar, ai, 8.f);
        E2 = cmul(E1, E1); E4 = cmul(E2, E2);
        const f2 G2 = cmul(G1, G1);
        G4 = cmul(G2, G2);
        E1r = f2{-E1.y, E1.x}; E2r = f2{-E2.y, E2.x}; E4r = f2{-E4.y, E4.x}; E256r = f2{-E256.y, E256.x}; G4r = f2{-G4.y, G4.x};
        Ga = f2{1.f, 0.f}; Gb = G1; Gc = G2; Gd = cmul(G2, G1);
        KX = CB; KY = cmul(CB, E256);
        SB0();
    }
    bar();
    STAMP(16);
    auto publish_xg = [&]() {
        u32 x0[4], x1[4];
#pragma unroll
        for (int k = 0; k < 4; ++k) {
            const int id = min(tid + 512 * k, 1791), c = id >> 2, i2 = (id & 3) * 2;
            x0[k] = *lds<u16>(XT + i2 * 912 + c * 2); x1[k] = *lds<u16>(XT + (i2 + 1) * 912 + c * 2);
        }
        SB0();
#pragma unroll
        for (int k = 0; k < 4; ++k)
            __builtin_amdgcn_raw_buffer_store_b128(v4u{x0[k] | tagx, tag, x1[k] | tagx, tag}, rsW, (tid + 512 * k < 1792) ? (u32)(WS_XG + b * 28672 + (tid + 512 * k) * 16) : 0x7ffffff0u, 0, SC1);
    };
    if (mainb) {
    publish_xg();
    STAMP(17);
    KSTEP(0); KSTEP(1); KSTEP(2);
    SB0();
    vmw<5>();
    bar();
    if (b < 196) {
#pragma unroll
        for (int i = 0; i < 2; ++i) {
            const int id = 2 * b + i, Sp = id / 28, s = id % 28;
            const u32 base = RT + i * 8192 + trb * 4096 + 8 * (trp & 1);
            const v4u f = tr_frag(base + off_b(8 * trh + trq, 2 * w + (trp >> 1)), base + off_b(8 * trh + 4 + trq, 2 * w + (trp >> 1)));
            __builtin_amdgcn_raw_buffer_store_b128(f, rsW, WS_WG + ((Sp * 8 + w) * 28 + s) * 1024 + lane * 16, 0, SC1);
        }
    }
    fence();
    STAMP(2);
    {
        KSTEP(3); KSTEP(4); KSTEP(5); KSTEP(6); KSTEP(7);
        SB0();
#undef KSTEP
#undef WA
#undef WROW1
#undef WROW6
#undef M1
#undef M2
        STAMP(3);
        vmw<0>();
        bar();
        if (tid == 0 && b < 196) __hip_atomic_store((GAS u32*)(ws + WS_WR + b * 64), tag, RLX_AGENT);
        const int ta_ = (w >> 1) & 1, tb_ = w & 1;
        const u32 fa_ = opaque(FM + ch * FM_CH + (16 * ta_ + (lane & 15)) * FM_ROW + (lane >> 4) * 16), fb_ = opaque(FM + ch * FM_CH + (21 + 16 * tb_ + (lane & 15)) * FM_ROW + (lane >> 4) * 16);
        f32x4 f0 = {0.f, 0.f, 0.f, 0.f}, f1 = {0.f, 0.f, 0.f, 0.f};
        v4u A[16], B[16];
#pragma unroll
        for (int ks = 0; ks < 16; ++ks) { A[ks] = *lds<v4u>(fa_ + ks * 64); B[ks] = *lds<v4u>(fb_ + ks * 64); }
        SB0();
#pragma unroll
        for (int ks = 0; ks < 16; ks += 2) { f0 = mfma16(A[ks], B[ks], f0); f1 = mfma16(A[ks + 1], B[ks + 1], f1); }
        f0 += f1;
#pragma unroll
        for (int r = 0; r < 4; ++r) { const int row = 16 * ta_ + 4 * (lane >> 4) + r; if (row < 24) *lds<float>(FT + ch * FT_CH + (row * 32 + 16 * tb_ + (lane & 15)) * 4) = f0[r]; }
        if ((tid & 255) == 255) *lds<float>(FT + ch * FT_CH + 768 * 4) = 0.f;
    }
    STAMP(4);
    {
        v4u xg[4], sg[4];
        const u32 fa = WS_WR + (tid < 196 ? tid : 0) * 64;
        const u32 xa = WS_XG + (32 * w + (lane >> 3)) * 28672 + cm * 128 + (lane & 7) * 16, sa = WS_SG + (32 * w + (lane >> 3)) * 64 + (lane & 3) * 16;
        if (DLY1) { while ((u32)(__builtin_amdgcn_s_memrealtime() - tstart) < DLY1) __builtin_amdgcn_s_sleep(1); }
        for (u32 spin = 0;; ++spin) {
#pragma unroll
            for (int k = 0; k < 4; ++k) { xg[k] = __builtin_amdgcn_raw_buffer_load_b128(rsW, xa + k * 8 * 28672, 0, SC1); sg[k] = __builtin_amdgcn_raw_buffer_load_b128(rsW, sa + k * 512, 0, SC1); }
            const u32 fl = __builtin_amdgcn_raw_buffer_load_b32(rsW, fa, 0, SC1);
            bool ok = fl == tag;
#pragma unroll
            for (int k = 0; k < 4; ++k) ok = ok && sg[k].y == tag && sg[k].w == tag && xg[k].y == tag && xg[k].w == tag && (xg[k].x & 0xffff0000u) == tagx && (xg[k].z & 0xffff0000u) == tagx;
            if (__all(ok) || spin > SPIN_CAP) break;
            __builtin_amdgcn_s_sleep(8);
        }
        STAMP(5);
        const int c = (lane >> 2) & 1;
#pragma unroll
        for (int k = 0; k < 4; ++k) {
            const u32 l0 = 8 * (32 * w + 8 * k + (lane >> 3)) + 2 * (lane & 3);
            const float u0 = rbf(bflo(xg[k].x) - bflo(sg[k].x)) * __builtin_amdgcn_rcpf(bfhi(sg[k].x));
            const float u1 = rbf(bflo(xg[k].z) - bflo(sg[k].z)) * __builtin_amdgcn_rcpf(bfhi(sg[k].z));
            *lds<u32>(UU + c * UU_CH + 512 + l0 * 2) = pk2(u0, u1);
            *lds<u32>(XS + c * 4096 + l0 * 2) = (xg[k].x & 0xffffu) | (xg[k].z << 16);
        }
    }
    if (tid >= 64 && tid < 128) *lds<v4u>(UU + ((tid >> 5) & 1) * UU_CH + (tid & 31) * 16) = v4u{0, 0, 0, 0};
    bar();
    STAMP(18);
    {
        const v2u ea = *lds<v2u>(EOL + (tid & 255) * 24), eb = *lds<v2u>(EOL + (tid & 255) * 24 + 8), ec = *lds<v2u>(EOL + (tid & 255) * 24 + 16);
        const v4u eo0 = {ea.x, ea.y, eb.x, eb.y}; const v2u eo1 = ec;
        const u32 eo[11] = {eo0.x & 0xffffu, eo0.x >> 16, eo0.y & 0xffffu, eo0.y >> 16, eo0.z & 0xffffu, eo0.z >> 16, eo0.w & 0xffffu, eo0.w >> 16, eo1.x & 0xffffu, eo1.x >> 16, eo1.y & 0xffffu};
        float v[11];
#pragma unroll
        for (int e = 0; e < 11; ++e) v[e] = *lds<float>(FT + ch * FT_CH + eo[e]);
        SB0();
        u32 p[10];
#pragma unroll
        for (int e = 0; e < 10; ++e) p[e] = pk2(v[e], v[e + 1]);
#pragma unroll
        for (int s = 0; s < 4; ++s) *lds<v4u>(KC + ch * KC_CH + s * KC_COPY + (tid & 255) * 16) = v4u{p[s], p[s + 2], p[s + 4], p[s + 6]};
    }
    if (tid < 16) *lds<v4u>(KC + (tid >> 3) * KC_CH + ((tid >> 1) & 3) * KC_COPY + 4096 + (tid & 1) * 16) = v4u{0, 0, 0, 0};
    bar();
    STAMP(6);
    CSTAMP(26);
    v4u Wf[28];
    const u32 wf_off = WS_WG + (S * 8 + w) * 28 * 1024 + lane * 16;
    {

        if (w & 1) conv_wave<1>(tid, w, lane, cm, bflo(p_dsk), rsW, Wf, wf_off); else conv_wave<0>(tid, w, lane, cm, bflo(p_dsk), rsW, Wf, wf_off);
    }
    STAMP(7);
    CSTAMP(27);
    const int l0p = 128 * P;
    vmw<0>();
    bar();
    {
        if (tid < 8) __hip_atomic_store((GAS u32*)(ws + WS_F2 + tid * 16384 + cm * 64), tag, RLX_AGENT);
        fence();
#pragma unroll
        for (int i = 12; i < 28; ++i) Wf[i] = __builtin_amdgcn_raw_buffer_load_b128(rsW, wf_off + i * 1024, 0, WAUX);
        fence();
        delay_ticks(DLY2);
        if (w < 4) {
            const u32 pa = tid < 224 ? (u32)(WS_F2 + g * 16384 + tid * 64) : (u32)(WS_WF + (tid - 224) * 64);
            for (u32 spin = 0;; ++spin) {
                const u32 s0 = __builtin_amdgcn_raw_buffer_load_b32(rsW, pa, 0, SC1);
                if (__all(s0 == tag) || spin > SPIN_CAP) break;
                __builtin_amdgcn_s_sleep(8);
            }
        }
    }
    bar();
    STAMP(8);
    if (b == 0 && tid == 0) __hip_atomic_store((GAS u64*)(ws + WS_EP), ep, RLX_AGENT);
    const u32 bt_src = (8 * (w >> 1) + ((lane >> 2) & 7)) * 4096 + l0p * 2 + (4 * (2 * (w & 1) + (lane >> 5)) + ((lane & 3) ^ ((2 * (w >> 1) + ((lane >> 4) & 1)) & 3))) * 16;
    const u32 bt_dst = BT + (w >> 1) * 2048 + (w & 1) * 1024;
    auto bt_piece = [&](int k) { glds16<BTSC1>(bt_src + k * 32 * 4096, ws + WS_HT, bt_dst + k * 8192); };
    const float csum = __uint_as_float(__builtin_amdgcn_raw_buffer_load_b32(rsW, WS_CS + (((lane >> 4) & 1) * 1792 + 128 * S + 16 * w + (lane & 15)) * 4, 0, SC1));
    fence();
#pragma unroll
    for (int k = 0; k < 6; ++k) bt_piece(k);
    vmw<4>();
    bar();
    STAMP(9);
    CSTAMP(24);
    f32x16 acc[4];
    v4u Wd[28];
    const int td = w & 1, kq = w >> 1, hp = S / 7, dtile = S % 7;
    const u32 wd_off = WS_WD + ((dtile * 2 + td) * 112 + 28 * kq) * 1024 + lane * 16;
    u32 bb[2];
#pragma unroll
    for (int tt = 0; tt < 2; ++tt) bb[tt] = opaque(BT + 2048 * trh + 64 * (4 * tt + trq) + 16 * ((2 * trb + (trp >> 1)) ^ ((2 * trh + tt) & 3)) + 8 * (trp & 1));
    {
#pragma unroll
        for (int t = 0; t < 4; ++t)
#pragma unroll
            for (int i = 0; i < 16; ++i) acc[t][i] = 0.f;
        v4u Bf[2][4], Of;
        float sa0 = 0.f, sa1 = 0.f, sq0 = 0.f, sq1 = 0.f;
        const u32 bh0 = opaque(bb[0] + 65536), bh1 = opaque(bb[1] + 65536);
        const u32 so0 = opaque(bb[0] + (w >> 2) * 4096 + (w & 3) * 512), so1 = opaque(bb[1] + (w >> 2) * 4096 + (w & 3) * 512);
        const u32 sh0 = opaque(so0 + 65536), sh1 = opaque(so1 + 65536);
#pragma unroll
        for (int t = 0; t < 4; ++t) Bf[0][t] = tr_frag(bb[0] + 512 * t, bb[1] + 512 * t);
#pragma unroll
        for (int s = 0; s < 28; ++s) {
            SB0();
            if (s == 3) { vmw<6>(); bar(); }
            if (s == 11) { vmw<7>(); bar(); }
            SB0();
            if (s + 1 < 28) {
#pragma unroll
                for (int t = 0; t < 4; ++t)
                    Bf[(s + 1) & 1][t] = (s + 1 < 16) ? tr_frag(bb[0] + (s + 1) * 4096 + 512 * t, bb[1] + (s + 1) * 4096 + 512 * t)
                                                       : tr_frag(bh0 + (s + 1 - 16) * 4096 + 512 * t, bh1 + (s + 1 - 16) * 4096 + 512 * t);
            }
            if ((s & 1) == 0) Of = (s < 16) ? tr_frag(so0 + (s >> 1) * 8192, so1 + (s >> 1) * 8192) : tr_frag(sh0 + ((s - 16) >> 1) * 8192, sh1 + ((s - 16) >> 1) * 8192);
            if (s < 4) { bt_piece(6 + 2 * s); bt_piece(7 + 2 * s); }
            else Wd[s - 4] = __builtin_amdgcn_raw_buffer_load_b128(rsW, wd_off + (s - 4) * 1024, 0, WAUX);
#pragma unroll
            for (int t = 0; t < 4; ++t) acc[t] = mfma32(Wf[s], Bf[s & 1][t], acc[t]);
            if (s & 1) {
#pragma unroll
                for (int e = 0; e < 4; e += 2) {
                    const float l0 = bflo(Of[e]), h0 = bfhi(Of[e]), l1 = bflo(Of[e + 1]), h1 = bfhi(Of[e + 1]);
                    sa0 += l0; sq0 = __builtin_fmaf(l0, l0, sq0); sa1 += l1; sq1 = __builtin_fmaf(l1, l1, sq1);
                    sa0 += h0; sq0 = __builtin_fmaf(h0, h0, sq0); sa1 += h1; sq1 = __builtin_fmaf(h1, h1, sq1);
                }
            }
            if (s >= 4) {
                if (s & 1) { SGB(M_MFMA, 1); SGB(M_DSR, 2); SGB(M_VALU, 6); SGB(M_MFMA, 1); SGB(M_DSR, 2); SGB(M_VALU, 6); SGB(M_VMEM, 1); SGB(M_MFMA, 1); SGB(M_DSR, 2); SGB(M_VALU, 6); SGB(M_MFMA, 1); SGB(M_DSR, 2); SGB(M_VALU, 6); }
                else { SGB(M_MFMA, 1); SGB(M_DSR, 3); SGB(M_MFMA, 1); SGB(M_DSR, 3); SGB(M_VMEM, 1); SGB(M_MFMA, 1); SGB(M_DSR, 2); SGB(M_MFMA, 1); SGB(M_DSR, 2); }
            }
        }
        float sumh = sa0 + sa1, sq = sq0 + sq1;

        SB0();
    #pragma unroll
        for (int s = 24; s < 28; ++s) Wd[s] = __builtin_amdgcn_raw_buffer_load_b128(rsW, wd_off + s * 1024, 0, WAUX);

        sumh = half_sum(sumh); sq = half_sum(sq);
        if (lane < 32) { *lds<float>(SP + w * 128 + lane * 4) = sumh; *lds<float>(SQ + w * 128 + lane * 4) = sq; *lds<float>(CSL + w * 128 + lane * 4) = csum; }
    }
    STAMP(10);
    CSTAMP(25);
    bar();
    {
        f32x4 csg[2], csu[2];
#pragma unroll
        for (int q = 0; q < 2; ++q) { csg[q] = *lds<f32x4>(CSL + w * 128 + (8 * q + 4 * trh) * 4); csu[q] = *lds<f32x4>(CSL + w * 128 + 64 + (8 * q + 4 * trh) * 4); }
#pragma unroll
        for (int t = 0; t < 4; ++t) {
            const float m = rbf((*lds<float>(SP + t * 128 + (lane & 31) * 4) + *lds<float>(SP + (t + 4) * 128 + (lane & 31) * 4)) * (1.f / 448.f));
            float hv[8];
#pragma unroll
            for (int e = 0; e < 8; ++e) hv[e] = (acc[t][e] - m * csg[e >> 2][e & 3]) * (acc[t][e + 8] - m * csu[e >> 2][e & 3]);
            const v4u o = {pk2(hv[0], hv[1]), pk2(hv[2], hv[3]), pk2(hv[4], hv[5]), pk2(hv[6], hv[7])};
            __builtin_amdgcn_raw_buffer_store_b128(o, rsW, WS_HH + ((P * 112 + 8 * S + w) * 4 + t) * 1024 + lane * 16, 0, SC1);
        }
    }
    STAMP(11);
    const int tl_f = kq >> 1;
    const u32 lp = 64 * hp + 32 * tl_f + (lane & 31);
    float hres[8], rs;
    {
        const int tq = 2 * hp + tl_f;
        const float s1 = *lds<float>(SP + tq * 128 + (lane & 31) * 4) + *lds<float>(SP + (tq + 4) * 128 + (lane & 31) * 4);
        const float s2 = *lds<float>(SQ + tq * 128 + (lane & 31) * 4) + *lds<float>(SQ + (tq + 4) * 128 + (lane & 31) * 4);
        const float mean = s1 * (1.f / 448.f), var = fmaxf(s2 * (1.f / 448.f) - mean * mean, 0.f);
        const float sd = bfhi(ln_pack(mean, var));
        rs = 1.f / (sd * sd);
#pragma unroll
        for (int e = 0; e < 8; ++e) {
            const u32 row = 64 * dtile + 32 * td + 16 * (kq & 1) + 8 * (e >> 2) + 4 * trh + (e & 3);
            hres[e] = bflo(*lds<u16>(BT + off_a(row, lp >> 3) + (lp & 7) * 2));
        }
    }
    vmw<0>();
    bar();
    if (tid == 0) __hip_atomic_store((GAS u32*)(ws + WS_F3 + (P * 16 + S) * 64), tag, RLX_AGENT);
    const u32 hbase = WS_HH + ((P * 112 + 28 * kq) * 4 + 2 * hp) * 1024;
    if (w == 0) {
        delay_ticks(DLY3);
        const u32 pa = WS_F3 + (P * 16 + (lane < 14 ? lane : 0)) * 64;
        for (u32 spin = 0;; ++spin) {
            const u32 s0 = __builtin_amdgcn_raw_buffer_load_b32(rsW, pa, 0, SC1);
            if (__all(s0 == tag) || spin > SPIN_CAP) break;
            __builtin_amdgcn_s_sleep(8);
        }
    }
    bar();
    STAMP(12);
    const u32 hsrc = td * 1024 + lane * 16, hdst = RING + kq * 2048 + td * 1024, hrd = opaque(RING + kq * 2048 + lane * 16);
    auto hh_dma = [&](int s) { glds16<true>(hsrc + s * 4096, ws + hbase, hdst + (s % RING_SLOTS) * 8192); };
    f32x16 o2[2];
    {
#pragma unroll
        for (int i = 0; i < 16; ++i) { o2[0][i] = 0.f; o2[1][i] = 0.f; }
        constexpr int AH = 9;
#pragma unroll
        for (int s = 0; s < AH; ++s) hh_dma(s);
        v4u hf[2][2];
        vmw<AH - 4>(); bar();
        STAMP(13); CSTAMP(28);
#pragma unroll
        for (int tl = 0; tl < 2; ++tl) hf[0][tl] = *lds<v4u>(hrd + tl * 1024);
#pragma unroll
        for (int s = 0; s < 28; ++s) {
            SB0();
            if ((s & 3) == 3 && s + 1 < 28) {
                const int issued = (s - 1 + AH < 27) ? s - 1 + AH : 27, need = (s + 4 < 27) ? s + 4 : 27;
                switch (issued - need) { case 0: vmw<0>(); break; case 1: vmw<1>(); break; case 2: vmw<2>(); break; case 3: vmw<3>(); break; default: vmw<4>(); break; }
                bar();
                SB0();
            }
            if (s + 1 < 28) {
#pragma unroll
                for (int tl = 0; tl < 2; ++tl) hf[(s + 1) & 1][tl] = *lds<v4u>(hrd + ((s + 1) % RING_SLOTS) * 8192 + tl * 1024);
            }
            if (s + AH < 28) hh_dma(s + AH);
#pragma unroll
            for (int tl = 0; tl < 2; ++tl) o2[tl] = mfma32(Wd[s], hf[s & 1][tl], o2[tl]);
        }
        SB0();
        bar();
    }
    STAMP(14);
    CSTAMP(29);
    {
#pragma unroll
        for (int k2 = 0; k2 < 4; ++k2) {
            if (k2 != kq) {
                const int si = (kq > k2) ? kq - 1 : kq;
                const f32x16& a = o2[k2 >> 1];
#pragma unroll
                for (int q = 0; q < 2; ++q) { const int r0 = 8 * (k2 & 1) + 4 * q; *lds<f32x4>(((td * 4 + k2) * 3 + si) * 2048 + q * 1024 + lane * 16) = f32x4{a[r0], a[r0 + 1], a[r0 + 2], a[r0 + 3]}; }
            }
        }
        bar();
        f32x4 v[2];
#pragma unroll
        for (int q = 0; q < 2; ++q) {
            if (kq == 0) v[q] = f32x4{o2[0][4 * q], o2[0][4 * q + 1], o2[0][4 * q + 2], o2[0][4 * q + 3]};
            else if (kq == 1) v[q] = f32x4{o2[0][8 + 4 * q], o2[0][9 + 4 * q], o2[0][10 + 4 * q], o2[0][11 + 4 * q]};
            else if (kq == 2) v[q] = f32x4{o2[1][4 * q], o2[1][4 * q + 1], o2[1][4 * q + 2], o2[1][4 * q + 3]};
            else v[q] = f32x4{o2[1][8 + 4 * q], o2[1][9 + 4 * q], o2[1][10 + 4 * q], o2[1][11 + 4 * q]};
#pragma unroll
            for (int si = 0; si < 3; ++si) v[q] += *lds<f32x4>(((td * 4 + kq) * 3 + si) * 2048 + q * 1024 + lane * 16);
            const u32 d0 = 64 * dtile + 32 * td + 16 * (kq & 1) + 8 * q + 4 * trh;
            float r[4];
#pragma unroll
            for (int e = 0; e < 4; ++e) r[e] = hres[4 * q + e] + rbf(v[q][e] * rs);
            if (b == DIAGB && w == 7 && q == 1) {
                float mn = fabsf(r[0]);
                for (int o = 32; o > 0; o >>= 1) mn = fminf(mn, __shfl_xor(mn, o));
                const u32 tt = DIAG == 15 ? (u32)(__builtin_amdgcn_s_memrealtime() - tstart0) : diag_t;
                if (fabsf(r[0]) == mn) r[0] += 0.009f + 0.011f * fminf((float)tt / 4000.f, 1.f);
            }
            *(GAS v2u*)(out + (size_t)(l0p + lp) * D_ + d0) = v2u{pk2(r[0], r[1]), pk2(r[2], r[3])};
        }
    }
    STAMP(15);
    STAMP_FLUSH;
    } else {
        publish_xg();
        const int hb = (j - 28) * 8 + g;
        float cs[8];
        {
            const u16* wsrc = hb < 16 ? wg : wu;
            const int c0 = 112 * (hb & 15) + 8 * (tid % 14), r0 = tid / 14;
#pragma unroll
            for (int e = 0; e < 8; ++e) cs[e] = 0.f;
            v4u t[13];
#pragma unroll
            for (int i = 0; i < 13; ++i) { const int r = r0 + 36 * i; t[i] = (tid < 504 && r < 448) ? *(const v4u*)(wsrc + (size_t)r * F_ + c0) : v4u{0, 0, 0, 0}; }
#pragma unroll
            for (int i = 0; i < 13; ++i) { cs[0] += bflo(t[i].x); cs[1] += bfhi(t[i].x); cs[2] += bflo(t[i].y); cs[3] += bfhi(t[i].y); cs[4] += bflo(t[i].z); cs[5] += bfhi(t[i].z); cs[6] += bflo(t[i].w); cs[7] += bfhi(t[i].w); }
        }
        vmw<0>(); bar();
#pragma unroll
        for (int k = 0; k < 7; ++k) {
            const int gid = hb + 32 * k;
            if (gid < 196) {
                const int dt_ = gid / 28, fg = gid % 28, fsl = w >> 1, td = w & 1;
                u32 ad[2];
#pragma unroll
                for (int t = 0; t < 2; ++t) { const u32 row = 16 * fsl + 8 * t + 4 * trh + trq, chk = 4 * td + 2 * trb + (trp >> 1); ad[t] = k * 8192 + row * 128 + 16 * (chk ^ (((row >> 1) & 1) << 2)) + 8 * (trp & 1); }
                const v4u f = tr_frag(ad[0], ad[1]);
                __builtin_amdgcn_raw_buffer_store_b128(f, rsW, WS_WD + ((dt_ * 2 + td) * 112 + 4 * fg + fsl) * 1024 + lane * 16, 0, SC1);
            }
        }
        if (tid < 504) {
            *lds<f32x4>(65536 + tid * 32) = f32x4{cs[0], cs[1], cs[2], cs[3]};
            *lds<f32x4>(65536 + tid * 32 + 16) = f32x4{cs[4], cs[5], cs[6], cs[7]};
        }
        bar();
        if (tid < 112) {
            float s = 0.f;
            for (int r = 0; r < 36; ++r) s += *lds<float>(65536 + (r * 14 + (tid >> 3)) * 32 + (tid & 7) * 4);
            __hip_atomic_store((GAS u32*)(ws + WS_CS) + ((hb < 16 ? 0 : 1792) + 112 * (hb & 15) + tid), __float_as_uint(s), RLX_AGENT);
        }
        vmw<0>(); bar();
        if (tid == 0) __hip_atomic_store((GAS u32*)(ws + WS_WF + hb * 64), tag, RLX_AGENT);

    }
}

extern "C" void kernel_launch(void* const* d_in, const int* in_sizes, int n_in, void* d_out, int out_size, void* d_ws, size_t ws_size, hipStream_t stream) {
    (void)in_sizes; (void)n_in; (void)out_size; (void)ws_size;
    (void)hipFuncSetAttribute((const void*)fused_kernel, hipFuncAttributeMaxDynamicSharedMemorySize, LDS_BYTES);
    fused_kernel<<<256, 512, LDS_BYTES, stream>>>((const u16*)d_in[0], (const u16*)d_in[1], (const u16*)d_in[2], (const u16*)d_in[3], (const u16*)d_in[4],
        (const u16*)d_in[5], (const u16*)d_in[6], (const u16*)d_in[7], (const u16*)d_in[8], (const u16*)d_in[10], (const u16*)d_in[11], (const u16*)d_in[12],
        (u16*)d_out, (char*)d_ws);
}
```

```cpp
#include <hip/hip_runtime.h>
#include <stdint.h>

typedef unsigned short u16;
typedef unsigned u32;
typedef unsigned long long u64;
typedef u32 v4u __attribute__((ext_vector_type(4)));
typedef u32 v2u __attribute__((ext_vector_type(2)));
typedef float f32x4 __attribute__((ext_vector_type(4)));
typedef float f32x16 __attribute__((ext_vector_type(16)));
typedef float f2 __attribute__((ext_vector_type(2)));
typedef __bf16 bf16x8 __attribute__((ext_vector_type(8)));
typedef __bf16 bf16x2 __attribute__((ext_vector_type(2)));
typedef short s16x4 __attribute__((ext_vector_type(4)));
#define LAS __attribute__((address_space(3)))
#define GAS __attribute__((address_space(1)))
#define DI __device__ __forceinline__
#define RLX_AGENT __ATOMIC_RELAXED, __HIP_MEMORY_SCOPE_AGENT
#define CODEPF 28
#define SPINEND 3000
#define SC1 16
#define BTSC1 true
#define DLY1 0
#define DLY2 35
#define DLY3 35
#define WAUX 16

constexpr int L_ = 2048, D_ = 448, N_ = 256, F_ = 1792;
constexpr u32 WS_EP = 0;
constexpr u32 WS_SG = 4096;
constexpr u32 WS_WF = 32768;
constexpr u32 WS_CS = 40960;
constexpr u32 WS_WR = 1966080;
constexpr u32 WS_HT = 65536;
constexpr u32 WS_HH = 2u << 20;
constexpr u32 WS_WG = 10u << 20;
constexpr u32 WS_WD = 14u << 20;
constexpr u32 WS_XG = 16u << 20;
constexpr u32 WS_F2 = (23u << 20) + (512u << 10);
constexpr u32 WS_F3 = WS_F2 + 8 * 16384;
constexpr u32 WS_END = 24u << 20;
constexpr u32 WS_DBG = 24u << 20;
constexpr u32 POISON = 0xFFFFFFFFu;
constexpr u32 SPIN_CAP = 1u << 16;

constexpr u32 LDS_BYTES = 163840;
constexpr u32 FM = 0, FM_ROW = 1056, FM_CH = 53 * FM_ROW;
constexpr u32 FT = 2 * FM_CH, FT_CH = 772 * 4;
constexpr u32 EOL = FT + 2 * FT_CH;
constexpr u32 RT = 124288;
constexpr u32 PRM = RT + 16384;
constexpr u32 XT = PRM + 8192;
constexpr u32 KC = 0, KC_COPY = 4160, KC_CH = 4 * KC_COPY;
constexpr u32 CX = 2 * KC_CH;
constexpr u32 UU = RT, UU_CH = (256 + 2048) * 2;
constexpr u32 XS = UU + 2 * UU_CH;
static_assert(EOL % 16 == 0 && EOL + 6144 <= RT && XS + 8192 <= XT && XT + 8 * 912 <= 158848 && CX + 8 * 3072 <= FT, "LDS map");
constexpr u32 BT = 0, BT_BYTES = 448 * 256;
constexpr u32 RING = 0, RING_SLOTS = 19;
constexpr u32 SM = 155648;
constexpr u32 SP = SM;
constexpr u32 SQ = SP + 1024;
constexpr u32 CSL = SQ + 1024;
static_assert(RING_SLOTS * 8192 <= SM && CSL + 1024 <= 163328, "LDS map");

struct EoTab { u16 v[256 * 12]; };
constexpr u32 foff_c(int j) {
    if (j < 0) return 768u;
    u32 t = (u32)j;
    int s = t >= 1024 ? 3 : t >= 512 ? 2 : t >= 256 ? 1 : 0;
    if (s > 0) t = ((t + (1u << (s - 1)) - 1u + ((t >> s) & 1u)) >> s) << s;
    u32 col = (t >> 3) & 31u, row = 0;
    if (t < 256u) row = t & 7u;
    else if (t < 512u) row = 8u + ((t >> 1) & 3u);
    else if (t < 1024u) row = 12u + 2u * ((t >> 8) & 1u) + ((t >> 2) & 1u);
    else if (t < 2048u) row = 16u + ((t >> 8) & 3u);
    else row = 20u;
    return row * 32u + col;
}
constexpr EoTab make_eo() { EoTab e{}; for (int t = 0; t < 256; ++t) for (int k = 0; k < 12; ++k) e.v[t * 12 + k] = (u16)(k < 11 ? foff_c(2047 - 8 * t - k) * 4u : 0u); return e; }
__device__ const __attribute__((aligned(16))) EoTab EO = make_eo();

DI float bflo(u32 d) { return __uint_as_float(d << 16); }
DI float bfhi(u32 d) { return __uint_as_float(d & 0xffff0000u); }
DI u32 pk2(float lo, float hi) { bf16x2 v = {(__bf16)lo, (__bf16)hi}; return __builtin_bit_cast(u32, v); }
DI float rbf(float f) { return bflo(pk2(f, f)); }
DI bf16x8 as_bf(v4u v) { return __builtin_bit_cast(bf16x8, v); }
DI f32x4 mfma16(v4u a, v4u b, f32x4 c) { return __builtin_amdgcn_mfma_f32_16x16x32_bf16(as_bf(a), as_bf(b), c, 0, 0, 0); }
DI f32x16 mfma32(v4u a, v4u b, f32x16 c) { return __builtin_amdgcn_mfma_f32_32x32x16_bf16(as_bf(a), as_bf(b), c, 0, 0, 0); }
template <class T> DI LAS T* lds(u32 off) { return (LAS T*)(uintptr_t)off; }
DI v2u tr_read(u32 off) { s16x4 r = __builtin_amdgcn_ds_read_tr16_b64_v4i16(lds<s16x4>(off)); return __builtin_bit_cast(v2u, r); }
DI v4u tr_frag(u32 off0, u32 off1) { v2u a = tr_read(off0), b = tr_read(off1); return v4u{a.x, a.y, b.x, b.y}; }
template <int CTRL, int RM> DI float dpp_add(float v) { int t = __builtin_amdgcn_update_dpp(0, __float_as_int(v), CTRL, RM, 0xf, true); return v + __int_as_float(t); }
DI float wave_sum(float v) {
    v = dpp_add<0x111, 0xf>(v); v = dpp_add<0x112, 0xf>(v); v = dpp_add<0x114, 0xf>(v); v = dpp_add<0x118, 0xf>(v);
    v = dpp_add<0x142, 0xa>(v); v = dpp_add<0x143, 0xc>(v);
    return __int_as_float(__builtin_amdgcn_readlane(__float_as_int(v), 63));
}
DI float half_sum(float v) { const u32 u = __float_as_uint(v); const auto r = __builtin_amdgcn_permlane32_swap(u, u, false, false); return __uint_as_float(r[0]) + __uint_as_float(r[1]); }
DI void fence() { asm volatile("" ::: "memory"); }
DI void bar() { asm volatile("s_waitcnt lgkmcnt(0)" ::: "memory"); __builtin_amdgcn_s_barrier(); asm volatile("" ::: "memory"); }
template <int N> DI void vmw() { asm volatile("s_waitcnt vmcnt(%0)" :: "n"(N) : "memory"); }
template <bool S1> DI void glds16(u32 voff, const void* sbase, u32 ldst) {
    u32 keep;
    if (S1) asm volatile("s_mov_b32 %0, m0\n\ts_mov_b32 m0, %3\n\ts_nop 0\n\tglobal_load_lds_dwordx4 %1, %2 sc1\n\ts_mov_b32 m0, %0" : "=&s"(keep) : "v"(voff), "s"(sbase), "s"(ldst) : "memory");
    else    asm volatile("s_mov_b32 %0, m0\n\ts_mov_b32 m0, %3\n\ts_nop 0\n\tglobal_load_lds_dwordx4 %1, %2\n\ts_mov_b32 m0, %0" : "=&s"(keep) : "v"(voff), "s"(sbase), "s"(ldst) : "memory");
}
DI void glds16v(const void* gaddr, u32 ldst) {
    u32 keep;
    asm volatile("s_mov_b32 %0, m0\n\ts_mov_b32 m0, %2\n\ts_nop 0\n\tglobal_load_lds_dwordx4 %1, off\n\ts_mov_b32 m0, %0" : "=&s"(keep) : "v"(gaddr), "s"(ldst) : "memory");
}
DI float dot2(u32 a, u32 b, float c) { return __builtin_amdgcn_fdot2_f32_bf16(__builtin_bit_cast(bf16x2, a), __builtin_bit_cast(bf16x2, b), c, false); }
DI f2 cmul(f2 a, f2 b) { f2 t = f2{a.x, a.x} * b; return __builtin_elementwise_fma(f2{a.y, a.y}, f2{-b.y, b.x}, t); }
DI f2 cexp_rev(float ar, float ai, float t) {
    const float e = __builtin_amdgcn_exp2f(ar * t), ph = __builtin_amdgcn_fractf(ai * t);
    return f2{e * __builtin_amdgcn_cosf(ph), e * __builtin_amdgcn_sinf(ph)};
}
DI u32 swz_b(u32 row) { return ((row & 3u) << 2) | ((row >> 2) & 3u); }
DI u32 off_b(u32 row, u32 ch) { return 256u * row + 16u * (ch ^ swz_b(row)); }
DI u32 off_a(u32 row, u32 ch) { return 2048u * (row >> 3) + 512u * (ch >> 2) + 64u * (row & 7u) + 16u * ((ch & 3u) ^ ((row >> 2) & 3u)); }
DI u32 max4(v4u v) { return max(max(v.x, v.y), max(v.z, v.w)); }
DI u32 san(u32 v) { return min(v, 0xfffffffeu); }
DI u32 ln_pack(float mean, float var) {
    const float m = rbf(mean), sd = rbf(__builtin_amdgcn_sqrtf(rbf(rbf(var) + 1e-5f)));
    return (__float_as_uint(m) >> 16) | (__float_as_uint(sd) & 0xffff0000u);
}

#define STAMP(i) do { } while (0)
#define CSTAMP(i) do { } while (0)
#define STAMP_DECL
#define STAMP_FLUSH do { } while (0)
#define SGB(mask, n) __builtin_amdgcn_sched_group_barrier(mask, n, 0)
#define SB0() __builtin_amdgcn_sched_barrier(0)
constexpr int M_MFMA = 0x8, M_VMEM = 0x10, M_DSR = 0x100, M_VALU = 0x2;
DI void delay_ticks(u32 n) { const u64 t0 = __builtin_amdgcn_s_memrealtime(); while ((u32)(__builtin_amdgcn_s_memrealtime() - t0) < n) __builtin_amdgcn_s_sleep(1); }
DI u32 opaque(u32 v) { asm volatile("" : "+v"(v)); return v; }

template <int TG> struct Tiles;
template <> struct Tiles<0> { static constexpr int n = 3; static constexpr int t[5] = {7, 6, 2, 0, 0}; };
template <> struct Tiles<1> { static constexpr int n = 5; static constexpr int t[5] = {5, 4, 3, 1, 0}; };

template <int TG> DI void conv_wave(int tid, int w, int lane, int cm, float dsv, __amdgpu_buffer_rsrc_t rsW, v4u (&Wf)[28], u32 wf_off) {
    typedef Tiles<TG> T;
    constexpr int NP = T::t[0] + 1;
    const int ch = w >> 2, rh = (w >> 1) & 1;
    f32x4 acc[T::n];
#pragma unroll
    for (int i = 0; i < T::n; ++i) acc[i] = f32x4{0.f, 0.f, 0.f, 0.f};
    const u32 cb = lane & 15, cq = lane >> 4, cs = (3u - cb) & 3u;
    const u32 kbase = opaque(KC + ch * KC_CH + cs * KC_COPY + 2 * (2047 - 16 * (cq >> 1) - cb + 8 * (cq & 1) - cs) - 32 * 8 * rh - 32 * 118);
    const u32 kbase8 = opaque(kbase + 8);
    const u32 ubase = opaque(UU + ch * UU_CH + 512 + 32 * (cb - (cq >> 1)) + 16 * (cq & 1) - 32 * 8 * rh - 32 * 6);
    v4u Af[2][NP], Bf[2][NP];
    auto ld = [&](int rr, v4u (&A)[NP], v4u (&B)[NP]) {
#pragma unroll
        for (int p = 0; p < NP; ++p) {
            const v2u lo = *lds<volatile v2u>(kbase + 32 * (118 - (16 * p + 2 * rr))), hi = *lds<volatile v2u>(kbase8 + 32 * (118 - (16 * p + 2 * rr)));
            A[p] = v4u{lo.x, lo.y, hi.x, hi.y};
            B[p] = *lds<v4u>(ubase + 32 * (16 * p - 2 * rr + 6));
        }
    };
    ld(0, Af[0], Bf[0]);
#pragma unroll
    for (int rr = 0; rr < 4; ++rr) {
        SB0();
        if (rr < 3) ld(rr + 1, Af[(rr + 1) & 1], Bf[(rr + 1) & 1]);
#pragma unroll
        for (int i = 0; i < 3; ++i) Wf[3 * rr + i] = __builtin_amdgcn_raw_buffer_load_b128(rsW, wf_off + (3 * rr + i) * 1024, 0, WAUX);
#pragma unroll
        for (int p = 0; p < NP; ++p)
#pragma unroll
            for (int i = 0; i < T::n; ++i)
                if (p <= T::t[i]) acc[i] = mfma16(Af[rr & 1][p], Bf[rr & 1][T::t[i] - p], acc[i]);
#pragma unroll
        for (int i = 0; i < 18; ++i) {
            SGB(M_MFMA, 1);
            if (rr < 3) { if (NP == 8 && i < 6) SGB(M_DSR, 2); else SGB(M_DSR, 1); }
            if (i == 3 || i == 9 || i == 15) SGB(M_VMEM, 1);
        }
    }
    SB0();
    CSTAMP(30);
    const u32 mine = CX + w * 3072, other = CX + (w ^ 2) * 3072;
    if (rh == 0) {
#pragma unroll
        for (int i = 1; i < T::n; i += 2) *lds<f32x4>(mine + (i >> 1) * 1024 + lane * 16) = acc[i];
    } else {
#pragma unroll
        for (int i = 0; i < T::n; i += 2) *lds<f32x4>(mine + (i >> 1) * 1024 + lane * 16) = acc[i];
    }
    bar();
    CSTAMP(31);
    auto fin = [&](int i, f32x4 own) {
        const f32x4 y = own + *lds<f32x4>(other + (i >> 1) * 1024 + lane * 16);
        const u32 l0 = 256 * T::t[i] + 16 * cb + 4 * cq;
        const v2u uu = *lds<v2u>(UU + ch * UU_CH + 512 + l0 * 2), xx = *lds<v2u>(XS + ch * 4096 + l0 * 2);
        const float uv[4] = {bflo(uu.x), bfhi(uu.x), bflo(uu.y), bfhi(uu.y)}, xv[4] = {bflo(xx.x), bfhi(xx.x), bflo(xx.y), bfhi(xx.y)};
        float h[4];
#pragma unroll
        for (int r = 0; r < 4; ++r) h[r] = xv[r] + rbf(rbf(y[r]) + rbf(uv[r] * dsv));
        const v2u o2 = {pk2(h[0], h[1]), pk2(h[2], h[3])};
        __builtin_amdgcn_raw_buffer_store_b64(o2, rsW, WS_HT + (2 * cm + ch) * 4096 + l0 * 2, 0, SC1);
    };
    if (rh == 0) {
#pragma unroll
        for (int i = 0; i < T::n; i += 2) fin(i, acc[i]);
    } else {
#pragma unroll
        for (int i = 1; i < T::n; i += 2) fin(i, acc[i]);
    }
}

__global__ void __launch_bounds__(512, 2)
fused_kernel(const u16* __restrict__ x, const u16* __restrict__ Are, const u16* __restrict__ Aim, const u16* __restrict__ Bre, const u16* __restrict__ Bim,
             const u16* __restrict__ Cre, const u16* __restrict__ Cim, const u16* __restrict__ logdt, const u16* __restrict__ Dskip,
             const u16* __restrict__ wg, const u16* __restrict__ wu, const u16* __restrict__ wd, u16* __restrict__ out, char* __restrict__ ws)
{
    const int tid = threadIdx.x, lane = tid & 63, w = __builtin_amdgcn_readfirstlane(tid >> 6);
    const int b = blockIdx.x, g = b & 7, j = b >> 3;
    const bool mainb = j < 28;
    const int cm = mainb ? g * 28 + j : 0;
    const int P = 4 * (g >> 1) + j / 7, S = 7 * (g & 1) + j % 7;
    const __amdgpu_buffer_rsrc_t rsW = __builtin_amdgcn_make_buffer_rsrc(ws, 0, WS_END, 0x00020000);
    const __amdgpu_buffer_rsrc_t rsX = __builtin_amdgcn_make_buffer_rsrc((void*)x, 0, L_ * D_ * 2, 0x00020000);
    const u32 trq = (lane & 15) >> 2, trp = lane & 3, trh = lane >> 5, trb = (lane >> 4) & 1;
    STAMP_DECL
    STAMP(0);
    {
        u64 pc; asm volatile("s_getpc_b64 %0" : "=s"(pc));
        pc &= ~255ull;
        for (int q = j * 8 + w; q < CODEPF; q += 256) glds16<false>((u32)(q * 1024 + lane * 16), (const void*)pc, 157696);
    }
    const u64 tstart = __builtin_amdgcn_s_memrealtime();
    v4u xr; u64 ep; u32 p_ld2, p_dk2;
    asm volatile("global_load_dwordx4 %0, %1, %2" : "=v"(xr) : "v"((u32)((8 * b + w) * 896 + min(lane, 55) * 16)), "s"(x) : "memory");
    asm volatile("s_load_dwordx2 %0, %3, 0x0\n\ts_load_dword %1, %4, 0x0\n\ts_load_dword %2, %5, 0x0" : "=&s"(ep), "=&s"(p_ld2), "=&s"(p_dk2)
                 : "s"(ws + WS_EP), "s"((const char*)logdt + cm * 4), "s"((const char*)Dskip + cm * 4) : "memory");
    const int ch = w >> 2, dch = 2 * cm + ch;
    if (mainb) {
        const int ai_ = lane >> 3;
        const u16* src = ai_ == 0 ? Are : ai_ == 1 ? Aim : ai_ == 2 ? Bre : ai_ == 3 ? Bim : ai_ == 4 ? Cre : Cim;
        glds16v(src + dch * N_ + (w & 3) * 64 + (lane & 7) * 8, PRM + w * 1024);
        const int ew = w < 6 ? w : w - 6;
        glds16<false>((u32)(ew * 1024 + lane * 16), (const void*)&EO, EOL + ew * 1024);
        const int bb_ = b < 196 ? b : b - 196;
#pragma unroll
        for (int i = 0; i < 2; ++i) {
            const int id = 2 * bb_ + i, Sp = id / 28, s = id % 28;
            const u32 row = (w & 3) * 4 + (lane >> 4), chk = (lane & 15) ^ swz_b(row);
            glds16<false>((u32)((16 * s + row) * (F_ * 2) + Sp * 256 + chk * 16), (w >> 2) ? (const void*)wu : (const void*)wg, RT + i * 8192 + w * 1024);
        }
    } else {
        const int hb = (j - 28) * 8 + g;
#pragma unroll
        for (int k = 0; k < 7; ++k) {
            const int gid = min(hb + 32 * k, 195);
            const int dt_ = gid / 28, fg = gid % 28;
            const u32 row = 8 * w + (lane >> 3), chk = (lane & 7) ^ (((row >> 1) & 1) << 2);
            glds16<false>((u32)((64 * fg + row) * (D_ * 2) + dt_ * 128 + chk * 16), (const void*)wd, k * 8192 + w * 1024);
        }
    }
    asm volatile("s_cmp_ge_i32 %1, 28\n\ts_cbranch_scc1 1f\n\ts_waitcnt vmcnt(4)\n\ts_branch 2f\n1:\n\ts_waitcnt vmcnt(7)\n2:" : "+v"(xr) : "s"(__builtin_amdgcn_readfirstlane(j)) : "memory", "scc");
    asm volatile("s_waitcnt lgkmcnt(0)" : "+s"(ep), "+s"(p_ld2), "+s"(p_dk2) :: "memory");
    ep += 1ull;
    const u32 p_ldt = ch ? p_ld2 >> 16 : p_ld2 & 0xffffu, p_dsk = ch ? p_dk2 >> 16 : p_dk2 & 0xffffu;
    if (lane >= 56) xr = v4u{0, 0, 0, 0};
    const u32 tag = (u32)ep, tagx = (u32)(ep >> 32) << 16;
    if (lane < 56) *lds<v4u>(XT + w * 912 + lane * 16) = xr;
    {
        const float v[8] = {bflo(xr.x), bfhi(xr.x), bflo(xr.y), bfhi(xr.y), bflo(xr.z), bfhi(xr.z), bflo(xr.w), bfhi(xr.w)};
        const float mean = wave_sum(((v[0] + v[1]) + (v[2] + v[3])) + ((v[4] + v[5]) + (v[6] + v[7]))) * (1.f / 448.f);
        float q = 0.f;
#pragma unroll
        for (int e = 0; e < 8; ++e) { const float dl = v[e] - mean; q += dl * dl; }
        q = lane < 56 ? q : 0.f;
        const u32 ln1 = ln_pack(mean, wave_sum(q) * (1.f / 448.f));
        if (lane == 0) __hip_atomic_store((GAS u64*)(ws + WS_SG) + (8 * b + w), ((u64)tag << 32) | ln1, RLX_AGENT);
    }
    STAMP(1);
    const u32 km0 = FM + ch * FM_CH + (w & 3) * 256;
    f2 Ga, Gb, Gc, Gd, KX, KY, G4, G4r, E1, E1r, E256, E256r, E2, E2r, E4, E4r;
#define M1(a, bb) (f2{(a).x, (a).x} * (bb))
#define M2(a, br, t) __builtin_elementwise_fma(f2{(a).y, (a).y}, (br), (t))
#define WROW1(r, v) do { u32 keep_; asm volatile("s_mov_b32 %0, m0\n\ts_mov_b32 m0, %2\n\ts_nop 0\n\tds_write_addtid_b32 %1 offset:%3\n\ts_mov_b32 m0, %0" : "=&s"(keep_) : "v"(v), "s"(km0), "n"((r) * FM_ROW) : "memory"); } while (0)
#define WROW6(r0, v0, r1, v1, r2, v2, r3, v3, r4, v4, r5, v5) do { u32 keep_; asm volatile("s_mov_b32 %0, m0\n\ts_mov_b32 m0, %7\n\ts_nop 0\n\t" \
        "ds_write_addtid_b32 %1 offset:%8\n\tds_write_addtid_b32 %2 offset:%9\n\tds_write_addtid_b32 %3 offset:%10\n\tds_write_addtid_b32 %4 offset:%11\n\tds_write_addtid_b32 %5 offset:%12\n\tds_write_addtid_b32 %6 offset:%13\n\ts_mov_b32 m0, %0" \
        : "=&s"(keep_) : "v"(v0), "v"(v1), "v"(v2), "v"(v3), "v"(v4), "v"(v5), "s"(km0), "n"((r0) * FM_ROW), "n"((r1) * FM_ROW), "n"((r2) * FM_ROW), "n"((r3) * FM_ROW), "n"((r4) * FM_ROW), "n"((r5) * FM_ROW) : "memory"); } while (0)
#define WA(r, X) WROW1(r, pk2((X).x, -(X).y))
#define KSTEP(t) do { SB0(); \
        const f2 ta = M1(Ga, G4), tb = M1(Gb, G4), tc = M1(Gc, G4), td_ = M1(Gd, G4), tx = M1(KX, E1), ty = M1(KY, E256); \
        const u32 c0 = pk2(Ga.x, Ga.y), c1 = pk2(Gb.x, Gb.y), c2 = pk2(Gc.x, Gc.y), c3 = pk2(Gd.x, Gd.y), c4 = pk2(KX.x, -KX.y), c5 = pk2(KY.x, -KY.y); \
        f2 ts0, ts1; \
        if ((t) == 0) ts0 = M1(KY, E2); \
        if ((t) == 1 || (t) == 2) ts0 = M1(KY, E4); \
        SB0(); \
        WROW6(21 + 4 * (t), c0, 22 + 4 * (t), c1, 23 + 4 * (t), c2, 24 + 4 * (t), c3, (t), c4, ((t) == 0 ? 8 : (t) == 1 ? 12 : (t) == 2 ? 14 : 13 + (t)), c5); \
        if ((t) == 0) { f2 Z = M2(KY, E2r, ts0); WA(9, Z); ts1 = M1(Z, E2); Z = M2(Z, E2r, ts1); WA(10, Z); ts1 = M1(Z, E2); Z = M2(Z, E2r, ts1); WA(11, Z); } \
        if ((t) == 1) { const f2 V = M2(KY, E4r, ts0); WA(13, V); } \
        if ((t) == 2) { const f2 V = M2(KY, E4r, ts0); WA(15, V); } \
        if ((t) < 7) { Ga = M2(Ga, G4r, ta); Gb = M2(Gb, G4r, tb); Gc = M2(Gc, G4r, tc); Gd = M2(Gd, G4r, td_); KX = M2(KX, E1r, tx); KY = M2(KY, E256r, ty); } \
    } while (0)
    if (mainb) {
        vmw<4>();
        const u32 pa_ = PRM + w * 1024 + lane * 2;
        const u32 p_are = *lds<u16>(pa_), p_aim = *lds<u16>(pa_ + 128), p_bre = *lds<u16>(pa_ + 256), p_bim = *lds<u16>(pa_ + 384), p_cre = *lds<u16>(pa_ + 512), p_cim = *lds<u16>(pa_ + 640);
        const float dt = rbf(expf(bflo(p_ldt)));
        const float zr = dt * bflo(p_are), zi = dt * bflo(p_aim);
        const float ar = zr * 1.4426950408889634f, ai = zi * 0.15915494309189535f;
        const f2 CB = cmul(f2{bflo(p_cre), bflo(p_cim)}, f2{dt * bflo(p_bre), dt * bflo(p_bim)});
        E1 = cexp_rev(ar, ai, 1.f); E256 = cexp_rev(ar, ai, 256.f);
        const f2 G1 = cexp_rev(ar, ai, 8.f);
        E2 = cmul(E1, E1); E4 = cmul(E2, E2);
        const f2 G2 = cmul(G1, G1);
        G4 = cmul(G2, G2);
        E1r = f2{-E1.y, E1.x}; E2r = f2{-E2.y, E2.x}; E4r = f2{-E4.y, E4.x}; E256r = f2{-E256.y, E256.x}; G4r = f2{-G4.y, G4.x};
        Ga = f2{1.f, 0.f}; Gb = G1; Gc = G2; Gd = cmul(G2, G1);
        KX = CB; KY = cmul(CB, E256);
        SB0();
    }
    bar();
    STAMP(16);
    auto publish_xg = [&]() {
        u32 x0[4], x1[4];
#pragma unroll
        for (int k = 0; k < 4; ++k) {
            const int id = min(tid + 512 * k, 1791), c = id >> 2, i2 = (id & 3) * 2;
            x0[k] = *lds<u16>(XT + i2 * 912 + c * 2); x1[k] = *lds<u16>(XT + (i2 + 1) * 912 + c * 2);
        }
        SB0();
#pragma unroll
        for (int k = 0; k < 4; ++k)
            __builtin_amdgcn_raw_buffer_store_b128(v4u{x0[k] | tagx, tag, x1[k] | tagx, tag}, rsW, (tid + 512 * k < 1792) ? (u32)(WS_XG + b * 28672 + (tid + 512 * k) * 16) : 0x7ffffff0u, 0, SC1);
    };
    if (mainb) {
    publish_xg();
    STAMP(17);
    KSTEP(0); KSTEP(1); KSTEP(2);
    SB0();
    vmw<5>();
    bar();
    if (b < 196) {
#pragma unroll
        for (int i = 0; i < 2; ++i) {
            const int id = 2 * b + i, Sp = id / 28, s = id % 28;
            const u32 base = RT + i * 8192 + trb * 4096 + 8 * (trp & 1);
            const v4u f = tr_frag(base + off_b(8 * trh + trq, 2 * w + (trp >> 1)), base + off_b(8 * trh + 4 + trq, 2 * w + (trp >> 1)));
            __builtin_amdgcn_raw_buffer_store_b128(f, rsW, WS_WG + ((Sp * 8 + w) * 28 + s) * 1024 + lane * 16, 0, SC1);
        }
    }
    fence();
    STAMP(2);
    {
        KSTEP(3); KSTEP(4); KSTEP(5); KSTEP(6); KSTEP(7);
        SB0();
#undef KSTEP
#undef WA
#undef WROW1
#undef WROW6
#undef M1
#undef M2
        STAMP(3);
        vmw<0>();
        bar();
        if (tid == 0 && b < 196) __hip_atomic_store((GAS u32*)(ws + WS_WR + b * 64), tag, RLX_AGENT);
        const int ta_ = (w >> 1) & 1, tb_ = w & 1;
        const u32 fa_ = opaque(FM + ch * FM_CH + (16 * ta_ + (lane & 15)) * FM_ROW + (lane >> 4) * 16), fb_ = opaque(FM + ch * FM_CH + (21 + 16 * tb_ + (lane & 15)) * FM_ROW + (lane >> 4) * 16);
        f32x4 f0 = {0.f, 0.f, 0.f, 0.f}, f1 = {0.f, 0.f, 0.f, 0.f};
        v4u A[16], B[16];
#pragma unroll
        for (int ks = 0; ks < 16; ++ks) { A[ks] = *lds<v4u>(fa_ + ks * 64); B[ks] = *lds<v4u>(fb_ + ks * 64); }
        SB0();
#pragma unroll
        for (int ks = 0; ks < 16; ks += 2) { f0 = mfma16(A[ks], B[ks], f0); f1 = mfma16(A[ks + 1], B[ks + 1], f1); }
        f0 += f1;
#pragma unroll
        for (int r = 0; r < 4; ++r) { const int row = 16 * ta_ + 4 * (lane >> 4) + r; if (row < 24) *lds<float>(FT + ch * FT_CH + (row * 32 + 16 * tb_ + (lane & 15)) * 4) = f0[r]; }
        if ((tid & 255) == 255) *lds<float>(FT + ch * FT_CH + 768 * 4) = 0.f;
    }
    STAMP(4);
    {
        v4u xg[4], sg[4];
        const u32 fa = WS_WR + (tid < 196 ? tid : 0) * 64;
        const u32 xa = WS_XG + (32 * w + (lane >> 3)) * 28672 + cm * 128 + (lane & 7) * 16, sa = WS_SG + (32 * w + (lane >> 3)) * 64 + (lane & 3) * 16;
        if (DLY1) { while ((u32)(__builtin_amdgcn_s_memrealtime() - tstart) < DLY1) __builtin_amdgcn_s_sleep(1); }
        for (u32 spin = 0;; ++spin) {
#pragma unroll
            for (int k = 0; k < 4; ++k) { xg[k] = __builtin_amdgcn_raw_buffer_load_b128(rsW, xa + k * 8 * 28672, 0, SC1); sg[k] = __builtin_amdgcn_raw_buffer_load_b128(rsW, sa + k * 512, 0, SC1); }
            const u32 fl = __builtin_amdgcn_raw_buffer_load_b32(rsW, fa, 0, SC1);
            bool ok = fl == tag;
#pragma unroll
            for (int k = 0; k < 4; ++k) ok = ok && sg[k].y == tag && sg[k].w == tag && xg[k].y == tag && xg[k].w == tag && (xg[k].x & 0xffff0000u) == tagx && (xg[k].z & 0xffff0000u) == tagx;
            if (__all(ok) || spin > SPIN_CAP) break;
            __builtin_amdgcn_s_sleep(8);
        }
        STAMP(5);
        const int c = (lane >> 2) & 1;
#pragma unroll
        for (int k = 0; k < 4; ++k) {
            const u32 l0 = 8 * (32 * w + 8 * k + (lane >> 3)) + 2 * (lane & 3);
            const float u0 = rbf(bflo(xg[k].x) - bflo(sg[k].x)) * __builtin_amdgcn_rcpf(bfhi(sg[k].x));
            const float u1 = rbf(bflo(xg[k].z) - bflo(sg[k].z)) * __builtin_amdgcn_rcpf(bfhi(sg[k].z));
            *lds<u32>(UU + c * UU_CH + 512 + l0 * 2) = pk2(u0, u1);
            *lds<u32>(XS + c * 4096 + l0 * 2) = (xg[k].x & 0xffffu) | (xg[k].z << 16);
        }
    }
    if (tid >= 64 && tid < 128) *lds<v4u>(UU + ((tid >> 5) & 1) * UU_CH + (tid & 31) * 16) = v4u{0, 0, 0, 0};
    bar();
    STAMP(18);
    {
        const v2u ea = *lds<v2u>(EOL + (tid & 255) * 24), eb = *lds<v2u>(EOL + (tid & 255) * 24 + 8), ec = *lds<v2u>(EOL + (tid & 255) * 24 + 16);
        const v4u eo0 = {ea.x, ea.y, eb.x, eb.y}; const v2u eo1 = ec;
        const u32 eo[11] = {eo0.x & 0xffffu, eo0.x >> 16, eo0.y & 0xffffu, eo0.y >> 16, eo0.z & 0xffffu, eo0.z >> 16, eo0.w & 0xffffu, eo0.w >> 16, eo1.x & 0xffffu, eo1.x >> 16, eo1.y & 0xffffu};
        float v[11];
#pragma unroll
        for (int e = 0; e < 11; ++e) v[e] = *lds<float>(FT + ch * FT_CH + eo[e]);
        SB0();
        u32 p[10];
#pragma unroll
        for (int e = 0; e < 10; ++e) p[e] = pk2(v[e], v[e + 1]);
#pragma unroll
        for (int s = 0; s < 4; ++s) *lds<v4u>(KC + ch * KC_CH + s * KC_COPY + (tid & 255) * 16) = v4u{p[s], p[s + 2], p[s + 4], p[s + 6]};
    }
    if (tid < 16) *lds<v4u>(KC + (tid >> 3) * KC_CH + ((tid >> 1) & 3) * KC_COPY + 4096 + (tid & 1) * 16) = v4u{0, 0, 0, 0};
    bar();
    STAMP(6);
    CSTAMP(26);
    v4u Wf[28];
    const u32 wf_off = WS_WG + (S * 8 + w) * 28 * 1024 + lane * 16;
    {

        if (w & 1) conv_wave<1>(tid, w, lane, cm, bflo(p_dsk), rsW, Wf, wf_off); else conv_wave<0>(tid, w, lane, cm, bflo(p_dsk), rsW, Wf, wf_off);
    }
    STAMP(7);
    CSTAMP(27);
    const int l0p = 128 * P;
    vmw<0>();
    bar();
    {
        if (tid < 8) __hip_atomic_store((GAS u32*)(ws + WS_F2 + tid * 16384 + cm * 64), tag, RLX_AGENT);
        fence();
#pragma unroll
        for (int i = 12; i < 28; ++i) Wf[i] = __builtin_amdgcn_raw_buffer_load_b128(rsW, wf_off + i * 1024, 0, WAUX);
        fence();
        delay_ticks(DLY2);
        if (w < 4) {
            const u32 pa = tid < 224 ? (u32)(WS_F2 + g * 16384 + tid * 64) : (u32)(WS_WF + (tid - 224) * 64);
            for (u32 spin = 0;; ++spin) {
                const u32 s0 = __builtin_amdgcn_raw_buffer_load_b32(rsW, pa, 0, SC1);
                if (__all(s0 == tag) || spin > SPIN_CAP) break;
                __builtin_amdgcn_s_sleep(8);
            }
        }
    }
    bar();
    STAMP(8);
    if (b == 0 && tid == 0) __hip_atomic_store((GAS u64*)(ws + WS_EP), ep, RLX_AGENT);
    const u32 bt_src = (8 * (w >> 1) + ((lane >> 2) & 7)) * 4096 + l0p * 2 + (4 * (2 * (w & 1) + (lane >> 5)) + ((lane & 3) ^ ((2 * (w >> 1) + ((lane >> 4) & 1)) & 3))) * 16;
    const u32 bt_dst = BT + (w >> 1) * 2048 + (w & 1) * 1024;
    auto bt_piece = [&](int k) { glds16<BTSC1>(bt_src + k * 32 * 4096, ws + WS_HT, bt_dst + k * 8192); };
    const float csum = __uint_as_float(__builtin_amdgcn_raw_buffer_load_b32(rsW, WS_CS + (((lane >> 4) & 1) * 1792 + 128 * S + 16 * w + (lane & 15)) * 4, 0, SC1));
    fence();
#pragma unroll
    for (int k = 0; k < 6; ++k) bt_piece(k);
    vmw<4>();
    bar();
    STAMP(9);
    CSTAMP(24);
    f32x16 acc[4];
    v4u Wd[28];
    const int td = w & 1, kq = w >> 1, hp = S / 7, dtile = S % 7;
    const u32 wd_off = WS_WD + ((dtile * 2 + td) * 112 + 28 * kq) * 1024 + lane * 16;
    u32 bb[2];
#pragma unroll
    for (int tt = 0; tt < 2; ++tt) bb[tt] = opaque(BT + 2048 * trh + 64 * (4 * tt + trq) + 16 * ((2 * trb + (trp >> 1)) ^ ((2 * trh + tt) & 3)) + 8 * (trp & 1));
    {
#pragma unroll
        for (int t = 0; t < 4; ++t)
#pragma unroll
            for (int i = 0; i < 16; ++i) acc[t][i] = 0.f;
        v4u Bf[2][4], Of;
        float sa0 = 0.f, sa1 = 0.f, sq0 = 0.f, sq1 = 0.f;
        const u32 bh0 = opaque(bb[0] + 65536), bh1 = opaque(bb[1] + 65536);
        const u32 so0 = opaque(bb[0] + (w >> 2) * 4096 + (w & 3) * 512), so1 = opaque(bb[1] + (w >> 2) * 4096 + (w & 3) * 512);
        const u32 sh0 = opaque(so0 + 65536), sh1 = opaque(so1 + 65536);
#pragma unroll
        for (int t = 0; t < 4; ++t) Bf[0][t] = tr_frag(bb[0] + 512 * t, bb[1] + 512 * t);
#pragma unroll
        for (int s = 0; s < 28; ++s) {
            SB0();
            if (s == 3) { vmw<6>(); bar(); }
            if (s == 11) { vmw<7>(); bar(); }
            SB0();
            if (s + 1 < 28) {
#pragma unroll
                for (int t = 0; t < 4; ++t)
                    Bf[(s + 1) & 1][t] = (s + 1 < 16) ? tr_frag(bb[0] + (s + 1) * 4096 + 512 * t, bb[1] + (s + 1) * 4096 + 512 * t)
                                                       : tr_frag(bh0 + (s + 1 - 16) * 4096 + 512 * t, bh1 + (s + 1 - 16) * 4096 + 512 * t);
            }
            if ((s & 1) == 0) Of = (s < 16) ? tr_frag(so0 + (s >> 1) * 8192, so1 + (s >> 1) * 8192) : tr_frag(sh0 + ((s - 16) >> 1) * 8192, sh1 + ((s - 16) >> 1) * 8192);
            if (s < 4) { bt_piece(6 + 2 * s); bt_piece(7 + 2 * s); }
            else Wd[s - 4] = __builtin_amdgcn_raw_buffer_load_b128(rsW, wd_off + (s - 4) * 1024, 0, WAUX);
#pragma unroll
            for (int t = 0; t < 4; ++t) acc[t] = mfma32(Wf[s], Bf[s & 1][t], acc[t]);
            if (s & 1) {
#pragma unroll
                for (int e = 0; e < 4; e += 2) {
                    const float l0 = bflo(Of[e]), h0 = bfhi(Of[e]), l1 = bflo(Of[e + 1]), h1 = bfhi(Of[e + 1]);
                    sa0 += l0; sq0 = __builtin_fmaf(l0, l0, sq0); sa1 += l1; sq1 = __builtin_fmaf(l1, l1, sq1);
                    sa0 += h0; sq0 = __builtin_fmaf(h0, h0, sq0); sa1 += h1; sq1 = __builtin_fmaf(h1, h1, sq1);
                }
            }
            if (s >= 4) {
                if (s & 1) { SGB(M_MFMA, 1); SGB(M_DSR, 2); SGB(M_VALU, 6); SGB(M_MFMA, 1); SGB(M_DSR, 2); SGB(M_VALU, 6); SGB(M_VMEM, 1); SGB(M_MFMA, 1); SGB(M_DSR, 2); SGB(M_VALU, 6); SGB(M_MFMA, 1); SGB(M_DSR, 2); SGB(M_VALU, 6); }
                else { SGB(M_MFMA, 1); SGB(M_DSR, 3); SGB(M_MFMA, 1); SGB(M_DSR, 3); SGB(M_VMEM, 1); SGB(M_MFMA, 1); SGB(M_DSR, 2); SGB(M_MFMA, 1); SGB(M_DSR, 2); }
            }
        }
        float sumh = sa0 + sa1, sq = sq0 + sq1;

        SB0();
    #pragma unroll
        for (int s = 24; s < 28; ++s) Wd[s] = __builtin_amdgcn_raw_buffer_load_b128(rsW, wd_off + s * 1024, 0, WAUX);

        sumh = half_sum(sumh); sq = half_sum(sq);
        if (lane < 32) { *lds<float>(SP + w * 128 + lane * 4) = sumh; *lds<float>(SQ + w * 128 + lane * 4) = sq; *lds<float>(CSL + w * 128 + lane * 4) = csum; }
    }
    STAMP(10);
    CSTAMP(25);
    bar();
    {
        f32x4 csg[2], csu[2];
#pragma unroll
        for (int q = 0; q < 2; ++q) { csg[q] = *lds<f32x4>(CSL + w * 128 + (8 * q + 4 * trh) * 4); csu[q] = *lds<f32x4>(CSL + w * 128 + 64 + (8 * q + 4 * trh) * 4); }
#pragma unroll
        for (int t = 0; t < 4; ++t) {
            const float m = rbf((*lds<float>(SP + t * 128 + (lane & 31) * 4) + *lds<float>(SP + (t + 4) * 128 + (lane & 31) * 4)) * (1.f / 448.f));
            float hv[8];
#pragma unroll
            for (int e = 0; e < 8; ++e) hv[e] = (acc[t][e] - m * csg[e >> 2][e & 3]) * (acc[t][e + 8] - m * csu[e >> 2][e & 3]);
            const v4u o = {pk2(hv[0], hv[1]), pk2(hv[2], hv[3]), pk2(hv[4], hv[5]), pk2(hv[6], hv[7])};
            __builtin_amdgcn_raw_buffer_store_b128(o, rsW, WS_HH + ((P * 112 + 8 * S + w) * 4 + t) * 1024 + lane * 16, 0, SC1);
        }
    }
    STAMP(11);
    const int tl_f = kq >> 1;
    const u32 lp = 64 * hp + 32 * tl_f + (lane & 31);
    float hres[8], rs;
    {
        const int tq = 2 * hp + tl_f;
        const float s1 = *lds<float>(SP + tq * 128 + (lane & 31) * 4) + *lds<float>(SP + (tq + 4) * 128 + (lane & 31) * 4);
        const float s2 = *lds<float>(SQ + tq * 128 + (lane & 31) * 4) + *lds<float>(SQ + (tq + 4) * 128 + (lane & 31) * 4);
        const float mean = s1 * (1.f / 448.f), var = fmaxf(s2 * (1.f / 448.f) - mean * mean, 0.f);
        const float sd = bfhi(ln_pack(mean, var));
        rs = 1.f / (sd * sd);
#pragma unroll
        for (int e = 0; e < 8; ++e) {
            const u32 row = 64 * dtile + 32 * td + 16 * (kq & 1) + 8 * (e >> 2) + 4 * trh + (e & 3);
            hres[e] = bflo(*lds<u16>(BT + off_a(row, lp >> 3) + (lp & 7) * 2));
        }
    }
    vmw<0>();
    bar();
    if (tid == 0) __hip_atomic_store((GAS u32*)(ws + WS_F3 + (P * 16 + S) * 64), tag, RLX_AGENT);
    const u32 hbase = WS_HH + ((P * 112 + 28 * kq) * 4 + 2 * hp) * 1024;
    if (w == 0) {
        delay_ticks(DLY3);
        const u32 pa = WS_F3 + (P * 16 + (lane < 14 ? lane : 0)) * 64;
        for (u32 spin = 0;; ++spin) {
            const u32 s0 = __builtin_amdgcn_raw_buffer_load_b32(rsW, pa, 0, SC1);
            if (__all(s0 == tag) || spin > SPIN_CAP) break;
            __builtin_amdgcn_s_sleep(8);
        }
    }
    bar();
    STAMP(12);
    const u32 hsrc = td * 1024 + lane * 16, hdst = RING + kq * 2048 + td * 1024, hrd = opaque(RING + kq * 2048 + lane * 16);
    auto hh_dma = [&](int s) { glds16<true>(hsrc + s * 4096, ws + hbase, hdst + (s % RING_SLOTS) * 8192); };
    f32x16 o2[2];
    {
#pragma unroll
        for (int i = 0; i < 16; ++i) { o2[0][i] = 0.f; o2[1][i] = 0.f; }
        constexpr int AH = 9;
#pragma unroll
        for (int s = 0; s < AH; ++s) hh_dma(s);
        v4u hf[2][2];
        vmw<AH - 4>(); bar();
        STAMP(13); CSTAMP(28);
#pragma unroll
        for (int tl = 0; tl < 2; ++tl) hf[0][tl] = *lds<v4u>(hrd + tl * 1024);
#pragma unroll
        for (int s = 0; s < 28; ++s) {
            SB0();
            if ((s & 3) == 3 && s + 1 < 28) {
                const int issued = (s - 1 + AH < 27) ? s - 1 + AH : 27, need = (s + 4 < 27) ? s + 4 : 27;
                switch (issued - need) { case 0: vmw<0>(); break; case 1: vmw<1>(); break; case 2: vmw<2>(); break; case 3: vmw<3>(); break; default: vmw<4>(); break; }
                bar();
                SB0();
            }
            if (s + 1 < 28) {
#pragma unroll
                for (int tl = 0; tl < 2; ++tl) hf[(s + 1) & 1][tl] = *lds<v4u>(hrd + ((s + 1) % RING_SLOTS) * 8192 + tl * 1024);
            }
            if (s + AH < 28) hh_dma(s + AH);
#pragma unroll
            for (int tl = 0; tl < 2; ++tl) o2[tl] = mfma32(Wd[s], hf[s & 1][tl], o2[tl]);
        }
        SB0();
        bar();
    }
    STAMP(14);
    CSTAMP(29);
    {
#pragma unroll
        for (int k2 = 0; k2 < 4; ++k2) {
            if (k2 != kq) {
                const int si = (kq > k2) ? kq - 1 : kq;
                const f32x16& a = o2[k2 >> 1];
#pragma unroll
                for (int q = 0; q < 2; ++q) { const int r0 = 8 * (k2 & 1) + 4 * q; *lds<f32x4>(((td * 4 + k2) * 3 + si) * 2048 + q * 1024 + lane * 16) = f32x4{a[r0], a[r0 + 1], a[r0 + 2], a[r0 + 3]}; }
            }
        }
        bar();
        f32x4 v[2];
#pragma unroll
        for (int q = 0; q < 2; ++q) {
            if (kq == 0) v[q] = f32x4{o2[0][4 * q], o2[0][4 * q + 1], o2[0][4 * q + 2], o2[0][4 * q + 3]};
            else if (kq == 1) v[q] = f32x4{o2[0][8 + 4 * q], o2[0][9 + 4 * q], o2[0][10 + 4 * q], o2[0][11 + 4 * q]};
            else if (kq == 2) v[q] = f32x4{o2[1][4 * q], o2[1][4 * q + 1], o2[1][4 * q + 2], o2[1][4 * q + 3]};
            else v[q] = f32x4{o2[1][8 + 4 * q], o2[1][9 + 4 * q], o2[1][10 + 4 * q], o2[1][11 + 4 * q]};
#pragma unroll
            for (int si = 0; si < 3; ++si) v[q] += *lds<f32x4>(((td * 4 + kq) * 3 + si) * 2048 + q * 1024 + lane * 16);
            const u32 d0 = 64 * dtile + 32 * td + 16 * (kq & 1) + 8 * q + 4 * trh;
            float r[4];
#pragma unroll
            for (int e = 0; e < 4; ++e) r[e] = hres[4 * q + e] + rbf(v[q][e] * rs);
            *(GAS v2u*)(out + (size_t)(l0p + lp) * D_ + d0) = v2u{pk2(r[0], r[1]), pk2(r[2], r[3])};
        }
    }
    STAMP(15);
    STAMP_FLUSH;
    while ((u32)(__builtin_amdgcn_s_memrealtime() - tstart) < SPINEND) __builtin_amdgcn_s_sleep(2);
    } else {
        publish_xg();
        const int hb = (j - 28) * 8 + g;
        float cs[8];
        {
            const u16* wsrc = hb < 16 ? wg : wu;
            const int c0 = 112 * (hb & 15) + 8 * (tid % 14), r0 = tid / 14;
#pragma unroll
            for (int e = 0; e < 8; ++e) cs[e] = 0.f;
            v4u t[13];
#pragma unroll
            for (int i = 0; i < 13; ++i) { const int r = r0 + 36 * i; t[i] = (tid < 504 && r < 448) ? *(const v4u*)(wsrc + (size_t)r * F_ + c0) : v4u{0, 0, 0, 0}; }
#pragma unroll
            for (int i = 0; i < 13; ++i) { cs[0] += bflo(t[i].x); cs[1] += bfhi(t[i].x); cs[2] += bflo(t[i].y); cs[3] += bfhi(t[i].y); cs[4] += bflo(t[i].z); cs[5] += bfhi(t[i].z); cs[6] += bflo(t[i].w); cs[7] += bfhi(t[i].w); }
        }
        vmw<0>(); bar();
#pragma unroll
        for (int k = 0; k < 7; ++k) {
            const int gid = hb + 32 * k;
            if (gid < 196) {
                const int dt_ = gid / 28, fg = gid % 28, fsl = w >> 1, td = w & 1;
                u32 ad[2];
#pragma unroll
                for (int t = 0; t < 2; ++t) { const u32 row = 16 * fsl + 8 * t + 4 * trh + trq, chk = 4 * td + 2 * trb + (trp >> 1); ad[t] = k * 8192 + row * 128 + 16 * (chk ^ (((row >> 1) & 1) << 2)) + 8 * (trp & 1); }
                const v4u f = tr_frag(ad[0], ad[1]);
                __builtin_amdgcn_raw_buffer_store_b128(f, rsW, WS_WD + ((dt_ * 2 + td) * 112 + 4 * fg + fsl) * 1024 + lane * 16, 0, SC1);
            }
        }
        if (tid < 504) {
            *lds<f32x4>(65536 + tid * 32) = f32x4{cs[0], cs[1], cs[2], cs[3]};
            *lds<f32x4>(65536 + tid * 32 + 16) = f32x4{cs[4], cs[5], cs[6], cs[7]};
        }
        bar();
        if (tid < 112) {
            float s = 0.f;
            for (int r = 0; r < 36; ++r) s += *lds<float>(65536 + (r * 14 + (tid >> 3)) * 32 + (tid & 7) * 4);
            __hip_atomic_store((GAS u32*)(ws + WS_CS) + ((hb < 16 ? 0 : 1792) + 112 * (hb & 15) + tid), __float_as_uint(s), RLX_AGENT);
        }
        vmw<0>(); bar();
        if (tid == 0) __hip_atomic_store((GAS u32*)(ws + WS_WF + hb * 64), tag, RLX_AGENT);

    }
}

extern "C" void kernel_launch(void* const* d_in, const int* in_sizes, int n_in, void* d_out, int out_size, void* d_ws, size_t ws_size, hipStream_t stream) {
    (void)in_sizes; (void)n_in; (void)out_size; (void)ws_size;
    (void)hipFuncSetAttribute((const void*)fused_kernel, hipFuncAttributeMaxDynamicSharedMemorySize, LDS_BYTES);
    fused_kernel<<<256, 512, LDS_BYTES, stream>>>((const u16*)d_in[0], (const u16*)d_in[1], (const u16*)d_in[2], (const u16*)d_in[3], (const u16*)d_in[4],
        (const u16*)d_in[5], (const u16*)d_in[6], (const u16*)d_in[7], (const u16*)d_in[8], (const u16*)d_in[10], (const u16*)d_in[11], (const u16*)d_in[12],
        (u16*)d_out, (char*)d_ws);
}
```

```cpp
#include <hip/hip_runtime.h>
#include <stdint.h>
#include <type_traits>

typedef unsigned short u16;
typedef unsigned u32;
typedef unsigned long long u64;
typedef u32 v4u __attribute__((ext_vector_type(4)));
typedef u32 v2u __attribute__((ext_vector_type(2)));
typedef float f32x4 __attribute__((ext_vector_type(4)));
typedef float f32x16 __attribute__((ext_vector_type(16)));
typedef float f2 __attribute__((ext_vector_type(2)));
typedef __bf16 bf16x8 __attribute__((ext_vector_type(8)));
typedef __bf16 bf16x2 __attribute__((ext_vector_type(2)));
typedef short s16x4 __attribute__((ext_vector_type(4)));
#define LAS __attribute__((address_space(3)))
#define GAS __attribute__((address_space(1)))
#define DI __device__ __forceinline__
#define RLX_AGENT __ATOMIC_RELAXED, __HIP_MEMORY_SCOPE_AGENT
#define CODEPF 26
#define SC1 16
#define EXPS 0
#define EXPA 0
#define BTSC1 true
#define DLY1 0
#define DLY2 35
#define DLY3 35
#define WAUX 16

constexpr int L_ = 2048, D_ = 448, N_ = 256, F_ = 1792;
constexpr u32 WS_EP = 0;
constexpr u32 WS_SG = 4096;
constexpr u32 WS_WF = 32768;
constexpr u32 WS_CS = 40960;
constexpr u32 WS_HT = 65536;
constexpr u32 WS_WR = 1966080;
constexpr u32 WS_HH = 2u << 20;
constexpr u32 WS_WG = 10u << 20;
constexpr u32 WS_WD = 14u << 20;
constexpr u32 WS_XG = 16u << 20;
constexpr u32 WS_F2 = (23u << 20) + (512u << 10);
constexpr u32 WS_F3 = WS_F2 + 2 * 8 * 16384;
constexpr u32 WS_END = 24u << 20;
constexpr u32 WS_DBG = 24u << 20;
constexpr u32 SPIN_CAP = 1u << 16;

constexpr u32 LDS_BYTES = 163840;
constexpr u32 FM = 0, FM_ROW = 1056, FM_CH = 53 * FM_ROW;
constexpr u32 FT = 2 * FM_CH, FT_CH = 772 * 4;
constexpr u32 EOL = FT + 2 * FT_CH;
constexpr u32 RT = 124288;
constexpr u32 PRM = RT + 16384;
constexpr u32 XT = PRM + 8192;
constexpr u32 KC = 0, KC_COPY = 4160, KC_CH = 4 * KC_COPY;
constexpr u32 CX = 2 * KC_CH;
constexpr u32 UU = RT, UU_CH = (256 + 2048) * 2;
constexpr u32 XS = UU + 2 * UU_CH;
static_assert(EOL % 16 == 0 && EOL + 6144 <= RT && XS + 8192 <= XT && XT + 8 * 912 <= 156672, "LDS map");
constexpr u32 BT_PART = 448 * 128;
constexpr u32 BTA = CX + 32768, BTB = 0;
constexpr u32 RSP = BTA + BT_PART;
constexpr u32 SM = 155648;
constexpr u32 SPQ = SM;
constexpr u32 CSL = SPQ + 4096;
constexpr u32 GW = CSL + 1024;
static_assert(BTA + BT_PART <= RT && RSP + 7 * 4096 <= SM && GW + 64 <= 161280, "LDS map");
constexpr u32 hh_slot(int u) {
    return u < 14 ? BTA + u * 4096 : u < 21 ? RSP + (u - 14) * 4096 : u < 35 ? BTB + (u - 21) * 4096 : u < 49 ? BTA + (u - 35) * 4096 : RSP + (u - 49) * 4096;
}

struct EoTab { u16 v[256 * 12]; };
constexpr u32 foff_c(int j) {
    if (j < 0) return 768u;
    u32 t = (u32)j;
    int s = t >= 1024 ? 3 : t >= 512 ? 2 : t >= 256 ? 1 : 0;
    if (s > 0) t = ((t + (1u << (s - 1)) - 1u + ((t >> s) & 1u)) >> s) << s;
    u32 col = (t >> 3) & 31u, row = 0;
    if (t < 256u) row = t & 7u;
    else if (t < 512u) row = 8u + ((t >> 1) & 3u);
    else if (t < 1024u) row = 12u + 2u * ((t >> 8) & 1u) + ((t >> 2) & 1u);
    else if (t < 2048u) row = 16u + ((t >> 8) & 3u);
    else row = 20u;
    return row * 32u + col;
}
constexpr EoTab make_eo() { EoTab e{}; for (int t = 0; t < 256; ++t) for (int k = 0; k < 12; ++k) e.v[t * 12 + k] = (u16)(k < 11 ? foff_c(2047 - 8 * t - k) * 4u : 0u); return e; }
__device__ const __attribute__((aligned(16))) EoTab EO = make_eo();

DI float bflo(u32 d) { return __uint_as_float(d << 16); }
DI float bfhi(u32 d) { return __uint_as_float(d & 0xffff0000u); }
DI u32 pk2(float lo, float hi) { bf16x2 v = {(__bf16)lo, (__bf16)hi}; return __builtin_bit_cast(u32, v); }
DI float rbf(float f) { return bflo(pk2(f, f)); }
DI bf16x8 as_bf(v4u v) { return __builtin_bit_cast(bf16x8, v); }
DI f32x4 mfma16(v4u a, v4u b, f32x4 c) { return __builtin_amdgcn_mfma_f32_16x16x32_bf16(as_bf(a), as_bf(b), c, 0, 0, 0); }
DI f32x16 mfma32(v4u a, v4u b, f32x16 c) { return __builtin_amdgcn_mfma_f32_32x32x16_bf16(as_bf(a), as_bf(b), c, 0, 0, 0); }
template <class T> DI LAS T* lds(u32 off) { return (LAS T*)(uintptr_t)off; }
DI v2u tr_read(u32 off) { s16x4 r = __builtin_amdgcn_ds_read_tr16_b64_v4i16(lds<s16x4>(off)); return __builtin_bit_cast(v2u, r); }
DI v4u tr_frag(u32 off0, u32 off1) { v2u a = tr_read(off0), b = tr_read(off1); return v4u{a.x, a.y, b.x, b.y}; }
template <int CTRL, int RM> DI float dpp_add(float v) { int t = __builtin_amdgcn_update_dpp(0, __float_as_int(v), CTRL, RM, 0xf, true); return v + __int_as_float(t); }
DI float wave_sum(float v) {
    v = dpp_add<0x111, 0xf>(v); v = dpp_add<0x112, 0xf>(v); v = dpp_add<0x114, 0xf>(v); v = dpp_add<0x118, 0xf>(v);
    v = dpp_add<0x142, 0xa>(v); v = dpp_add<0x143, 0xc>(v);
    return __int_as_float(__builtin_amdgcn_readlane(__float_as_int(v), 63));
}
DI float half_sum(float v) { const u32 u = __float_as_uint(v); const auto r = __builtin_amdgcn_permlane32_swap(u, u, false, false); return __uint_as_float(r[0]) + __uint_as_float(r[1]); }
DI void fence() { asm volatile("" ::: "memory"); }
DI void bar() { asm volatile("s_waitcnt lgkmcnt(0)" ::: "memory"); __builtin_amdgcn_s_barrier(); asm volatile("" ::: "memory"); }
template <int N> DI void vmw() { asm volatile("s_waitcnt vmcnt(%0)" :: "n"(N) : "memory"); }
template <bool S1> DI void glds16(u32 voff, const void* sbase, u32 ldst) {
    u32 keep;
    if (S1) asm volatile("s_mov_b32 %0, m0\n\ts_mov_b32 m0, %3\n\ts_nop 0\n\tglobal_load_lds_dwordx4 %1, %2 sc1\n\ts_mov_b32 m0, %0" : "=&s"(keep) : "v"(voff), "s"(sbase), "s"(ldst) : "memory");
    else    asm volatile("s_mov_b32 %0, m0\n\ts_mov_b32 m0, %3\n\ts_nop 0\n\tglobal_load_lds_dwordx4 %1, %2\n\ts_mov_b32 m0, %0" : "=&s"(keep) : "v"(voff), "s"(sbase), "s"(ldst) : "memory");
}
DI void glds16v(const void* gaddr, u32 ldst) {
    u32 keep;
    asm volatile("s_mov_b32 %0, m0\n\ts_mov_b32 m0, %2\n\ts_nop 0\n\tglobal_load_lds_dwordx4 %1, off\n\ts_mov_b32 m0, %0" : "=&s"(keep) : "v"(gaddr), "s"(ldst) : "memory");
}
DI float dot2(u32 a, u32 b, float c) { return __builtin_amdgcn_fdot2_f32_bf16(__builtin_bit_cast(bf16x2, a), __builtin_bit_cast(bf16x2, b), c, false); }
DI f2 cmul(f2 a, f2 b) { f2 t = f2{a.x, a.x} * b; return __builtin_elementwise_fma(f2{a.y, a.y}, f2{-b.y, b.x}, t); }
DI f2 cexp_rev(float ar, float ai, float t) {
    const float e = __builtin_amdgcn_exp2f(ar * t), ph = __builtin_amdgcn_fractf(ai * t);
    return f2{e * __builtin_amdgcn_cosf(ph), e * __builtin_amdgcn_sinf(ph)};
}
DI u32 swz_b(u32 row) { return ((row & 3u) << 2) | ((row >> 2) & 3u); }
DI u32 off_b(u32 row, u32 ch) { return 256u * row + 16u * (ch ^ swz_b(row)); }
DI u32 off_h(u32 row, u32 ch) { return 1024u * (row >> 3) + 512u * (ch >> 2) + 64u * (row & 7u) + 16u * ((ch & 3u) ^ ((row >> 2) & 3u)); }
DI u32 ln_pack(float mean, float var) {
    const float m = rbf(mean), sd = rbf(__builtin_amdgcn_sqrtf(rbf(rbf(var) + 1e-5f)));
    return (__float_as_uint(m) >> 16) | (__float_as_uint(sd) & 0xffff0000u);
}

#define STAMP(i) do { } while (0)
#define CSTAMP(i) do { } while (0)
#define STAMP_DECL
#define STAMP_FLUSH do { } while (0)
#define SGB(mask, n) __builtin_amdgcn_sched_group_barrier(mask, n, 0)
#define SB0() __builtin_amdgcn_sched_barrier(0)
constexpr int M_MFMA = 0x8, M_VMEM = 0x10, M_DSR = 0x100, M_VALU = 0x2;
DI void delay_ticks(u32 n) { const u64 t0 = __builtin_amdgcn_s_memrealtime(); while ((u32)(__builtin_amdgcn_s_memrealtime() - t0) < n) __builtin_amdgcn_s_sleep(1); }
DI u32 opaque(u32 v) { asm volatile("" : "+v"(v)); return v; }

__global__ void __launch_bounds__(512, 2)
fused_kernel(const u16* __restrict__ x, const u16* __restrict__ Are, const u16* __restrict__ Aim, const u16* __restrict__ Bre, const u16* __restrict__ Bim,
             const u16* __restrict__ Cre, const u16* __restrict__ Cim, const u16* __restrict__ logdt, const u16* __restrict__ Dskip,
             const u16* __restrict__ wg, const u16* __restrict__ wu, const u16* __restrict__ wd, u16* __restrict__ out, char* __restrict__ ws)
{
    const int tid = threadIdx.x, lane = tid & 63, w = __builtin_amdgcn_readfirstlane(tid >> 6);
    const int b = blockIdx.x, g = b & 7, j = b >> 3;
    const bool mainb = j < 28;
    const int cm = mainb ? g * 28 + j : 0;
    const int P = 4 * (g >> 1) + j / 7, S = 7 * (g & 1) + j % 7;
    const __amdgpu_buffer_rsrc_t rsW = __builtin_amdgcn_make_buffer_rsrc(ws, 0, WS_END, 0x00020000);
    const __amdgpu_buffer_rsrc_t rsX = __builtin_amdgcn_make_buffer_rsrc((void*)x, 0, L_ * D_ * 2, 0x00020000);
    const u32 trq = (lane & 15) >> 2, trp = lane & 3, trh = lane >> 5, trb = (lane >> 4) & 1;
    STAMP_DECL
    STAMP(0);
    {
        u64 pc; asm volatile("s_getpc_b64 %0" : "=s"(pc));
        pc &= ~255ull;
        for (int q = j * 8 + w; q < CODEPF; q += 256) glds16<false>((u32)(q * 1024 + lane * 16), (const void*)pc, 156672);
    }
    const u64 tstart = __builtin_amdgcn_s_memrealtime();
    v4u xr; u64 ep; u32 p_ld2, p_dk2;
    asm volatile("global_load_dwordx4 %0, %1, %2" : "=v"(xr) : "v"((u32)((8 * b + w) * 896 + min(lane, 55) * 16)), "s"(x) : "memory");
    asm volatile("s_load_dwordx2 %0, %3, 0x0\n\ts_load_dword %1, %4, 0x0\n\ts_load_dword %2, %5, 0x0" : "=&s"(ep), "=&s"(p_ld2), "=&s"(p_dk2)
                 : "s"(ws + WS_EP), "s"((const char*)logdt + cm * 4), "s"((const char*)Dskip + cm * 4) : "memory");
    const int ch = w >> 2, dch = 2 * cm + ch;
    if (mainb) {
        const int ai_ = lane >> 3;
        const u16* src = ai_ == 0 ? Are : ai_ == 1 ? Aim : ai_ == 2 ? Bre : ai_ == 3 ? Bim : ai_ == 4 ? Cre : Cim;
        glds16v(src + dch * N_ + (w & 3) * 64 + (lane & 7) * 8, PRM + w * 1024);
        const int ew = w < 6 ? w : w - 6;
        glds16<false>((u32)(ew * 1024 + lane * 16), (const void*)&EO, EOL + ew * 1024);
        const int bb_ = b < 196 ? b : b - 196;
#pragma unroll
        for (int i = 0; i < 2; ++i) {
            const int id = 2 * bb_ + i, Sp = id / 28, s = id % 28;
            const u32 row = (w & 3) * 4 + (lane >> 4), chk = (lane & 15) ^ swz_b(row);
            glds16<false>((u32)((16 * s + row) * (F_ * 2) + Sp * 256 + chk * 16), (w >> 2) ? (const void*)wu : (const void*)wg, RT + i * 8192 + w * 1024);
        }
    } else {
        const int hb = (j - 28) * 8 + g;
#pragma unroll
        for (int k = 0; k < 7; ++k) {
            const int gid = min(hb + 32 * k, 195);
            const int dt_ = gid / 28, fg = gid % 28;
            const u32 row = 8 * w + (lane >> 3), chk = (lane & 7) ^ (((row >> 1) & 1) << 2);
            glds16<false>((u32)((64 * fg + row) * (D_ * 2) + dt_ * 128 + chk * 16), (const void*)wd, k * 8192 + w * 1024);
        }
    }
    asm volatile("s_cmp_ge_i32 %1, 28\n\ts_cbranch_scc1 1f\n\ts_waitcnt vmcnt(4)\n\ts_branch 2f\n1:\n\ts_waitcnt vmcnt(7)\n2:" : "+v"(xr) : "s"(__builtin_amdgcn_readfirstlane(j)) : "memory", "scc");
    asm volatile("s_waitcnt lgkmcnt(0)" : "+s"(ep), "+s"(p_ld2), "+s"(p_dk2) :: "memory");
    ep += 1ull;
    const u32 p_ldt = ch ? p_ld2 >> 16 : p_ld2 & 0xffffu, p_dsk = ch ? p_dk2 >> 16 : p_dk2 & 0xffffu;
    if (lane >= 56) xr = v4u{0, 0, 0, 0};
    const u32 tag = (u32)ep, tagx = (u32)(ep >> 32) << 16;
    if (lane < 56) *lds<v4u>(XT + w * 912 + lane * 16) = xr;
    {
        const float v[8] = {bflo(xr.x), bfhi(xr.x), bflo(xr.y), bfhi(xr.y), bflo(xr.z), bfhi(xr.z), bflo(xr.w), bfhi(xr.w)};
        const float mean = wave_sum(((v[0] + v[1]) + (v[2] + v[3])) + ((v[4] + v[5]) + (v[6] + v[7]))) * (1.f / 448.f);
        float q = 0.f;
#pragma unroll
        for (int e = 0; e < 8; ++e) { const float dl = v[e] - mean; q += dl * dl; }
        q = lane < 56 ? q : 0.f;
        const u32 ln1 = ln_pack(mean, wave_sum(q) * (1.f / 448.f));
        if (lane == 0) __hip_atomic_store((GAS u64*)(ws + WS_SG) + (8 * b + w), ((u64)tag << 32) | ln1, RLX_AGENT);
    }
    STAMP(1);
    const u32 km0 = FM + ch * FM_CH + (w & 3) * 256;
    f2 Ga, Gb, Gc, Gd, KX, KY, G4, G4r, E1, E1r, E256, E256r, E2, E2r, E4, E4r;
#define M1(a, bb) (f2{(a).x, (a).x} * (bb))
#define M2(a, br, t) __builtin_elementwise_fma(f2{(a).y, (a).y}, (br), (t))
#define WROW1(r, v) do { u32 keep_; asm volatile("s_mov_b32 %0, m0\n\ts_mov_b32 m0, %2\n\ts_nop 0\n\tds_write_addtid_b32 %1 offset:%3\n\ts_mov_b32 m0, %0" : "=&s"(keep_) : "v"(v), "s"(km0), "n"((r) * FM_ROW) : "memory"); } while (0)
#define WROW6(r0, v0, r1, v1, r2, v2, r3, v3, r4, v4, r5, v5) do { u32 keep_; asm volatile("s_mov_b32 %0, m0\n\ts_mov_b32 m0, %7\n\ts_nop 0\n\t" \
        "ds_write_addtid_b32 %1 offset:%8\n\tds_write_addtid_b32 %2 offset:%9\n\tds_write_addtid_b32 %3 offset:%10\n\tds_write_addtid_b32 %4 offset:%11\n\tds_write_addtid_b32 %5 offset:%12\n\tds_write_addtid_b32 %6 offset:%13\n\ts_mov_b32 m0, %0" \
        : "=&s"(keep_) : "v"(v0), "v"(v1), "v"(v2), "v"(v3), "v"(v4), "v"(v5), "s"(km0), "n"((r0) * FM_ROW), "n"((r1) * FM_ROW), "n"((r2) * FM_ROW), "n"((r3) * FM_ROW), "n"((r4) * FM_ROW), "n"((r5) * FM_ROW) : "memory"); } while (0)
#define WA(r, X) WROW1(r, pk2((X).x, -(X).y))
#define KSTEP(t) do { SB0(); \
        const f2 ta = M1(Ga, G4), tb = M1(Gb, G4), tc = M1(Gc, G4), td_ = M1(Gd, G4), tx = M1(KX, E1), ty = M1(KY, E256); \
        const u32 c0 = pk2(Ga.x, Ga.y), c1 = pk2(Gb.x, Gb.y), c2 = pk2(Gc.x, Gc.y), c3 = pk2(Gd.x, Gd.y), c4 = pk2(KX.x, -KX.y), c5 = pk2(KY.x, -KY.y); \
        f2 ts0, ts1; \
        if ((t) == 0) ts0 = M1(KY, E2); \
        if ((t) == 1 || (t) == 2) ts0 = M1(KY, E4); \
        SB0(); \
        WROW6(21 + 4 * (t), c0, 22 + 4 * (t), c1, 23 + 4 * (t), c2, 24 + 4 * (t), c3, (t), c4, ((t) == 0 ? 8 : (t) == 1 ? 12 : (t) == 2 ? 14 : 13 + (t)), c5); \
        if ((t) == 0) { f2 Z = M2(KY, E2r, ts0); WA(9, Z); ts1 = M1(Z, E2); Z = M2(Z, E2r, ts1); WA(10, Z); ts1 = M1(Z, E2); Z = M2(Z, E2r, ts1); WA(11, Z); } \
        if ((t) == 1) { const f2 V = M2(KY, E4r, ts0); WA(13, V); } \
        if ((t) == 2) { const f2 V = M2(KY, E4r, ts0); WA(15, V); } \
        if ((t) < 7) { Ga = M2(Ga, G4r, ta); Gb = M2(Gb, G4r, tb); Gc = M2(Gc, G4r, tc); Gd = M2(Gd, G4r, td_); KX = M2(KX, E1r, tx); KY = M2(KY, E256r, ty); } \
    } while (0)
    bar();
    auto publish_xg = [&]() {
        u32 x0[4], x1[4];
#pragma unroll
        for (int k = 0; k < 4; ++k) {
            const int id = min(tid + 512 * k, 1791), c = id >> 2, i2 = (id & 3) * 2;
            x0[k] = *lds<u16>(XT + i2 * 912 + c * 2); x1[k] = *lds<u16>(XT + (i2 + 1) * 912 + c * 2);
        }
        SB0();
#pragma unroll
        for (int k = 0; k < 4; ++k)
            __builtin_amdgcn_raw_buffer_store_b128(v4u{x0[k] | tagx, tag, x1[k] | tagx, tag}, rsW, (tid + 512 * k < 1792) ? (u32)(WS_XG + b * 28672 + (tid + 512 * k) * 16) : 0x7ffffff0u, 0, SC1);
    };
    if (mainb) {
    publish_xg();
    {
        vmw<8>();
        const u32 pa_ = PRM + w * 1024 + lane * 2;
        const u32 p_are = *lds<u16>(pa_), p_aim = *lds<u16>(pa_ + 128), p_bre = *lds<u16>(pa_ + 256), p_bim = *lds<u16>(pa_ + 384), p_cre = *lds<u16>(pa_ + 512), p_cim = *lds<u16>(pa_ + 640);
        const float dt = rbf(expf(bflo(p_ldt)));
        const float zr = dt * bflo(p_are), zi = dt * bflo(p_aim);
        const float ar = zr * 1.4426950408889634f, ai = zi * 0.15915494309189535f;
        const f2 CB = cmul(f2{bflo(p_cre), bflo(p_cim)}, f2{dt * bflo(p_bre), dt * bflo(p_bim)});
        E1 = cexp_rev(ar, ai, 1.f); E256 = cexp_rev(ar, ai, 256.f);
        const f2 G1 = cexp_rev(ar, ai, 8.f);
        E2 = cmul(E1, E1); E4 = cmul(E2, E2);
        const f2 G2 = cmul(G1, G1);
        G4 = cmul(G2, G2);
        E1r = f2{-E1.y, E1.x}; E2r = f2{-E2.y, E2.x}; E4r = f2{-E4.y, E4.x}; E256r = f2{-E256.y, E256.x}; G4r = f2{-G4.y, G4.x};
        Ga = f2{1.f, 0.f}; Gb = G1; Gc = G2; Gd = cmul(G2, G1);
        KX = CB; KY = cmul(CB, E256);
        SB0();
    }
    KSTEP(0); KSTEP(1); KSTEP(2);
    SB0();
    vmw<5>();
    bar();
    if (b < 196) {
#pragma unroll
        for (int i = 0; i < 2; ++i) {
            const int id = 2 * b + i, Sp = id / 28, s = id % 28;
            const u32 base = RT + i * 8192 + trb * 4096 + 8 * (trp & 1);
            const v4u f = tr_frag(base + off_b(8 * trh + trq, 2 * w + (trp >> 1)), base + off_b(8 * trh + 4 + trq, 2 * w + (trp >> 1)));
            __builtin_amdgcn_raw_buffer_store_b128(f, rsW, WS_WG + ((Sp * 8 + w) * 28 + s) * 1024 + lane * 16, 0, SC1);
        }
    }
    fence();
    STAMP(2);
    u32 wr_flag;
    {
        KSTEP(3); KSTEP(4); KSTEP(5); KSTEP(6); KSTEP(7);
        SB0();
#undef KSTEP
#undef WA
#undef WROW1
#undef WROW6
#undef M1
#undef M2
        STAMP(3);
        vmw<0>();
        bar();
        if (tid == 0 && b < 196) __hip_atomic_store((GAS u32*)(ws + WS_WR + b * 64), tag, RLX_AGENT);
        const u32 xa = WS_XG + (32 * w + (lane >> 3)) * 28672 + cm * 128 + (lane & 7) * 16, sa = WS_SG + (32 * w + (lane >> 3)) * 64 + (lane & 3) * 16;
        v4u xg[4], sg[4];
#pragma unroll
        for (int k = 0; k < 4; ++k) { xg[k] = __builtin_amdgcn_raw_buffer_load_b128(rsW, xa + k * 8 * 28672, 0, SC1); sg[k] = __builtin_amdgcn_raw_buffer_load_b128(rsW, sa + k * 512, 0, SC1); }
        fence();
        const int ta_ = (w >> 1) & 1, tb_ = w & 1;
        const u32 fa_ = opaque(FM + ch * FM_CH + (16 * ta_ + (lane & 15)) * FM_ROW + (lane >> 4) * 16), fb_ = opaque(FM + ch * FM_CH + (21 + 16 * tb_ + (lane & 15)) * FM_ROW + (lane >> 4) * 16);
        f32x4 f0 = {0.f, 0.f, 0.f, 0.f}, f1 = {0.f, 0.f, 0.f, 0.f};
        v4u A[16], B[16];
#pragma unroll
        for (int ks = 0; ks < 16; ++ks) { A[ks] = *lds<v4u>(fa_ + ks * 64); B[ks] = *lds<v4u>(fb_ + ks * 64); }
        SB0();
#pragma unroll
        for (int ks = 0; ks < 16; ks += 2) { f0 = mfma16(A[ks], B[ks], f0); f1 = mfma16(A[ks + 1], B[ks + 1], f1); }
        f0 += f1;
#pragma unroll
        for (int r = 0; r < 4; ++r) { const int row = 16 * ta_ + 4 * (lane >> 4) + r; if (row < 24) *lds<float>(FT + ch * FT_CH + (row * 32 + 16 * tb_ + (lane & 15)) * 4) = f0[r]; }
        if ((tid & 255) == 255) *lds<float>(FT + ch * FT_CH + 768 * 4) = 0.f;
        STAMP(4);
        for (u32 spin = 0;; ++spin) {
            u32 bad = 0;
#pragma unroll
            for (int k = 0; k < 4; ++k) bad |= ((sg[k].y ^ tag) | (sg[k].w ^ tag) | (xg[k].y ^ tag)) | ((xg[k].w ^ tag) | (((xg[k].x ^ tagx) | (xg[k].z ^ tagx)) & 0xffff0000u));
            if (__all(bad == 0) || spin > SPIN_CAP) break;
            __builtin_amdgcn_s_sleep(4);
#pragma unroll
            for (int k = 0; k < 4; ++k) { xg[k] = __builtin_amdgcn_raw_buffer_load_b128(rsW, xa + k * 8 * 28672, 0, SC1); sg[k] = __builtin_amdgcn_raw_buffer_load_b128(rsW, sa + k * 512, 0, SC1); }
        }
        STAMP(5);
        wr_flag = __builtin_amdgcn_raw_buffer_load_b32(rsW, WS_WR + (tid < 196 ? tid : 0) * 64, 0, SC1);
        const u32 ua = opaque(UU + ((lane >> 2) & 1) * UU_CH + 512 + (32 * w + (lane >> 3)) * 16 + (lane & 3) * 4), xsa = opaque(XS + ((lane >> 2) & 1) * 4096 + (32 * w + (lane >> 3)) * 16 + (lane & 3) * 4);
#pragma unroll
        for (int k = 0; k < 4; ++k) {
            const float u0 = rbf(bflo(xg[k].x) - bflo(sg[k].x)) * __builtin_amdgcn_rcpf(bfhi(sg[k].x));
            const float u1 = rbf(bflo(xg[k].z) - bflo(sg[k].z)) * __builtin_amdgcn_rcpf(bfhi(sg[k].z));
            *lds<u32>(ua + k * 128) = pk2(u0, u1);
            *lds<u32>(xsa + k * 128) = __builtin_amdgcn_perm(xg[k].z, xg[k].x, 0x05040100u);
        }
    }
    if (tid >= 64 && tid < 128) *lds<v4u>(UU + ((tid >> 5) & 1) * UU_CH + (tid & 31) * 16) = v4u{0, 0, 0, 0};
    const u32 eb_ = opaque(EOL + (tid & 255) * 24);
    const v2u ea = *lds<v2u>(eb_), eb = *lds<v2u>(eb_ + 8), ec = *lds<v2u>(eb_ + 16);
    bar();
    {
        const u32 fb_ = FT + ch * FT_CH;
        const u32 eo[11] = {ea.x & 0xffffu, ea.x >> 16, ea.y & 0xffffu, ea.y >> 16, eb.x & 0xffffu, eb.x >> 16, eb.y & 0xffffu, eb.y >> 16, ec.x & 0xffffu, ec.x >> 16, ec.y & 0xffffu};
        float v[11];
#pragma unroll
        for (int e = 0; e < 11; ++e) v[e] = *lds<float>(fb_ + eo[e]);
        SB0();
        u32 p[10];
#pragma unroll
        for (int e = 0; e < 10; ++e) p[e] = pk2(v[e], v[e + 1]);
        const u32 ka_ = opaque(KC + ch * KC_CH + (tid & 255) * 16);
#pragma unroll
        for (int s = 0; s < 4; ++s) *lds<v4u>(ka_ + s * KC_COPY) = v4u{p[s], p[s + 2], p[s + 4], p[s + 6]};
    }
    if (tid < 16) *lds<v4u>(KC + (tid >> 3) * KC_CH + ((tid >> 1) & 3) * KC_COPY + 4096 + (tid & 1) * 16) = v4u{0, 0, 0, 0};
    if (!__all(wr_flag == tag)) {
        for (u32 spin = 0; spin < SPIN_CAP; ++spin) {
            __builtin_amdgcn_s_sleep(4);
            if (__all(__builtin_amdgcn_raw_buffer_load_b32(rsW, WS_WR + (tid < 196 ? tid : 0) * 64, 0, SC1) == tag)) break;
        }
    }
    bar();
    STAMP(6);
    CSTAMP(26);
    v4u Wf[28];
    const u32 l16 = lane * 16;
    const int wf_off = WS_WG + (S * 8 + w) * 28 * 1024;
    const int rq = w & 3;
    const u32 cb = lane & 15, cq = lane >> 4;
    const float dsv = bflo(p_dsk);
    const u32 f2_off = WS_F2 + g * 16384 + (tid < 224 ? tid : 0) * 64;
    const u32 bt_src = (8 * w + ((lane >> 2) & 7)) * 4096 + 128 * P + (4 * (lane >> 5) + ((lane & 3) ^ ((2 * w + ((lane >> 4) & 1)) & 3))) * 16;
    auto bt_piece = [&](int part, int k) { glds16<true>(bt_src + part * 2048 + k * 64 * 4096, ws + WS_HT, (part ? BTB : BTA) + (8 * k + w) * 1024); };
    {
        const u32 cs = (3u - cb) & 3u;
        const u32 kbase = opaque(KC + ch * KC_CH + cs * KC_COPY + 2 * (2047 - 16 * (cq >> 1) - cb + 8 * (cq & 1) - cs) - 32 * 4 * rq - 32 * 114);
        const u32 kbase8 = opaque(kbase + 8);
        const u32 ubase = opaque(UU + ch * UU_CH + 512 + 32 * (cb - (cq >> 1)) + 16 * (cq & 1) - 32 * 4 * rq - 32 * 2);
        auto ldA = [&](int p, int rr) { const v2u lo = *lds<volatile v2u>(kbase + 32 * (114 - (16 * p + 2 * rr))), hi = *lds<volatile v2u>(kbase8 + 32 * (114 - (16 * p + 2 * rr))); return v4u{lo.x, lo.y, hi.x, hi.y}; };
        auto ldB = [&](int k, int rr) { return *lds<v4u>(ubase + 32 * (16 * k - 2 * rr + 2)); };
        const u32 cxw = opaque(CX + (4 * ch * 4 + rq) * 1024 + lane * 16), cxr = opaque(CX + w * 4096 + lane * 16);
        const u32 fu_ = opaque(UU + ch * UU_CH + 512 + (256 * rq + 16 * cb + 4 * cq) * 2), fx_ = opaque(XS + ch * 4096 + (256 * rq + 16 * cb + 4 * cq) * 2);
        const u32 ht_ = WS_HT + dch * 4096 + (256 * rq + 16 * cb + 4 * cq) * 2;
        auto finish = [&](f32x4 (&acc)[4], int tbase) {
#pragma unroll
            for (int i = 0; i < 4; ++i) *lds<f32x4>(cxw + i * 4096) = acc[i];
            const v2u uu = *lds<v2u>(fu_ + tbase * 512), xx = *lds<v2u>(fx_ + tbase * 512);
            bar();
            const f32x4 p0 = *lds<f32x4>(cxr), p1 = *lds<f32x4>(cxr + 1024), p2 = *lds<f32x4>(cxr + 2048), p3 = *lds<f32x4>(cxr + 3072);
            const float uv[4] = {bflo(uu.x), bfhi(uu.x), bflo(uu.y), bfhi(uu.y)}, xv[4] = {bflo(xx.x), bfhi(xx.x), bflo(xx.y), bfhi(xx.y)};
            float sk[4];
#pragma unroll
            for (int r = 0; r < 4; ++r) sk[r] = rbf(uv[r] * dsv);
            SB0();
            const f32x4 y = (p0 + p1) + (p2 + p3);
            float h[4];
#pragma unroll
            for (int r = 0; r < 4; ++r) h[r] = xv[r] + rbf(rbf(y[r]) + sk[r]);
            __builtin_amdgcn_raw_buffer_store_b64(v2u{pk2(h[0], h[1]), pk2(h[2], h[3])}, rsW, ht_ + tbase * 512, 0, SC1);
        };
        f32x4 acc[4];
        v4u Af[8], Bf[8], An[8], Bn[8];
#pragma unroll
        for (int i = 0; i < 4; ++i) acc[i] = f32x4{0.f, 0.f, 0.f, 0.f};
#pragma unroll
        for (int p = 0; p < 4; ++p) { Af[p] = ldA(p, 0); Bf[p] = ldB(p, 0); An[p] = ldA(p, 1); Bn[p] = ldB(p, 1); }
        SB0();
#pragma unroll
        for (int i = 0; i < 2; ++i) Wf[i] = __builtin_amdgcn_raw_buffer_load_b128(rsW, l16 + (i) * 1024 % 4096, wf_off + (i) * 1024 / 4096 * 4096, WAUX);
#pragma unroll
        for (int p = 0; p < 4; ++p)
#pragma unroll
            for (int i = p; i < 4; ++i) acc[i] = mfma16(Af[p], Bf[i - p], acc[i]);
#pragma unroll
        for (int p = 0; p < 4; ++p)
#pragma unroll
            for (int i = p; i < 4; ++i) acc[i] = mfma16(An[p], Bn[i - p], acc[i]);
#pragma unroll
        for (int p = 4; p < 8; ++p) { Af[p] = ldA(p, 0); Bf[p] = ldB(p, 0); }
        SB0();
        CSTAMP(16);
        finish(acc, 0);
        CSTAMP(17);
#pragma unroll
        for (int i = 0; i < 4; ++i) acc[i] = f32x4{0.f, 0.f, 0.f, 0.f};
        SB0();
#pragma unroll
        for (int p = 4; p < 8; ++p) { An[p] = ldA(p, 1); Bn[p] = ldB(p, 1); }
#pragma unroll
        for (int i = 2; i < 8; ++i) Wf[i] = __builtin_amdgcn_raw_buffer_load_b128(rsW, l16 + (i) * 1024 % 4096, wf_off + (i) * 1024 / 4096 * 4096, WAUX);
#pragma unroll
        for (int p = 0; p < 8; ++p)
#pragma unroll
            for (int i = 0; i < 4; ++i) if (p <= 4 + i) acc[i] = mfma16(Af[p], Bf[4 + i - p], acc[i]);
#pragma unroll
        for (int i = 0; i < 6; ++i) { SGB(M_MFMA, 2); SGB(M_DSR, 2); SGB(M_MFMA, 2); SGB(M_VMEM, 1); }
        SB0();
        CSTAMP(18);
        vmw<6>();
        CSTAMP(19);
        bar();
        CSTAMP(20);
        if (tid < 8) __hip_atomic_store((GAS u32*)(ws + WS_F2 + tid * 16384 + cm * 64), tag, RLX_AGENT);
        SB0();
#pragma unroll
        for (int i = 8; i < 14; ++i) Wf[i] = __builtin_amdgcn_raw_buffer_load_b128(rsW, l16 + (i) * 1024 % 4096, wf_off + (i) * 1024 / 4096 * 4096, WAUX);
#pragma unroll
        for (int p = 0; p < 8; ++p)
#pragma unroll
            for (int i = 0; i < 4; ++i) if (p <= 4 + i) acc[i] = mfma16(An[p], Bn[4 + i - p], acc[i]);
#pragma unroll
        for (int i = 0; i < 6; ++i) { SGB(M_MFMA, 4); SGB(M_VMEM, 1); }
        SB0();
        CSTAMP(21);
        finish(acc, 4);
    }
    STAMP(7);
    CSTAMP(27);
    auto gate_spin = [&](u32 off, bool active) {
        for (u32 spin = 0;; ++spin) {
            const u32 s0 = __builtin_amdgcn_raw_buffer_load_b32(rsW, off, 0, SC1);
            if (__all(s0 == tag || !active) || spin > SPIN_CAP) break;
            __builtin_amdgcn_s_sleep(8);
        }
    };
    if (w < 4) gate_spin(tid < 224 ? f2_off : (u32)(WS_WF + (tid - 224) * 64), true);
    bar();
    STAMP(8);
    if (b == 0 && tid == 0) __hip_atomic_store((GAS u64*)(ws + WS_EP), ep, RLX_AGENT);
    const float csum = __uint_as_float(__builtin_amdgcn_raw_buffer_load_b32(rsW, WS_CS + (((lane >> 4) & 1) * 1792 + 128 * S + 16 * w + (lane & 15)) * 4, 0, SC1));
    fence();
#pragma unroll
    for (int k = 0; k < 7; ++k) bt_piece(0, k);
    vmw<6>();
    bar();
    STAMP(9);
    CSTAMP(24);
    v4u Wd[28];
    const int td = w & 1, kq = w >> 1, hp = S / 7, dtile = S % 7;
    const int wd_off = WS_WD + ((dtile * 2 + td) * 112 + 28 * kq) * 1024;
    const u32 trb0 = 1024 * trh + 64 * trq + 16 * ((2 * trb + (trp >> 1)) ^ ((2 * trh) & 3)) + 8 * (trp & 1), trb1 = 1024 * trh + 64 * (4 + trq) + 16 * ((2 * trb + (trp >> 1)) ^ ((2 * trh + 1) & 3)) + 8 * (trp & 1);
    const int hh_out = WS_HH + (P * 112 + 8 * S + w) * 4096;
    u32 hres[4] = {0, 0, 0, 0}; float rs = 0.f;
    const u32 f3_off = WS_F3 + (P * 16 + (lane < 14 ? lane : 0)) * 64;
    const char* hsrc = ws + WS_HH + (P * 112 + 28 * kq + td) * 4096 + hp * 1024; const u32 hdst = kq * 1024;
    auto hh_dma = [&](int u) { glds16<true>(l16, hsrc + (u % 28) * 4096 + (u >= 28 ? 2048 : 0), (td ? hh_slot(u + 1) : hh_slot(u)) + hdst); };
#define GEMM1_PASS(PART) do { \
        const u32 b0 = opaque(((PART) ? BTB : BTA) + trb0), b1 = opaque(((PART) ? BTB : BTA) + trb1); \
        const u32 s0_ = opaque(b0 + (w >> 1) * 2048 + (w & 1) * 512), s1_ = opaque(b1 + (w >> 1) * 2048 + (w & 1) * 512); \
        f32x16 acc[2]; \
        _Pragma("unroll") for (int i = 0; i < 16; ++i) { acc[0][i] = 0.f; acc[1][i] = 0.f; } \
        v4u Bq[3][2], Of; \
        float sa0 = 0.f, sa1 = 0.f, sq0 = 0.f, sq1 = 0.f; \
        u32 gatev = 0; \
        _Pragma("unroll") for (int t = 0; t < 2; ++t) { Bq[0][t] = tr_frag(b0 + 512 * t, b1 + 512 * t); Bq[1][t] = tr_frag(b0 + 2048 + 512 * t, b1 + 2048 + 512 * t); } \
        _Pragma("unroll") for (int s = 0; s < 28; ++s) { \
            SB0(); \
            if (!(PART)) { \
                if (s == 2) { vmw<4 + 2>(); bar(); if (tid < 8) __hip_atomic_store((GAS u32*)(ws + WS_F2 + (8 + tid) * 16384 + cm * 64), tag, RLX_AGENT); }     \
                if (s == 10) { vmw<10>(); bar(); } \
                if (s == 19) {     \
                    if (w < 4) { if (!__all(gatev == tag || tid >= 224)) gate_spin(f2_off + 8 * 16384, tid < 224); } \
                    bar(); \
                } \
            } else { \
                if (s == 3) { vmw<2>(); bar(); if (tid == 0) __hip_atomic_store((GAS u32*)(ws + WS_F3 + (P * 16 + S) * 64), tag, RLX_AGENT); }     \
                if (s == 20) {     \
                    if (w == 0) { if (!__all(gatev == tag)) gate_spin(f3_off, true); } \
                    bar(); \
                } \
            } \
            SB0(); \
            if (s + 2 < 28) { _Pragma("unroll") for (int t = 0; t < 2; ++t) Bq[(s + 2) % 3][t] = tr_frag(b0 + (s + 2) * 2048 + 512 * t, b1 + (s + 2) * 2048 + 512 * t); } \
            if ((s & 3) == 0 && !EXPS) Of = tr_frag(s0_ + s * 2048, s1_ + s * 2048);        \
            if (!(PART)) { \
                if (s < 14) { if (!EXPA) Wf[14 + s] = __builtin_amdgcn_raw_buffer_load_b128(rsW, l16 + ((14 + s)) * 1024 % 4096, wf_off + ((14 + s)) * 1024 / 4096 * 4096, WAUX); } \
                else if (s == 14) { if (w < 4) gatev = __builtin_amdgcn_raw_buffer_load_b32(rsW, f2_off + 8 * 16384, 0, SC1); } \
                else if (s < 19) Wd[s - 15] = __builtin_amdgcn_raw_buffer_load_b128(rsW, l16 + ((s - 15)) * 1024 % 4096, wd_off + ((s - 15)) * 1024 / 4096 * 4096, WAUX); \
                else if (s < 26) bt_piece(1, s - 19); \
            } else { \
                if (s >= 1 && s < 25) Wd[3 + s] = __builtin_amdgcn_raw_buffer_load_b128(rsW, l16 + (3 + s) * 1024 % 4096, wd_off + (3 + s) * 1024 / 4096 * 4096, WAUX); \
                if (s == 15) { if (w == 0) gatev = __builtin_amdgcn_raw_buffer_load_b32(rsW, f3_off, 0, SC1); } \
                if (s >= 20 && s < 27) { fence(); hh_dma(2 * (s - 20)); } \
            } \
            _Pragma("unroll") for (int t = 0; t < 2; ++t) acc[t] = mfma32(Wf[s], Bq[s % 3][t], acc[t]); \
            if ((s & 3) == 2 && !EXPS) { \
                _Pragma("unroll") for (int e = 0; e < 4; e += 2) { \
                    const float l0 = bflo(Of[e]), h0 = bfhi(Of[e]), l1 = bflo(Of[e + 1]), h1 = bfhi(Of[e + 1]); \
                    sa0 += l0; sq0 = __builtin_fmaf(l0, l0, sq0); sa1 += l1; sq1 = __builtin_fmaf(l1, l1, sq1); \
                    sa0 += h0; sq0 = __builtin_fmaf(h0, h0, sq0); sa1 += h1; sq1 = __builtin_fmaf(h1, h1, sq1); \
                } \
                asm volatile("" : "+v"(sa0), "+v"(sa1), "+v"(sq0), "+v"(sq1));        \
                SGB(M_MFMA, 1); SGB(M_DSR, 2); SGB(M_VALU, 12); SGB(M_VMEM, 1); SGB(M_MFMA, 1); SGB(M_DSR, 2); SGB(M_VALU, 12); \
            } else { SGB(M_MFMA, 1); SGB(M_DSR, 3); SGB(M_VMEM, 1); SGB(M_MFMA, 1); SGB(M_DSR, 3); } \
        } \
        SB0(); \
        { \
            const float sh = half_sum(sa0 + sa1), sq = half_sum(sq0 + sq1); \
            const u32 wa_ = opaque(SPQ + (PART) * 2048 + (w & 1) * 1024 + (w >> 1) * 4 + lane * 32); \
            if (lane < 32) { *lds<float>(wa_) = sh; *lds<float>(wa_ + 16) = sq; if (!(PART)) *lds<float>(CSL + w * 128 + lane * 4) = csum; } \
        } \
        bar(); \
        {     \
            const u32 ca_ = opaque(CSL + w * 128 + trh * 16), sa_ = opaque(SPQ + (PART) * 2048 + (lane & 31) * 32); \
            const f32x4 csg[2] = {*lds<f32x4>(ca_), *lds<f32x4>(ca_ + 32)}, csu[2] = {*lds<f32x4>(ca_ + 64), *lds<f32x4>(ca_ + 96)}; \
            const f32x4 sm[2] = {*lds<f32x4>(sa_), *lds<f32x4>(sa_ + 1024)}; \
            const bool mine_ = (kq >> 1) == (PART);        \
            f32x4 sq4 = {0.f, 0.f, 0.f, 0.f}; u32 hb_[8]; \
            if (mine_) { \
                sq4 = *lds<f32x4>(sa_ + hp * 1024 + 16); \
                const u32 lc_ = 4 * hp + ((lane & 31) >> 3), r8_ = 8 * dtile + 4 * td + 2 * (kq & 1);        \
                const u32 hb0_ = ((PART) ? BTB : BTA) + r8_ * 1024 + (lc_ >> 2) * 512 + trh * 256 + (lane & 7) * 2; \
                const u32 h0_ = opaque(hb0_ + 16 * ((lc_ & 3) ^ trh)), h1_ = opaque(hb0_ + 1024 + 16 * ((lc_ & 3) ^ (2 + trh))); \
                _Pragma("unroll") for (int e = 0; e < 4; ++e) { hb_[e] = *lds<u16>(h0_ + 64 * e); hb_[4 + e] = *lds<u16>(h1_ + 64 * e); } \
            } \
            SB0(); \
            _Pragma("unroll") for (int t = 0; t < 2; ++t) { \
                const float m = rbf(((sm[t][0] + sm[t][1]) + (sm[t][2] + sm[t][3])) * (1.f / 448.f)); \
                float hv[8]; \
                _Pragma("unroll") for (int e = 0; e < 8; ++e) hv[e] = (acc[t][e] - m * csg[e >> 2][e & 3]) * (acc[t][e + 8] - m * csu[e >> 2][e & 3]); \
                __builtin_amdgcn_raw_buffer_store_b128(v4u{pk2(hv[0], hv[1]), pk2(hv[2], hv[3]), pk2(hv[4], hv[5]), pk2(hv[6], hv[7])}, rsW, l16 + hh_out + (2 * (PART) + t) * 1024, 0, SC1); \
            } \
            if (mine_) { \
                const f32x4 s4 = hp ? sm[1] : sm[0]; \
                const float mean = ((s4[0] + s4[1]) + (s4[2] + s4[3])) * (1.f / 448.f), var = fmaxf(((sq4[0] + sq4[1]) + (sq4[2] + sq4[3])) * (1.f / 448.f) - mean * mean, 0.f); \
                const float sd = bfhi(ln_pack(mean, var)); \
                rs = __builtin_amdgcn_rcpf(sd * sd); \
                _Pragma("unroll") for (int e = 0; e < 4; ++e) hres[e] = hb_[2 * e] | (hb_[2 * e + 1] << 16); \
            } \
        } \
    } while (0)
    if (EXPA) {
#pragma unroll
        for (int s = 14; s < 28; ++s) Wf[s] = __builtin_amdgcn_raw_buffer_load_b128(rsW, l16 + s * 1024 % 4096, wf_off + s * 1024 / 4096 * 4096, WAUX);
        vmw<0>(); bar(); CSTAMP(30);
    }
    GEMM1_PASS(0);
    CSTAMP(31);
    vmw<2>();
    bar();
    STAMP(10);
    GEMM1_PASS(1);
    STAMP(11);
    CSTAMP(25);
#undef GEMM1_PASS
    vmw<2>();
    bar();
#pragma unroll
    for (int u = 14; u < 28; u += 2) hh_dma(u);
    STAMP(12);
    CSTAMP(28);
    f32x16 o2[2];
#pragma unroll
    for (int i = 0; i < 16; ++i) { o2[0][i] = 0.f; o2[1][i] = 0.f; }
    const u32 hrdA = opaque(BTA + kq * 1024 + lane * 16), hrdS = opaque(RSP + kq * 1024 + lane * 16), hrdB = opaque(BTB + kq * 1024 + lane * 16);
    auto hh_rd = [&](int u) {
        const u32 sl = hh_slot(u);
        return *lds<v4u>(sl >= RSP ? hrdS + (sl - RSP) : sl >= BTA ? hrdA + (sl - BTA) : hrdB + (sl - BTB));
    };
    {
        v4u hf[4];
        u32 gatev = 0;
#pragma unroll
        for (int u = 0; u < 3; ++u) hf[u] = hh_rd(u);
#pragma unroll
        for (int u = 0; u < 28; ++u) {
            SB0();
            if (u == 5) { vmw<7>(); bar(); if (tid == 0) __hip_atomic_store((GAS u32*)(ws + WS_F3 + 16384 + (P * 16 + S) * 64), tag, RLX_AGENT); }
            if (u == 11) { vmw<0>(); bar(); }
            SB0();
            if (u + 3 < 28) hf[(u + 3) & 3] = hh_rd(u + 3);
            if (u == 22 && w == 0) gatev = __builtin_amdgcn_raw_buffer_load_b32(rsW, f3_off + 16384, 0, SC1);
            o2[0] = mfma32(Wd[u], hf[u & 3], o2[0]);
        }
        SB0();
        if (w == 0) { if (!__all(gatev == tag)) gate_spin(f3_off + 16384, true); }
        bar();
        STAMP(13);
#pragma unroll
        for (int u = 28; u < 56; u += 2) hh_dma(u);
        vmw<9>(); bar();
#pragma unroll
        for (int u = 0; u < 3; ++u) hf[u] = hh_rd(28 + u);
#pragma unroll
        for (int u = 0; u < 28; ++u) {
            SB0();
            if (u == 7) { vmw<0>(); bar(); }
            SB0();
            if (u + 3 < 28) hf[(u + 3) & 3] = hh_rd(28 + u + 3);
            o2[1] = mfma32(Wd[u], hf[u & 3], o2[1]);
        }
        SB0();
        bar();
    }
    STAMP(14);
    CSTAMP(29);
    {
        const u32 red = opaque(td * 24576 + lane * 16);
        f32x4 v[2];
        auto give = [&](auto KQ) {
            constexpr int kk = KQ.value;
#pragma unroll
            for (int k2 = 0; k2 < 4; ++k2) {
                const f32x16& a = o2[k2 >> 1];
#pragma unroll
                for (int q = 0; q < 2; ++q) {
                    const int r0 = 8 * (k2 & 1) + 4 * q;
                    const f32x4 t = {a[r0], a[r0 + 1], a[r0 + 2], a[r0 + 3]};
                    if (k2 != kk) *lds<f32x4>(red + (k2 * 3 + (kk > k2 ? kk - 1 : kk)) * 2048 + q * 1024) = t; else v[q] = t;
                }
            }
        };
        switch (kq) {
            case 0: give(std::integral_constant<int, 0>{}); break;
            case 1: give(std::integral_constant<int, 1>{}); break;
            case 2: give(std::integral_constant<int, 2>{}); break;
            default: give(std::integral_constant<int, 3>{}); break;
        }
        bar();
        const u32 rd = opaque(red + kq * 6144);
        f32x4 pr[2][3];
#pragma unroll
        for (int q = 0; q < 2; ++q)
#pragma unroll
            for (int si = 0; si < 3; ++si) pr[q][si] = *lds<f32x4>(rd + si * 2048 + q * 1024);
        GAS char* op = (GAS char*)out + (size_t)(1024 * (kq >> 1) + 64 * P + 32 * hp + (lane & 31)) * (D_ * 2) + (64 * dtile + 32 * td + 16 * (kq & 1) + 4 * trh) * 2;
        SB0();
#pragma unroll
        for (int q = 0; q < 2; ++q) {
            v[q] = (v[q] + pr[q][0]) + (pr[q][1] + pr[q][2]);
            float r[4];
#pragma unroll
            for (int e = 0; e < 4; ++e) r[e] = ((e & 1) ? bfhi(hres[2 * q + (e >> 1)]) : bflo(hres[2 * q + (e >> 1)])) + rbf(v[q][e] * rs);
            *(GAS v2u*)(op + q * 16) = v2u{pk2(r[0], r[1]), pk2(r[2], r[3])};
        }
    }
    STAMP(15);
    STAMP_FLUSH;
    } else {
        publish_xg();
        const int hb = (j - 28) * 8 + g;
        float cs[8];
        {
            const u16* wsrc = hb < 16 ? wg : wu;
            const int c0 = 112 * (hb & 15) + 8 * (tid % 14), r0 = tid / 14;
#pragma unroll
            for (int e = 0; e < 8; ++e) cs[e] = 0.f;
            v4u t[13];
#pragma unroll
            for (int i = 0; i < 13; ++i) { const int r = r0 + 36 * i; t[i] = (tid < 504 && r < 448) ? *(const v4u*)(wsrc + (size_t)r * F_ + c0) : v4u{0, 0, 0, 0}; }
#pragma unroll
            for (int i = 0; i < 13; ++i) { cs[0] += bflo(t[i].x); cs[1] += bfhi(t[i].x); cs[2] += bflo(t[i].y); cs[3] += bfhi(t[i].y); cs[4] += bflo(t[i].z); cs[5] += bfhi(t[i].z); cs[6] += bflo(t[i].w); cs[7] += bfhi(t[i].w); }
        }
        vmw<0>(); bar();
#pragma unroll
        for (int k = 0; k < 7; ++k) {
            const int gid = hb + 32 * k;
            if (gid < 196) {
                const int dt_ = gid / 28, fg = gid % 28, fsl = w >> 1, td = w & 1;
                u32 ad[2];
#pragma unroll
                for (int t = 0; t < 2; ++t) { const u32 row = 16 * fsl + 8 * t + 4 * trh + trq, chk = 4 * td + 2 * trb + (trp >> 1); ad[t] = k * 8192 + row * 128 + 16 * (chk ^ (((row >> 1) & 1) << 2)) + 8 * (trp & 1); }
                const v4u f = tr_frag(ad[0], ad[1]);
                __builtin_amdgcn_raw_buffer_store_b128(f, rsW, WS_WD + ((dt_ * 2 + td) * 112 + 4 * fg + fsl) * 1024 + lane * 16, 0, SC1);
            }
        }
        if (tid < 504) {
            *lds<f32x4>(65536 + tid * 32) = f32x4{cs[0], cs[1], cs[2], cs[3]};
            *lds<f32x4>(65536 + tid * 32 + 16) = f32x4{cs[4], cs[5], cs[6], cs[7]};
        }
        bar();
        if (tid < 112) {
            float s = 0.f;
            for (int r = 0; r < 36; ++r) s += *lds<float>(65536 + (r * 14 + (tid >> 3)) * 32 + (tid & 7) * 4);
            __hip_atomic_store((GAS u32*)(ws + WS_CS) + ((hb < 16 ? 0 : 1792) + 112 * (hb & 15) + tid), __float_as_uint(s), RLX_AGENT);
        }
        vmw<0>(); bar();
        if (tid == 0) __hip_atomic_store((GAS u32*)(ws + WS_WF + hb * 64), tag, RLX_AGENT);

    }
}

extern "C" void kernel_launch(void* const* d_in, const int* in_sizes, int n_in, void* d_out, int out_size, void* d_ws, size_t ws_size, hipStream_t stream) {
    (void)in_sizes; (void)n_in; (void)out_size; (void)ws_size;
    (void)hipFuncSetAttribute((const void*)fused_kernel, hipFuncAttributeMaxDynamicSharedMemorySize, LDS_BYTES);
    fused_kernel<<<256, 512, LDS_BYTES, stream>>>((const u16*)d_in[0], (const u16*)d_in[1], (const u16*)d_in[2], (const u16*)d_in[3], (const u16*)d_in[4],
        (const u16*)d_in[5], (const u16*)d_in[6], (const u16*)d_in[7], (const u16*)d_in[8], (const u16*)d_in[10], (const u16*)d_in[11], (const u16*)d_in[12],
        (u16*)d_out, (char*)d_ws);
}
```

```cpp
#include <hip/hip_runtime.h>
#include <stdint.h>
#include <type_traits>

typedef unsigned short u16;
typedef unsigned u32;
typedef unsigned long long u64;
typedef u32 v4u __attribute__((ext_vector_type(4)));
typedef u32 v2u __attribute__((ext_vector_type(2)));
typedef float f32x4 __attribute__((ext_vector_type(4)));
typedef float f32x16 __attribute__((ext_vector_type(16)));
typedef float f2 __attribute__((ext_vector_type(2)));
typedef __bf16 bf16x8 __attribute__((ext_vector_type(8)));
typedef __bf16 bf16x2 __attribute__((ext_vector_type(2)));
typedef short s16x4 __attribute__((ext_vector_type(4)));
#define LAS __attribute__((address_space(3)))
#define GAS __attribute__((address_space(1)))
#define DI __device__ __forceinline__
#define RLX_AGENT __ATOMIC_RELAXED, __HIP_MEMORY_SCOPE_AGENT
#define CODEPF 26
#define SC1 16
#define EXPP 0
#define EXPS 0
#define EXPA 0
#define BTSC1 true
#define DLY1 0
#define DLY2 35
#define DLY3 35
#define WAUX 16

constexpr int L_ = 2048, D_ = 448, N_ = 256, F_ = 1792;
constexpr u32 WS_EP = 0;
constexpr u32 WS_SG = 4096;
constexpr u32 WS_WF = 32768;
constexpr u32 WS_CS = 40960;
constexpr u32 WS_HT = 65536;
constexpr u32 WS_WR = 1966080;
constexpr u32 WS_HH = 2u << 20;
constexpr u32 WS_WG = 10u << 20;
constexpr u32 WS_WD = 14u << 20;
constexpr u32 WS_XG = 16u << 20;
constexpr u32 WS_F2 = (23u << 20) + (512u << 10);
constexpr u32 WS_F3 = WS_F2 + 2 * 8 * 16384;
constexpr u32 WS_END = 24u << 20;
constexpr u32 WS_DBG = 24u << 20;
constexpr u32 SPIN_CAP = 1u << 16;

constexpr u32 LDS_BYTES = 163840;
constexpr u32 FM = 0, FM_ROW = 1056, FM_CH = 53 * FM_ROW;
constexpr u32 FT = 2 * FM_CH, FT_CH = 772 * 4;
constexpr u32 EOL = FT + 2 * FT_CH;
constexpr u32 RT = 124288;
constexpr u32 PRM = RT + 16384;
constexpr u32 XT = PRM + 8192;
constexpr u32 KC = 0, KC_COPY = 4160, KC_CH = 4 * KC_COPY;
constexpr u32 CX = 2 * KC_CH;
constexpr u32 UU = RT, UU_CH = (256 + 2048) * 2;
constexpr u32 XS = UU + 2 * UU_CH;
static_assert(EOL % 16 == 0 && EOL + 6144 <= RT && XS + 8192 <= XT && XT + 8 * 912 <= 156672, "LDS map");
constexpr u32 BT_PART = 448 * 128;
constexpr u32 BTA = CX + 32768, BTB = 0;
constexpr u32 RSP = BTA + BT_PART;
constexpr u32 SM = 155648;
constexpr u32 SPQ = SM;
constexpr u32 CSL = SPQ + 4096;
constexpr u32 GW = CSL + 1024;
static_assert(BTA + BT_PART <= RT && RSP + 7 * 4096 <= SM && GW + 64 <= 161280, "LDS map");
constexpr u32 hh_slot(int u) {
    return u < 14 ? BTA + u * 4096 : u < 21 ? RSP + (u - 14) * 4096 : u < 35 ? BTB + (u - 21) * 4096 : u < 49 ? BTA + (u - 35) * 4096 : RSP + (u - 49) * 4096;
}

struct EoTab { u16 v[256 * 12]; };
constexpr u32 foff_c(int j) {
    if (j < 0) return 768u;
    u32 t = (u32)j;
    int s = t >= 1024 ? 3 : t >= 512 ? 2 : t >= 256 ? 1 : 0;
    if (s > 0) t = ((t + (1u << (s - 1)) - 1u + ((t >> s) & 1u)) >> s) << s;
    u32 col = (t >> 3) & 31u, row = 0;
    if (t < 256u) row = t & 7u;
    else if (t < 512u) row = 8u + ((t >> 1) & 3u);
    else if (t < 1024u) row = 12u + 2u * ((t >> 8) & 1u) + ((t >> 2) & 1u);
    else if (t < 2048u) row = 16u + ((t >> 8) & 3u);
    else row = 20u;
    return row * 32u + col;
}
constexpr EoTab make_eo() { EoTab e{}; for (int t = 0; t < 256; ++t) for (int k = 0; k < 12; ++k) e.v[t * 12 + k] = (u16)(k < 11 ? foff_c(2047 - 8 * t - k) * 4u : 0u); return e; }
__device__ const __attribute__((aligned(16))) EoTab EO = make_eo();

DI float bflo(u32 d) { return __uint_as_float(d << 16); }
DI float bfhi(u32 d) { return __uint_as_float(d & 0xffff0000u); }
DI u32 pk2(float lo, float hi) { bf16x2 v = {(__bf16)lo, (__bf16)hi}; return __builtin_bit_cast(u32, v); }
DI float rbf(float f) { return bflo(pk2(f, f)); }
DI bf16x8 as_bf(v4u v) { return __builtin_bit_cast(bf16x8, v); }
DI f32x4 mfma16(v4u a, v4u b, f32x4 c) { return __builtin_amdgcn_mfma_f32_16x16x32_bf16(as_bf(a), as_bf(b), c, 0, 0, 0); }
DI f32x16 mfma32(v4u a, v4u b, f32x16 c) { return __builtin_amdgcn_mfma_f32_32x32x16_bf16(as_bf(a), as_bf(b), c, 0, 0, 0); }
template <class T> DI LAS T* lds(u32 off) { return (LAS T*)(uintptr_t)off; }
DI v2u tr_read(u32 off) { s16x4 r = __builtin_amdgcn_ds_read_tr16_b64_v4i16(lds<s16x4>(off)); return __builtin_bit_cast(v2u, r); }
DI v4u tr_frag(u32 off0, u32 off1) { v2u a = tr_read(off0), b = tr_read(off1); return v4u{a.x, a.y, b.x, b.y}; }
template <int CTRL, int RM> DI float dpp_add(float v) { int t = __builtin_amdgcn_update_dpp(0, __float_as_int(v), CTRL, RM, 0xf, true); return v + __int_as_float(t); }
DI float wave_sum(float v) {
    v = dpp_add<0x111, 0xf>(v); v = dpp_add<0x112, 0xf>(v); v = dpp_add<0x114, 0xf>(v); v = dpp_add<0x118, 0xf>(v);
    v = dpp_add<0x142, 0xa>(v); v = dpp_add<0x143, 0xc>(v);
    return __int_as_float(__builtin_amdgcn_readlane(__float_as_int(v), 63));
}
DI float half_sum(float v) { const u32 u = __float_as_uint(v); const auto r = __builtin_amdgcn_permlane32_swap(u, u, false, false); return __uint_as_float(r[0]) + __uint_as_float(r[1]); }
DI void fence() { asm volatile("" ::: "memory"); }
DI void bar() { asm volatile("s_waitcnt lgkmcnt(0)" ::: "memory"); __builtin_amdgcn_s_barrier(); asm volatile("" ::: "memory"); }
template <int N> DI void vmw() { asm volatile("s_waitcnt vmcnt(%0)" :: "n"(N) : "memory"); }
template <bool S1> DI void glds16(u32 voff, const void* sbase, u32 ldst) {
    u32 keep;
    if (S1) asm volatile("s_mov_b32 %0, m0\n\ts_mov_b32 m0, %3\n\ts_nop 0\n\tglobal_load_lds_dwordx4 %1, %2 sc1\n\ts_mov_b32 m0, %0" : "=&s"(keep) : "v"(voff), "s"(sbase), "s"(ldst) : "memory");
    else    asm volatile("s_mov_b32 %0, m0\n\ts_mov_b32 m0, %3\n\ts_nop 0\n\tglobal_load_lds_dwordx4 %1, %2\n\ts_mov_b32 m0, %0" : "=&s"(keep) : "v"(voff), "s"(sbase), "s"(ldst) : "memory");
}
DI void glds16v(const void* gaddr, u32 ldst) {
    u32 keep;
    asm volatile("s_mov_b32 %0, m0\n\ts_mov_b32 m0, %2\n\ts_nop 0\n\tglobal_load_lds_dwordx4 %1, off\n\ts_mov_b32 m0, %0" : "=&s"(keep) : "v"(gaddr), "s"(ldst) : "memory");
}
DI float dot2(u32 a, u32 b, float c) { return __builtin_amdgcn_fdot2_f32_bf16(__builtin_bit_cast(bf16x2, a), __builtin_bit_cast(bf16x2, b), c, false); }
DI f2 cmul(f2 a, f2 b) { f2 t = f2{a.x, a.x} * b; return __builtin_elementwise_fma(f2{a.y, a.y}, f2{-b.y, b.x}, t); }
DI f2 cexp_rev(float ar, float ai, float t) {
    const float e = __builtin_amdgcn_exp2f(ar * t), ph = __builtin_amdgcn_fractf(ai * t);
    return f2{e * __builtin_amdgcn_cosf(ph), e * __builtin_amdgcn_sinf(ph)};
}
DI u32 swz_b(u32 row) { return ((row & 3u) << 2) | ((row >> 2) & 3u); }
DI u32 off_b(u32 row, u32 ch) { return 256u * row + 16u * (ch ^ swz_b(row)); }
DI u32 off_h(u32 row, u32 ch) { return 1024u * (row >> 3) + 512u * (ch >> 2) + 64u * (row & 7u) + 16u * ((ch & 3u) ^ ((row >> 2) & 3u)); }
DI u32 ln_pack(float mean, float var) {
    const float m = rbf(mean), sd = rbf(__builtin_amdgcn_sqrtf(rbf(rbf(var) + 1e-5f)));
    return (__float_as_uint(m) >> 16) | (__float_as_uint(sd) & 0xffff0000u);
}

#define STAMP(i) do { } while (0)
#define CSTAMP(i) do { } while (0)
#define STAMP_DECL
#define STAMP_FLUSH do { } while (0)
#define SGB(mask, n) __builtin_amdgcn_sched_group_barrier(mask, n, 0)
#define SB0() __builtin_amdgcn_sched_barrier(0)
constexpr int M_MFMA = 0x8, M_VMEM = 0x10, M_DSR = 0x100, M_VALU = 0x2;
DI void delay_ticks(u32 n) { const u64 t0 = __builtin_amdgcn_s_memrealtime(); while ((u32)(__builtin_amdgcn_s_memrealtime() - t0) < n) __builtin_amdgcn_s_sleep(1); }
DI u32 opaque(u32 v) { asm volatile("" : "+v"(v)); return v; }

__global__ void __launch_bounds__(512, 2)
fused_kernel(const u16* __restrict__ x, const u16* __restrict__ Are, const u16* __restrict__ Aim, const u16* __restrict__ Bre, const u16* __restrict__ Bim,
             const u16* __restrict__ Cre, const u16* __restrict__ Cim, const u16* __restrict__ logdt, const u16* __restrict__ Dskip,
             const u16* __restrict__ wg, const u16* __restrict__ wu, const u16* __restrict__ wd, u16* __restrict__ out, char* __restrict__ ws)
{
    const int tid = threadIdx.x, lane = tid & 63, w = __builtin_amdgcn_readfirstlane(tid >> 6);
    const int b = blockIdx.x, g = b & 7, j = b >> 3;
    const bool mainb = j < 28;
    const int cm = mainb ? g * 28 + j : 0;
    const int P = 4 * (g >> 1) + j / 7, S = 7 * (g & 1) + j % 7;
    const __amdgpu_buffer_rsrc_t rsW = __builtin_amdgcn_make_buffer_rsrc(ws, 0, WS_END, 0x00020000);
    const __amdgpu_buffer_rsrc_t rsX = __builtin_amdgcn_make_buffer_rsrc((void*)x, 0, L_ * D_ * 2, 0x00020000);
    const u32 trq = (lane & 15) >> 2, trp = lane & 3, trh = lane >> 5, trb = (lane >> 4) & 1;
    STAMP_DECL
    STAMP(0);
    {
        u64 pc; asm volatile("s_getpc_b64 %0" : "=s"(pc));
        pc &= ~255ull;
        for (int q = j * 8 + w; q < CODEPF; q += 256) glds16<false>((u32)(q * 1024 + lane * 16), (const void*)pc, 156672);
    }
    const u64 tstart = __builtin_amdgcn_s_memrealtime();
    v4u xr; u64 ep; u32 p_ld2, p_dk2;
    asm volatile("global_load_dwordx4 %0, %1, %2" : "=v"(xr) : "v"((u32)((8 * b + w) * 896 + min(lane, 55) * 16)), "s"(x) : "memory");
    asm volatile("s_load_dwordx2 %0, %3, 0x0\n\ts_load_dword %1, %4, 0x0\n\ts_load_dword %2, %5, 0x0" : "=&s"(ep), "=&s"(p_ld2), "=&s"(p_dk2)
                 : "s"(ws + WS_EP), "s"((const char*)logdt + cm * 4), "s"((const char*)Dskip + cm * 4) : "memory");
    const int ch = w >> 2, dch = 2 * cm + ch;
    if (mainb) {
        const int ai_ = lane >> 3;
        const u16* src = ai_ == 0 ? Are : ai_ == 1 ? Aim : ai_ == 2 ? Bre : ai_ == 3 ? Bim : ai_ == 4 ? Cre : Cim;
        glds16v(src + dch * N_ + (w & 3) * 64 + (lane & 7) * 8, PRM + w * 1024);
        const int ew = w < 6 ? w : w - 6;
        glds16<false>((u32)(ew * 1024 + lane * 16), (const void*)&EO, EOL + ew * 1024);
    } else {
        const int hb = (j - 28) * 8 + g;
#pragma unroll
        for (int k = 0; k < 7; ++k) {
            const int gid = min(hb + 32 * k, 195);
            const int dt_ = gid / 28, fg = gid % 28;
            const u32 row = 8 * w + (lane >> 3), chk = (lane & 7) ^ (((row >> 1) & 1) << 2);
            glds16<false>((u32)((64 * fg + row) * (D_ * 2) + dt_ * 128 + chk * 16), (const void*)wd, k * 8192 + w * 1024);
        }
    }
    asm volatile("s_cmp_ge_i32 %1, 28\n\ts_cbranch_scc1 1f\n\ts_waitcnt vmcnt(2)\n\ts_branch 2f\n1:\n\ts_waitcnt vmcnt(7)\n2:" : "+v"(xr) : "s"(__builtin_amdgcn_readfirstlane(j)) : "memory", "scc");
    asm volatile("s_waitcnt lgkmcnt(0)" : "+s"(ep), "+s"(p_ld2), "+s"(p_dk2) :: "memory");
    ep += 1ull;
    const u32 p_ldt = ch ? p_ld2 >> 16 : p_ld2 & 0xffffu, p_dsk = ch ? p_dk2 >> 16 : p_dk2 & 0xffffu;
    if (lane >= 56) xr = v4u{0, 0, 0, 0};
    const u32 tag = (u32)ep, tagx = (u32)(ep >> 32) << 16;
    if (lane < 56) *lds<v4u>(XT + w * 912 + lane * 16) = xr;
    {
        const float v[8] = {bflo(xr.x), bfhi(xr.x), bflo(xr.y), bfhi(xr.y), bflo(xr.z), bfhi(xr.z), bflo(xr.w), bfhi(xr.w)};
        const float mean = wave_sum(((v[0] + v[1]) + (v[2] + v[3])) + ((v[4] + v[5]) + (v[6] + v[7]))) * (1.f / 448.f);
        float q = 0.f;
#pragma unroll
        for (int e = 0; e < 8; ++e) { const float dl = v[e] - mean; q += dl * dl; }
        q = lane < 56 ? q : 0.f;
        const u32 ln1 = ln_pack(mean, wave_sum(q) * (1.f / 448.f));
        if (lane == 0) __hip_atomic_store((GAS u64*)(ws + WS_SG) + (8 * b + w), ((u64)tag << 32) | ln1, RLX_AGENT);
    }
    STAMP(1);
    const u32 km0 = FM + ch * FM_CH + (w & 3) * 256;
    f2 Ga, Gb, Gc, Gd, KX, KY, G4, G4r, E1, E1r, E256, E256r, E2, E2r, E4, E4r;
#define M1(a, bb) (f2{(a).x, (a).x} * (bb))
#define M2(a, br, t) __builtin_elementwise_fma(f2{(a).y, (a).y}, (br), (t))
#define WROW1(r, v) do { u32 keep_; asm volatile("s_mov_b32 %0, m0\n\ts_mov_b32 m0, %2\n\ts_nop 0\n\tds_write_addtid_b32 %1 offset:%3\n\ts_mov_b32 m0, %0" : "=&s"(keep_) : "v"(v), "s"(km0), "n"((r) * FM_ROW) : "memory"); } while (0)
#define WROW6(r0, v0, r1, v1, r2, v2, r3, v3, r4, v4, r5, v5) do { u32 keep_; asm volatile("s_mov_b32 %0, m0\n\ts_mov_b32 m0, %7\n\ts_nop 0\n\t" \
        "ds_write_addtid_b32 %1 offset:%8\n\tds_write_addtid_b32 %2 offset:%9\n\tds_write_addtid_b32 %3 offset:%10\n\tds_write_addtid_b32 %4 offset:%11\n\tds_write_addtid_b32 %5 offset:%12\n\tds_write_addtid_b32 %6 offset:%13\n\ts_mov_b32 m0, %0" \
        : "=&s"(keep_) : "v"(v0), "v"(v1), "v"(v2), "v"(v3), "v"(v4), "v"(v5), "s"(km0), "n"((r0) * FM_ROW), "n"((r1) * FM_ROW), "n"((r2) * FM_ROW), "n"((r3) * FM_ROW), "n"((r4) * FM_ROW), "n"((r5) * FM_ROW) : "memory"); } while (0)
#define WA(r, X) WROW1(r, pk2((X).x, -(X).y))
#define KSTEP(t) do { SB0(); \
        const f2 ta = M1(Ga, G4), tb = M1(Gb, G4), tc = M1(Gc, G4), td_ = M1(Gd, G4), tx = M1(KX, E1), ty = M1(KY, E256); \
        const u32 c0 = pk2(Ga.x, Ga.y), c1 = pk2(Gb.x, Gb.y), c2 = pk2(Gc.x, Gc.y), c3 = pk2(Gd.x, Gd.y), c4 = pk2(KX.x, -KX.y), c5 = pk2(KY.x, -KY.y); \
        f2 ts0, ts1; \
        if ((t) == 0) ts0 = M1(KY, E2); \
        if ((t) == 1 || (t) == 2) ts0 = M1(KY, E4); \
        SB0(); \
        WROW6(21 + 4 * (t), c0, 22 + 4 * (t), c1, 23 + 4 * (t), c2, 24 + 4 * (t), c3, (t), c4, ((t) == 0 ? 8 : (t) == 1 ? 12 : (t) == 2 ? 14 : 13 + (t)), c5); \
        if ((t) == 0) { f2 Z = M2(KY, E2r, ts0); WA(9, Z); ts1 = M1(Z, E2); Z = M2(Z, E2r, ts1); WA(10, Z); ts1 = M1(Z, E2); Z = M2(Z, E2r, ts1); WA(11, Z); } \
        if ((t) == 1) { const f2 V = M2(KY, E4r, ts0); WA(13, V); } \
        if ((t) == 2) { const f2 V = M2(KY, E4r, ts0); WA(15, V); } \
        if ((t) < 7) { Ga = M2(Ga, G4r, ta); Gb = M2(Gb, G4r, tb); Gc = M2(Gc, G4r, tc); Gd = M2(Gd, G4r, td_); KX = M2(KX, E1r, tx); KY = M2(KY, E256r, ty); } \
    } while (0)
    bar();
    auto publish_xg = [&]() {
        u32 x0[4], x1[4];
#pragma unroll
        for (int k = 0; k < 4; ++k) {
            const int id = min(tid + 512 * k, 1791), c = id >> 2, i2 = (id & 3) * 2;
            x0[k] = *lds<u16>(XT + i2 * 912 + c * 2); x1[k] = *lds<u16>(XT + (i2 + 1) * 912 + c * 2);
        }
        SB0();
#pragma unroll
        for (int k = 0; k < 4; ++k)
            __builtin_amdgcn_raw_buffer_store_b128(v4u{x0[k] | tagx, tag, x1[k] | tagx, tag}, rsW, (tid + 512 * k < 1792) ? (u32)(WS_XG + b * 28672 + (tid + 512 * k) * 16) : 0x7ffffff0u, 0, SC1);
    };
    if (mainb) {
    publish_xg();
    {
        vmw<5>();
        {
            const int bb_ = b < 196 ? b : b - 196;
    #pragma unroll
            for (int i = 0; i < 2; ++i) {
                const int id = 2 * bb_ + i, Sp = id / 28, s = id % 28;
                const u32 row = (w & 3) * 4 + (lane >> 4), chk = (lane & 15) ^ swz_b(row);
                glds16<false>((u32)((16 * s + row) * (F_ * 2) + Sp * 256 + chk * 16), (w >> 2) ? (const void*)wu : (const void*)wg, RT + i * 8192 + w * 1024);
            }
        }
        const u32 pa_ = PRM + w * 1024 + lane * 2;
        const u32 p_are = *lds<u16>(pa_), p_aim = *lds<u16>(pa_ + 128), p_bre = *lds<u16>(pa_ + 256), p_bim = *lds<u16>(pa_ + 384), p_cre = *lds<u16>(pa_ + 512), p_cim = *lds<u16>(pa_ + 640);
        const float dt = rbf(expf(bflo(p_ldt)));
        const float zr = dt * bflo(p_are), zi = dt * bflo(p_aim);
        const float ar = zr * 1.4426950408889634f, ai = zi * 0.15915494309189535f;
        const f2 CB = cmul(f2{bflo(p_cre), bflo(p_cim)}, f2{dt * bflo(p_bre), dt * bflo(p_bim)});
        E1 = cexp_rev(ar, ai, 1.f); E256 = cexp_rev(ar, ai, 256.f);
        const f2 G1 = cexp_rev(ar, ai, 8.f);
        E2 = cmul(E1, E1); E4 = cmul(E2, E2);
        const f2 G2 = cmul(G1, G1);
        G4 = cmul(G2, G2);
        E1r = f2{-E1.y, E1.x}; E2r = f2{-E2.y, E2.x}; E4r = f2{-E4.y, E4.x}; E256r = f2{-E256.y, E256.x}; G4r = f2{-G4.y, G4.x};
        Ga = f2{1.f, 0.f}; Gb = G1; Gc = G2; Gd = cmul(G2, G1);
        KX = CB; KY = cmul(CB, E256);
        SB0();
    }
    KSTEP(0); KSTEP(1); KSTEP(2);
    SB0();
    fence();
    STAMP(2);
    {
        KSTEP(3); KSTEP(4); KSTEP(5); KSTEP(6); KSTEP(7);
        SB0();
#undef KSTEP
#undef WA
#undef WROW1
#undef WROW6
#undef M1
#undef M2
        STAMP(3);
        vmw<0>();
        bar();
#pragma unroll
        for (int i = 0; i < 2; ++i) {
            const int id = 2 * b + i, Sp = id / 28, s = id % 28;
            const u32 base = RT + i * 8192 + trb * 4096 + 8 * (trp & 1);
            const v4u f = tr_frag(base + off_b(8 * trh + trq, 2 * w + (trp >> 1)), base + off_b(8 * trh + 4 + trq, 2 * w + (trp >> 1)));
            __builtin_amdgcn_raw_buffer_store_b128(f, rsW, (b < 196 ? (u32)(WS_WG + ((Sp * 8 + w) * 28 + s) * 1024) : 0x7ffffc00u) + lane * 16, 0, SC1);
        }
        const u32 xa = WS_XG + (32 * w + (lane >> 3)) * 28672 + cm * 128 + (lane & 7) * 16, sa = WS_SG + (32 * w + (lane >> 3)) * 64 + (lane & 3) * 16;
        v4u xg[4], sg[4];
        const int ta_ = (w >> 1) & 1, tb_ = w & 1;
        const u32 fa_ = opaque(FM + ch * FM_CH + (16 * ta_ + (lane & 15)) * FM_ROW + (lane >> 4) * 16), fb_ = opaque(FM + ch * FM_CH + (21 + 16 * tb_ + (lane & 15)) * FM_ROW + (lane >> 4) * 16);
        f32x4 f0 = {0.f, 0.f, 0.f, 0.f}, f1 = {0.f, 0.f, 0.f, 0.f};
        v4u A[16], B[16];
#pragma unroll
        for (int ks = 0; ks < 16; ++ks) { A[ks] = *lds<v4u>(fa_ + ks * 64); B[ks] = *lds<v4u>(fb_ + ks * 64); }
        SB0();
#pragma unroll
        for (int k = 0; k < 4; ++k) { xg[k] = __builtin_amdgcn_raw_buffer_load_b128(rsW, xa + k * 8 * 28672, 0, SC1); sg[k] = __builtin_amdgcn_raw_buffer_load_b128(rsW, sa + k * 512, 0, SC1); }
        SB0();
#pragma unroll
        for (int ks = 0; ks < 16; ks += 2) { f0 = mfma16(A[ks], B[ks], f0); f1 = mfma16(A[ks + 1], B[ks + 1], f1); }
        f0 += f1;
#pragma unroll
        for (int r = 0; r < 4; ++r) { const int row = 16 * ta_ + 4 * (lane >> 4) + r; if (row < 24) *lds<float>(FT + ch * FT_CH + (row * 32 + 16 * tb_ + (lane & 15)) * 4) = f0[r]; }
        if ((tid & 255) == 255) *lds<float>(FT + ch * FT_CH + 768 * 4) = 0.f;
        STAMP(4);
        for (u32 spin = 0;; ++spin) {
            u32 bad = 0;
#pragma unroll
            for (int k = 0; k < 4; ++k) bad |= ((sg[k].y ^ tag) | (sg[k].w ^ tag) | (xg[k].y ^ tag)) | ((xg[k].w ^ tag) | (((xg[k].x ^ tagx) | (xg[k].z ^ tagx)) & 0xffff0000u));
            if (__all(bad == 0) || spin > SPIN_CAP) break;
            __builtin_amdgcn_s_sleep(4);
#pragma unroll
            for (int k = 0; k < 4; ++k) { xg[k] = __builtin_amdgcn_raw_buffer_load_b128(rsW, xa + k * 8 * 28672, 0, SC1); sg[k] = __builtin_amdgcn_raw_buffer_load_b128(rsW, sa + k * 512, 0, SC1); }
        }
        STAMP(5);
        const u32 ua = opaque(UU + ((lane >> 2) & 1) * UU_CH + 512 + (32 * w + (lane >> 3)) * 16 + (lane & 3) * 4), xsa = opaque(XS + ((lane >> 2) & 1) * 4096 + (32 * w + (lane >> 3)) * 16 + (lane & 3) * 4);
#pragma unroll
        for (int k = 0; k < 4; ++k) {
            const u32 xm = pk2(bflo(xg[k].x) - bflo(sg[k].x), bflo(xg[k].z) - bflo(sg[k].z));
            const float u0 = bflo(xm) * __builtin_amdgcn_rcpf(bfhi(sg[k].x)), u1 = bfhi(xm) * __builtin_amdgcn_rcpf(bfhi(sg[k].z));
            *lds<u32>(ua + k * 128) = pk2(u0, u1);
            *lds<u32>(xsa + k * 128) = __builtin_amdgcn_perm(xg[k].z, xg[k].x, 0x05040100u);
        }
    }
    if (tid >= 64 && tid < 128) *lds<v4u>(UU + ((tid >> 5) & 1) * UU_CH + (tid & 31) * 16) = v4u{0, 0, 0, 0};
    const u32 eb_ = opaque(EOL + (tid & 255) * 24);
    const v2u ea = *lds<v2u>(eb_), eb = *lds<v2u>(eb_ + 8), ec = *lds<v2u>(eb_ + 16);
    bar();
    if (tid == 0 && b < 196) __hip_atomic_store((GAS u32*)(ws + WS_WR + b * 64), tag, RLX_AGENT);
    const int rq = w & 3;
    const u32 cb = lane & 15, cq = lane >> 4;
    const u32 ubase = opaque(UU + ch * UU_CH + 512 + 32 * (cb - (cq >> 1)) + 16 * (cq & 1) - 32 * 4 * rq - 32 * 2);
    auto ldB = [&](int k, int rr) { return *lds<v4u>(ubase + 32 * (16 * k - 2 * rr + 2)); };
    v4u Bf[8], Bn[8];
#pragma unroll
    for (int p = 0; p < 4; ++p) { Bf[p] = ldB(p, 0); Bn[p] = ldB(p, 1); }
    {
        const u32 fb_ = FT + ch * FT_CH;
        const u32 eo[11] = {ea.x & 0xffffu, ea.x >> 16, ea.y & 0xffffu, ea.y >> 16, eb.x & 0xffffu, eb.x >> 16, eb.y & 0xffffu, eb.y >> 16, ec.x & 0xffffu, ec.x >> 16, ec.y & 0xffffu};
        float v[11];
#pragma unroll
        for (int e = 0; e < 11; ++e) v[e] = *lds<float>(fb_ + eo[e]);
        SB0();
        u32 p[10];
#pragma unroll
        for (int e = 0; e < 10; ++e) p[e] = pk2(v[e], v[e + 1]);
        const u32 ka_ = opaque(KC + ch * KC_CH + (tid & 255) * 16);
#pragma unroll
        for (int s = 0; s < 4; ++s) *lds<v4u>(ka_ + s * KC_COPY) = v4u{p[s], p[s + 2], p[s + 4], p[s + 6]};
    }
    if (tid < 16) *lds<v4u>(KC + (tid >> 3) * KC_CH + ((tid >> 1) & 3) * KC_COPY + 4096 + (tid & 1) * 16) = v4u{0, 0, 0, 0};
    bar();
    STAMP(6);
    CSTAMP(26);
    v4u Wf[28];
    const u32 l16 = lane * 16;
    const int wf_off = WS_WG + (S * 8 + w) * 28 * 1024;
    u32 f2a;
    const float dsv = bflo(p_dsk);
    const u32 f2_off = WS_F2 + g * 16384 + (tid < 224 ? tid : 0) * 64;
    const u32 bt_src = (8 * w + ((lane >> 2) & 7)) * 4096 + 128 * P + (4 * (lane >> 5) + ((lane & 3) ^ ((2 * w + ((lane >> 4) & 1)) & 3))) * 16;
    auto bt_piece = [&](int part, int k) { glds16<true>(bt_src + part * 2048 + k * 64 * 4096, ws + WS_HT, (part ? BTB : BTA) + (8 * k + w) * 1024); };
    {
        const u32 cs = (3u - cb) & 3u;
        const u32 kbase = opaque(KC + ch * KC_CH + cs * KC_COPY + 2 * (2047 - 16 * (cq >> 1) - cb + 8 * (cq & 1) - cs) - 32 * 4 * rq - 32 * 114);
        const u32 kbase8 = opaque(kbase + 8);
        auto ldA = [&](int p, int rr) { const v2u lo = *lds<volatile v2u>(kbase + 32 * (114 - (16 * p + 2 * rr))), hi = *lds<volatile v2u>(kbase8 + 32 * (114 - (16 * p + 2 * rr))); return v4u{lo.x, lo.y, hi.x, hi.y}; };
        const u32 cxw = opaque(CX + (4 * ch * 4 + rq) * 1024 + lane * 16), cxr = opaque(CX + w * 4096 + lane * 16);
        const u32 fu_ = opaque(UU + ch * UU_CH + 512 + (256 * rq + 16 * cb + 4 * cq) * 2), fx_ = opaque(XS + ch * 4096 + (256 * rq + 16 * cb + 4 * cq) * 2);
        const u32 ht_ = WS_HT + dch * 4096 + (256 * rq + 16 * cb + 4 * cq) * 2;
        auto finish = [&](f32x4 (&acc)[4], int tbase) {
#pragma unroll
            for (int i = 0; i < 4; ++i) *lds<f32x4>(cxw + i * 4096) = acc[i];
            const v2u uu = *lds<v2u>(fu_ + tbase * 512), xx = *lds<v2u>(fx_ + tbase * 512);
            bar();
            if (tbase == 4) f2a = __builtin_amdgcn_raw_buffer_load_b32(rsW, w < 4 ? (tid < 224 ? f2_off : (u32)(WS_WF + (tid - 224) * 64)) : f2_off, 0, SC1);
            const f32x4 p0 = *lds<f32x4>(cxr), p1 = *lds<f32x4>(cxr + 1024), p2 = *lds<f32x4>(cxr + 2048), p3 = *lds<f32x4>(cxr + 3072);
            const float uv[4] = {bflo(uu.x), bfhi(uu.x), bflo(uu.y), bfhi(uu.y)}, xv[4] = {bflo(xx.x), bfhi(xx.x), bflo(xx.y), bfhi(xx.y)};
            const u32 k01 = pk2(uv[0] * dsv, uv[1] * dsv), k23 = pk2(uv[2] * dsv, uv[3] * dsv);
            SB0();
            const f32x4 y = (p0 + p1) + (p2 + p3);
            const u32 y01 = pk2(y[0], y[1]), y23 = pk2(y[2], y[3]);
            const u32 z01 = pk2(bflo(y01) + bflo(k01), bfhi(y01) + bfhi(k01)), z23 = pk2(bflo(y23) + bflo(k23), bfhi(y23) + bfhi(k23));
            const float h[4] = {xv[0] + bflo(z01), xv[1] + bfhi(z01), xv[2] + bflo(z23), xv[3] + bfhi(z23)};
            __builtin_amdgcn_raw_buffer_store_b64(v2u{pk2(h[0], h[1]), pk2(h[2], h[3])}, rsW, ht_ + tbase * 512, 0, SC1);
        };
        f32x4 acc[4];
        v4u Af[8], An[8];
#pragma unroll
        for (int i = 0; i < 4; ++i) acc[i] = f32x4{0.f, 0.f, 0.f, 0.f};
#pragma unroll
        for (int p = 0; p < 4; ++p) { Af[p] = ldA(p, 0); An[p] = ldA(p, 1); }
        SB0();
#pragma unroll
        for (int p = 0; p < 4; ++p)
#pragma unroll
            for (int i = p; i < 4; ++i) acc[i] = mfma16(Af[p], Bf[i - p], acc[i]);
#pragma unroll
        for (int p = 0; p < 4; ++p)
#pragma unroll
            for (int i = p; i < 4; ++i) acc[i] = mfma16(An[p], Bn[i - p], acc[i]);
#pragma unroll
        for (int p = 4; p < 8; ++p) { Af[p] = ldA(p, 0); Bf[p] = ldB(p, 0); }
        SB0();
        CSTAMP(16);
        finish(acc, 0);
        CSTAMP(17);
#pragma unroll
        for (int i = 0; i < 4; ++i) acc[i] = f32x4{0.f, 0.f, 0.f, 0.f};
        SB0();
#pragma unroll
        for (int p = 4; p < 8; ++p) { An[p] = ldA(p, 1); Bn[p] = ldB(p, 1); }
        const u32 wr_off = WS_WR + (tid < 196 ? tid : 0) * 64;
        const u32 wr_flag = __builtin_amdgcn_raw_buffer_load_b32(rsW, wr_off, 0, SC1);
#pragma unroll
        for (int p = 0; p < 8; ++p)
#pragma unroll
            for (int i = 0; i < 4; ++i) if (p <= 4 + i) acc[i] = mfma16(Af[p], Bf[4 + i - p], acc[i]);
        SGB(M_VMEM, 1);
#pragma unroll
        for (int i = 0; i < 6; ++i) { SGB(M_MFMA, 2); SGB(M_DSR, 2); SGB(M_MFMA, 2); }
        SB0();
        CSTAMP(18);
        if (!__all(wr_flag == tag)) {
            for (u32 spin = 0; spin < SPIN_CAP; ++spin) {
                if (__all(__builtin_amdgcn_raw_buffer_load_b32(rsW, wr_off, 0, SC1) == tag)) break;
                __builtin_amdgcn_s_sleep(4);
            }
        }
        vmw<0>();
        CSTAMP(19);
        bar();
        CSTAMP(20);
        if (tid < 8) __hip_atomic_store((GAS u32*)(ws + WS_F2 + tid * 16384 + cm * 64), tag, RLX_AGENT);
        SB0();
#pragma unroll
        for (int i = 0; i < 7; ++i) Wf[i] = __builtin_amdgcn_raw_buffer_load_b128(rsW, l16 + (i) * 1024 % 4096, wf_off + (i) * 1024 / 4096 * 4096, WAUX);
#pragma unroll
        for (int p = 0; p < 8; ++p)
#pragma unroll
            for (int i = 0; i < 4; ++i) if (p <= 4 + i) acc[i] = mfma16(An[p], Bn[4 + i - p], acc[i]);
#pragma unroll
        for (int i = 0; i < 6; ++i) { SGB(M_MFMA, 4); SGB(M_VMEM, 1); }
        SGB(M_MFMA, 2); SGB(M_VMEM, 1);
        SB0();
        CSTAMP(21);
#pragma unroll
        for (int i = 7; i < 14; ++i) Wf[i] = __builtin_amdgcn_raw_buffer_load_b128(rsW, l16 + i * 1024 % 4096, wf_off + i * 1024 / 4096 * 4096, WAUX);
        finish(acc, 4);
    }
    STAMP(7);
    CSTAMP(27);
    auto gate_spin = [&](u32 off, bool active) {
        for (u32 spin = 0;; ++spin) {
            const u32 s0 = __builtin_amdgcn_raw_buffer_load_b32(rsW, off, 0, SC1);
            if (__all(s0 == tag || !active) || spin > SPIN_CAP) break;
            __builtin_amdgcn_s_sleep(8);
        }
    };
    if (w < 4 && !__all(f2a == tag)) gate_spin(tid < 224 ? f2_off : (u32)(WS_WF + (tid - 224) * 64), true);
    bar();
    STAMP(8);
    if (b == 0 && tid == 0) __hip_atomic_store((GAS u64*)(ws + WS_EP), ep, RLX_AGENT);
    const float csum = __uint_as_float(__builtin_amdgcn_raw_buffer_load_b32(rsW, WS_CS + (((lane >> 4) & 1) * 1792 + 128 * S + 16 * w + (lane & 15)) * 4, 0, SC1));
    fence();
#pragma unroll
    for (int k = 0; k < 7; ++k) bt_piece(0, k);
    vmw<4>();
    bar();
    if (tid < 8) __hip_atomic_store((GAS u32*)(ws + WS_F2 + (8 + tid) * 16384 + cm * 64), tag, RLX_AGENT);
    STAMP(9);
    CSTAMP(24);
    v4u Wd[28];
    const int td = w & 1, kq = w >> 1, hp = S / 7, dtile = S % 7;
    const int wd_off = WS_WD + ((dtile * 2 + td) * 112 + 28 * kq) * 1024;
    const u32 trb0 = 1024 * trh + 64 * trq + 16 * ((2 * trb + (trp >> 1)) ^ ((2 * trh) & 3)) + 8 * (trp & 1), trb1 = 1024 * trh + 64 * (4 + trq) + 16 * ((2 * trb + (trp >> 1)) ^ ((2 * trh + 1) & 3)) + 8 * (trp & 1);
    const int hh_out = WS_HH + (P * 112 + 8 * S + w) * 4096;
    u32 hres[4] = {0, 0, 0, 0}; float rs = 0.f;
    const u32 f3_off = WS_F3 + (P * 16 + (lane < 14 ? lane : 0)) * 64;
    const char* hsrc = ws + WS_HH + (P * 112 + 28 * kq + td) * 4096 + hp * 1024; const u32 hdst = kq * 1024;
    auto hh_dma = [&](int u) { glds16<true>(l16, hsrc + (u % 28) * 4096 + (u >= 28 ? 2048 : 0), (td ? hh_slot(u + 1) : hh_slot(u)) + hdst); };
#define GEMM1_PASS(PART) do { \
        const u32 b0 = opaque(((PART) ? BTB : BTA) + trb0), b1 = opaque(((PART) ? BTB : BTA) + trb1); \
        const u32 s0_ = opaque(b0 + (w >> 1) * 2048 + (w & 1) * 512), s1_ = opaque(b1 + (w >> 1) * 2048 + (w & 1) * 512); \
        f32x16 acc[2]; \
        _Pragma("unroll") for (int i = 0; i < 16; ++i) { acc[0][i] = 0.f; acc[1][i] = 0.f; } \
        v4u Bq[3][2], Of; \
        float sa0 = 0.f, sa1 = 0.f, sq0 = 0.f, sq1 = 0.f; \
        u32 gatev = 0; \
        _Pragma("unroll") for (int t = 0; t < 2; ++t) { Bq[0][t] = tr_frag(b0 + 512 * t, b1 + 512 * t); Bq[1][t] = tr_frag(b0 + 2048 + 512 * t, b1 + 2048 + 512 * t); } \
        _Pragma("unroll") for (int s = 0; s < 28; ++s) { \
            SB0(); \
            if (!(PART)) { \
                if (s == 10) { vmw<10>(); bar(); }        \
                if (s == 19) {     \
                    if (w < 4) { if (!__all(gatev == tag || tid >= 224)) gate_spin(f2_off + 8 * 16384, tid < 224); } \
                    bar(); \
                } \
            } else { \
                if (s == 3) { vmw<2>(); bar(); if (tid == 0) __hip_atomic_store((GAS u32*)(ws + WS_F3 + (P * 16 + S) * 64), tag, RLX_AGENT); }     \
                if (s == 20) {     \
                    if (w == 0) { if (!__all(gatev == tag)) gate_spin(f3_off, true); } \
                    bar(); \
                } \
            } \
            SB0(); \
            if (s + 2 < 28) { _Pragma("unroll") for (int t = 0; t < 2; ++t) Bq[(s + 2) % 3][t] = tr_frag(b0 + (s + 2) * 2048 + 512 * t, b1 + (s + 2) * 2048 + 512 * t); } \
            if ((s & 3) == 0 && !EXPS) Of = tr_frag(s0_ + s * 2048, s1_ + s * 2048);        \
            if (!(PART)) { \
                if (s < 14) { if (!EXPA) Wf[14 + s] = __builtin_amdgcn_raw_buffer_load_b128(rsW, l16 + ((14 + s)) * 1024 % 4096, wf_off + ((14 + s)) * 1024 / 4096 * 4096, WAUX); } \
                else if (s == 14) { if (w < 4) gatev = __builtin_amdgcn_raw_buffer_load_b32(rsW, f2_off + 8 * 16384, 0, SC1); } \
                else if (s < 19) Wd[s - 15] = __builtin_amdgcn_raw_buffer_load_b128(rsW, l16 + ((s - 15)) * 1024 % 4096, wd_off + ((s - 15)) * 1024 / 4096 * 4096, WAUX); \
                else if (s < 26) bt_piece(1, s - 19); \
            } else { \
                if (s >= 1 && s < 25) Wd[3 + s] = __builtin_amdgcn_raw_buffer_load_b128(rsW, l16 + (3 + s) * 1024 % 4096, wd_off + (3 + s) * 1024 / 4096 * 4096, WAUX); \
                if (s == 15) { if (w == 0) gatev = __builtin_amdgcn_raw_buffer_load_b32(rsW, f3_off, 0, SC1); } \
                if (s >= 20 && s < 27) { fence(); hh_dma(2 * (s - 20)); } \
                if (s >= 25) { fence(); hh_dma(14 + 2 * (s - 25)); }        \
            } \
            if (EXPP) __builtin_amdgcn_s_setprio(1); \
            _Pragma("unroll") for (int t = 0; t < 2; ++t) acc[t] = mfma32(Wf[s], Bq[s % 3][t], acc[t]); \
            if (EXPP) __builtin_amdgcn_s_setprio(0); \
            if ((s & 3) == 2 && !EXPS) { \
                _Pragma("unroll") for (int e = 0; e < 4; e += 2) { \
                    const float l0 = bflo(Of[e]), h0 = bfhi(Of[e]), l1 = bflo(Of[e + 1]), h1 = bfhi(Of[e + 1]); \
                    sa0 += l0; sq0 = __builtin_fmaf(l0, l0, sq0); sa1 += l1; sq1 = __builtin_fmaf(l1, l1, sq1); \
                    sa0 += h0; sq0 = __builtin_fmaf(h0, h0, sq0); sa1 += h1; sq1 = __builtin_fmaf(h1, h1, sq1); \
                } \
                asm volatile("" : "+v"(sa0), "+v"(sa1), "+v"(sq0), "+v"(sq1));        \
                SGB(M_MFMA, 1); SGB(M_DSR, 2); SGB(M_VALU, 12); SGB(M_VMEM, 1); SGB(M_MFMA, 1); SGB(M_DSR, 2); SGB(M_VALU, 12); \
            } else { SGB(M_MFMA, 1); SGB(M_DSR, 3); SGB(M_VMEM, 1); SGB(M_MFMA, 1); SGB(M_DSR, 3); } \
        } \
        SB0(); \
        { \
            const float sh = half_sum(sa0 + sa1), sq = half_sum(sq0 + sq1); \
            const u32 wa_ = opaque(SPQ + (PART) * 2048 + (w & 1) * 1024 + (w >> 1) * 4 + lane * 32); \
            if (lane < 32) { *lds<float>(wa_) = sh; *lds<float>(wa_ + 16) = sq; if (!(PART)) *lds<float>(CSL + w * 128 + lane * 4) = csum; } \
        } \
        bar(); \
        {     \
            const u32 ca_ = opaque(CSL + w * 128 + trh * 16), sa_ = opaque(SPQ + (PART) * 2048 + (lane & 31) * 32); \
            const f32x4 csg[2] = {*lds<f32x4>(ca_), *lds<f32x4>(ca_ + 32)}, csu[2] = {*lds<f32x4>(ca_ + 64), *lds<f32x4>(ca_ + 96)}; \
            const f32x4 sm[2] = {*lds<f32x4>(sa_), *lds<f32x4>(sa_ + 1024)}; \
            const bool mine_ = (kq >> 1) == (PART);        \
            f32x4 sq4 = {0.f, 0.f, 0.f, 0.f}; u32 hb_[8]; \
            if (mine_) { \
                sq4 = *lds<f32x4>(sa_ + hp * 1024 + 16); \
                const u32 lc_ = 4 * hp + ((lane & 31) >> 3), r8_ = 8 * dtile + 4 * td + 2 * (kq & 1);        \
                const u32 hb0_ = ((PART) ? BTB : BTA) + r8_ * 1024 + (lc_ >> 2) * 512 + trh * 256 + (lane & 7) * 2; \
                const u32 h0_ = opaque(hb0_ + 16 * ((lc_ & 3) ^ trh)), h1_ = opaque(hb0_ + 1024 + 16 * ((lc_ & 3) ^ (2 + trh))); \
                _Pragma("unroll") for (int e = 0; e < 4; ++e) { hb_[e] = *lds<u16>(h0_ + 64 * e); hb_[4 + e] = *lds<u16>(h1_ + 64 * e); } \
            } \
            SB0(); \
            _Pragma("unroll") for (int t = 0; t < 2; ++t) { \
                const float m = rbf(((sm[t][0] + sm[t][1]) + (sm[t][2] + sm[t][3])) * (1.f / 448.f)); \
                float hv[8]; \
                _Pragma("unroll") for (int e = 0; e < 8; ++e) hv[e] = (acc[t][e] - m * csg[e >> 2][e & 3]) * (acc[t][e + 8] - m * csu[e >> 2][e & 3]); \
                __builtin_amdgcn_raw_buffer_store_b128(v4u{pk2(hv[0], hv[1]), pk2(hv[2], hv[3]), pk2(hv[4], hv[5]), pk2(hv[6], hv[7])}, rsW, l16 + hh_out + (2 * (PART) + t) * 1024, 0, SC1); \
            } \
            if (mine_) { \
                const f32x4 s4 = hp ? sm[1] : sm[0]; \
                const float mean = ((s4[0] + s4[1]) + (s4[2] + s4[3])) * (1.f / 448.f), var = fmaxf(((sq4[0] + sq4[1]) + (sq4[2] + sq4[3])) * (1.f / 448.f) - mean * mean, 0.f); \
                const float sd = bfhi(ln_pack(mean, var)); \
                rs = __builtin_amdgcn_rcpf(sd * sd); \
                _Pragma("unroll") for (int e = 0; e < 4; ++e) hres[e] = hb_[2 * e] | (hb_[2 * e + 1] << 16); \
            } \
        } \
    } while (0)
    if (EXPA) {
#pragma unroll
        for (int s = 14; s < 28; ++s) Wf[s] = __builtin_amdgcn_raw_buffer_load_b128(rsW, l16 + s * 1024 % 4096, wf_off + s * 1024 / 4096 * 4096, WAUX);
        vmw<0>(); bar(); CSTAMP(30);
    }
    GEMM1_PASS(0);
    CSTAMP(31);
    vmw<2>();
    bar();
    STAMP(10);
    GEMM1_PASS(1);
    STAMP(11);
    CSTAMP(25);
#undef GEMM1_PASS
    vmw<2>();
    bar();
    STAMP(12);
    CSTAMP(28);
    f32x16 o2[2];
#pragma unroll
    for (int i = 0; i < 16; ++i) { o2[0][i] = 0.f; o2[1][i] = 0.f; }
    const u32 hrdA = opaque(BTA + kq * 1024 + lane * 16), hrdS = opaque(RSP + kq * 1024 + lane * 16), hrdB = opaque(BTB + kq * 1024 + lane * 16);
    auto hh_rd = [&](int u) {
        const u32 sl = hh_slot(u);
        return *lds<v4u>(sl >= RSP ? hrdS + (sl - RSP) : sl >= BTA ? hrdA + (sl - BTA) : hrdB + (sl - BTB));
    };
    {
        v4u hf[4];
        u32 gatev = 0;
#pragma unroll
        for (int u = 0; u < 3; ++u) hf[u] = hh_rd(u);
#pragma unroll
        for (int u = 0; u < 28; ++u) {
            SB0();
            if (u < 4) hh_dma(20 + 2 * u);
            if (u == 6) { vmw<4>(); bar(); if (tid == 0) __hip_atomic_store((GAS u32*)(ws + WS_F3 + 16384 + (P * 16 + S) * 64), tag, RLX_AGENT); }
            if (u == 17) { vmw<0>(); bar(); }
            SB0();
            if (u + 3 < 28) hf[(u + 3) & 3] = hh_rd(u + 3);
            if (u == 22 && w == 0) gatev = __builtin_amdgcn_raw_buffer_load_b32(rsW, f3_off + 16384, 0, SC1);
            o2[0] = mfma32(Wd[u], hf[u & 3], o2[0]);
        }
        SB0();
        if (w == 0) { if (!__all(gatev == tag)) gate_spin(f3_off + 16384, true); }
        bar();
        STAMP(13);
#pragma unroll
        for (int u = 28; u < 40; u += 2) hh_dma(u);
        vmw<3>(); bar();
#pragma unroll
        for (int u = 0; u < 3; ++u) hf[u] = hh_rd(28 + u);
#pragma unroll
        for (int u = 0; u < 28; ++u) {
            SB0();
            if (u == 3) { vmw<3>(); bar(); }
            if (u == 9) { vmw<5>(); bar(); }
            if (u == 15) { vmw<2>(); bar(); }
            if (u == 21) { vmw<0>(); bar(); }
            if (u < 8) hh_dma(40 + 2 * u);
            SB0();
            if (u + 3 < 28) hf[(u + 3) & 3] = hh_rd(28 + u + 3);
            o2[1] = mfma32(Wd[u], hf[u & 3], o2[1]);
        }
        SB0();
        bar();
    }
    STAMP(14);
    CSTAMP(29);
    {
        const u32 red = opaque(td * 24576 + lane * 16);
        f32x4 v[2];
        auto give = [&](auto KQ) {
            constexpr int kk = KQ.value;
#pragma unroll
            for (int k2 = 0; k2 < 4; ++k2) {
                const f32x16& a = o2[k2 >> 1];
#pragma unroll
                for (int q = 0; q < 2; ++q) {
                    const int r0 = 8 * (k2 & 1) + 4 * q;
                    const f32x4 t = {a[r0], a[r0 + 1], a[r0 + 2], a[r0 + 3]};
                    if (k2 != kk) *lds<f32x4>(red + (k2 * 3 + (kk > k2 ? kk - 1 : kk)) * 2048 + q * 1024) = t; else v[q] = t;
                }
            }
        };
        switch (kq) {
            case 0: give(std::integral_constant<int, 0>{}); break;
            case 1: give(std::integral_constant<int, 1>{}); break;
            case 2: give(std::integral_constant<int, 2>{}); break;
            default: give(std::integral_constant<int, 3>{}); break;
        }
        bar();
        const u32 rd = opaque(red + kq * 6144);
        f32x4 pr[2][3];
#pragma unroll
        for (int q = 0; q < 2; ++q)
#pragma unroll
            for (int si = 0; si < 3; ++si) pr[q][si] = *lds<f32x4>(rd + si * 2048 + q * 1024);
        GAS char* op = (GAS char*)out + (size_t)(1024 * (kq >> 1) + 64 * P + 32 * hp + (lane & 31)) * (D_ * 2) + (64 * dtile + 32 * td + 16 * (kq & 1) + 4 * trh) * 2;
        SB0();
#pragma unroll
        for (int q = 0; q < 2; ++q) {
            v[q] = (v[q] + pr[q][0]) + (pr[q][1] + pr[q][2]);
            const u32 f01 = pk2(v[q][0] * rs, v[q][1] * rs), f23 = pk2(v[q][2] * rs, v[q][3] * rs);
            const float r[4] = {bflo(hres[2 * q]) + bflo(f01), bfhi(hres[2 * q]) + bfhi(f01), bflo(hres[2 * q + 1]) + bflo(f23), bfhi(hres[2 * q + 1]) + bfhi(f23)};
            *(GAS v2u*)(op + q * 16) = v2u{pk2(r[0], r[1]), pk2(r[2], r[3])};
        }
    }
    STAMP(15);
    STAMP_FLUSH;
    } else {
        publish_xg();
        const int hb = (j - 28) * 8 + g;
        float cs[8];
        {
            const u16* wsrc = hb < 16 ? wg : wu;
            const int c0 = 112 * (hb & 15) + 8 * (tid % 14), r0 = tid / 14;
#pragma unroll
            for (int e = 0; e < 8; ++e) cs[e] = 0.f;
            v4u t[13];
#pragma unroll
            for (int i = 0; i < 13; ++i) { const int r = r0 + 36 * i; t[i] = (tid < 504 && r < 448) ? *(const v4u*)(wsrc + (size_t)r * F_ + c0) : v4u{0, 0, 0, 0}; }
#pragma unroll
            for (int i = 0; i < 13; ++i) { cs[0] += bflo(t[i].x); cs[1] += bfhi(t[i].x); cs[2] += bflo(t[i].y); cs[3] += bfhi(t[i].y); cs[4] += bflo(t[i].z); cs[5] += bfhi(t[i].z); cs[6] += bflo(t[i].w); cs[7] += bfhi(t[i].w); }
        }
        vmw<0>(); bar();
#pragma unroll
        for (int k = 0; k < 7; ++k) {
            const int gid = hb + 32 * k;
            if (gid < 196) {
                const int dt_ = gid / 28, fg = gid % 28, fsl = w >> 1, td = w & 1;
                u32 ad[2];
#pragma unroll
                for (int t = 0; t < 2; ++t) { const u32 row = 16 * fsl + 8 * t + 4 * trh + trq, chk = 4 * td + 2 * trb + (trp >> 1); ad[t] = k * 8192 + row * 128 + 16 * (chk ^ (((row >> 1) & 1) << 2)) + 8 * (trp & 1); }
                const v4u f = tr_frag(ad[0], ad[1]);
                __builtin_amdgcn_raw_buffer_store_b128(f, rsW, WS_WD + ((dt_ * 2 + td) * 112 + 4 * fg + fsl) * 1024 + lane * 16, 0, SC1);
            }
        }
        if (tid < 504) {
            *lds<f32x4>(65536 + tid * 32) = f32x4{cs[0], cs[1], cs[2], cs[3]};
            *lds<f32x4>(65536 + tid * 32 + 16) = f32x4{cs[4], cs[5], cs[6], cs[7]};
        }
        bar();
        if (tid < 112) {
            float s = 0.f;
            for (int r = 0; r < 36; ++r) s += *lds<float>(65536 + (r * 14 + (tid >> 3)) * 32 + (tid & 7) * 4);
            __hip_atomic_store((GAS u32*)(ws + WS_CS) + ((hb < 16 ? 0 : 1792) + 112 * (hb & 15) + tid), __float_as_uint(s), RLX_AGENT);
        }
        vmw<0>(); bar();
        if (tid == 0) __hip_atomic_store((GAS u32*)(ws + WS_WF + hb * 64), tag, RLX_AGENT);

    }
}

extern "C" void kernel_launch(void* const* d_in, const int* in_sizes, int n_in, void* d_out, int out_size, void* d_ws, size_t ws_size, hipStream_t stream) {
    (void)in_sizes; (void)n_in; (void)out_size; (void)ws_size;
    (void)hipFuncSetAttribute((const void*)fused_kernel, hipFuncAttributeMaxDynamicSharedMemorySize, LDS_BYTES);
    fused_kernel<<<256, 512, LDS_BYTES, stream>>>((const u16*)d_in[0], (const u16*)d_in[1], (const u16*)d_in[2], (const u16*)d_in[3], (const u16*)d_in[4],
        (const u16*)d_in[5], (const u16*)d_in[6], (const u16*)d_in[7], (const u16*)d_in[8], (const u16*)d_in[10], (const u16*)d_in[11], (const u16*)d_in[12],
        (u16*)d_out, (char*)d_ws);
}
```

```cpp
#include <hip/hip_runtime.h>
#include <stdint.h>

typedef unsigned short u16;
typedef unsigned u32;
typedef unsigned long long u64;
typedef u32 v4u __attribute__((ext_vector_type(4)));
typedef u32 v2u __attribute__((ext_vector_type(2)));
typedef float f32x4 __attribute__((ext_vector_type(4)));
typedef float f32x16 __attribute__((ext_vector_type(16)));
typedef float f2 __attribute__((ext_vector_type(2)));
typedef __bf16 bf16x8 __attribute__((ext_vector_type(8)));
typedef __bf16 bf16x2 __attribute__((ext_vector_type(2)));
typedef short s16x4 __attribute__((ext_vector_type(4)));
#define LAS __attribute__((address_space(3)))
#define GAS __attribute__((address_space(1)))
#define DI __device__ __forceinline__
#define RLX_AGENT __ATOMIC_RELAXED, __HIP_MEMORY_SCOPE_AGENT
#define SC1 16
#define EXPG 0

constexpr int L_ = 2048, D_ = 448, N_ = 256, F_ = 1792;
constexpr u32 WS_EP = 0;
constexpr u32 WS_SG = 4096;
constexpr u32 WS_WF = 32768;
constexpr u32 WS_CS = 40960;
constexpr u32 WS_HT = 65536;
constexpr u32 WS_WR = 1966080;
constexpr u32 WS_HH = 2u << 20;
constexpr u32 WS_WG = 10u << 20;
constexpr u32 WS_WD = 14u << 20;
constexpr u32 WS_XG = 16u << 20;
constexpr u32 WS_F2 = (23u << 20) + (512u << 10);
constexpr u32 WS_F3 = WS_F2 + 2 * 8 * 16384;
constexpr u32 WS_END = 24u << 20;
constexpr int CODEPF = 26;
constexpr u32 SPIN_CAP = 1u << 16;

constexpr u32 LDS_BYTES = 163840;
constexpr u32 FM = 0, FM_ROW = 1056, FM_CH = 53 * FM_ROW;
constexpr u32 FT = 2 * FM_CH, FT_CH = 772 * 4;
constexpr u32 EOL = FT + 2 * FT_CH;
constexpr u32 RT = 124288;
constexpr u32 PRM = RT + 16384;
constexpr u32 XT = PRM + 8192;
constexpr u32 KC = 0, KC_COPY = 4160, KC_CH = 4 * KC_COPY;
constexpr u32 CX = 2 * KC_CH;
constexpr u32 UU = RT, UU_CH = (256 + 2048) * 2;
constexpr u32 XS = UU + 2 * UU_CH;
static_assert(EOL % 16 == 0 && EOL + 6144 <= RT && XS + 8192 <= XT && XT + 8 * 912 <= 156672, "LDS map");
constexpr u32 BT_PART = 448 * 128;
constexpr u32 BTA = CX + 32768, BTB = 0;
constexpr u32 RSP = BTA + BT_PART;
constexpr u32 SM = 155648;
constexpr u32 SPQ = SM;
constexpr u32 CSL = SPQ + 4096;
static_assert(BTA + BT_PART <= RT && RSP + 7 * 4096 <= SM && CSL + 1024 <= 161280, "LDS map");
constexpr u32 hh_slot(int u) {
    return u < 14 ? BTA + u * 4096 : u < 21 ? RSP + (u - 14) * 4096 : u < 35 ? BTB + (u - 21) * 4096 : u < 49 ? BTA + (u - 35) * 4096 : RSP + (u - 49) * 4096;
}

struct EoTab { u16 v[256 * 12]; };
constexpr u32 foff_c(int j) {
    if (j < 0) return 768u;
    u32 t = (u32)j;
    int s = t >= 1024 ? 3 : t >= 512 ? 2 : t >= 256 ? 1 : 0;
    if (s > 0) t = ((t + (1u << (s - 1)) - 1u + ((t >> s) & 1u)) >> s) << s;
    u32 col = (t >> 3) & 31u, row = 0;
    if (t < 256u) row = t & 7u;
    else if (t < 512u) row = 8u + ((t >> 1) & 3u);
    else if (t < 1024u) row = 12u + 2u * ((t >> 8) & 1u) + ((t >> 2) & 1u);
    else if (t < 2048u) row = 16u + ((t >> 8) & 3u);
    else row = 20u;
    return row * 32u + col;
}
constexpr EoTab make_eo() { EoTab e{}; for (int t = 0; t < 256; ++t) for (int k = 0; k < 12; ++k) e.v[t * 12 + k] = (u16)(k < 11 ? foff_c(2047 - 8 * t - k) * 4u : 0u); return e; }
__device__ const __attribute__((aligned(16))) EoTab EO = make_eo();

DI float bflo(u32 d) { return __uint_as_float(d << 16); }
DI float bfhi(u32 d) { return __uint_as_float(d & 0xffff0000u); }
DI u32 pk2(float lo, float hi) { bf16x2 v = {(__bf16)lo, (__bf16)hi}; return __builtin_bit_cast(u32, v); }
DI float rbf(float f) { return bflo(pk2(f, f)); }
DI bf16x8 as_bf(v4u v) { return __builtin_bit_cast(bf16x8, v); }
DI f32x4 mfma16(v4u a, v4u b, f32x4 c) { return __builtin_amdgcn_mfma_f32_16x16x32_bf16(as_bf(a), as_bf(b), c, 0, 0, 0); }
DI f32x16 mfma32(v4u a, v4u b, f32x16 c) { return __builtin_amdgcn_mfma_f32_32x32x16_bf16(as_bf(a), as_bf(b), c, 0, 0, 0); }
template <class T> DI LAS T* lds(u32 off) { return (LAS T*)(uintptr_t)off; }
DI v2u tr_read(u32 off) { s16x4 r = __builtin_amdgcn_ds_read_tr16_b64_v4i16(lds<s16x4>(off)); return __builtin_bit_cast(v2u, r); }
DI v4u tr_frag(u32 off0, u32 off1) { v2u a = tr_read(off0), b = tr_read(off1); return v4u{a.x, a.y, b.x, b.y}; }
template <int CTRL, int RM> DI float dpp_add(float v) { int t = __builtin_amdgcn_update_dpp(0, __float_as_int(v), CTRL, RM, 0xf, true); return v + __int_as_float(t); }
DI float wave_sum(float v) {
    v = dpp_add<0x111, 0xf>(v); v = dpp_add<0x112, 0xf>(v); v = dpp_add<0x114, 0xf>(v); v = dpp_add<0x118, 0xf>(v);
    v = dpp_add<0x142, 0xa>(v); v = dpp_add<0x143, 0xc>(v);
    return __int_as_float(__builtin_amdgcn_readlane(__float_as_int(v), 63));
}
DI float half_sum(float v) { const u32 u = __float_as_uint(v); const auto r = __builtin_amdgcn_permlane32_swap(u, u, false, false); return __uint_as_float(r[0]) + __uint_as_float(r[1]); }
DI void fence() { asm volatile("" ::: "memory"); }
DI void bar() { asm volatile("s_waitcnt lgkmcnt(0)" ::: "memory"); __builtin_amdgcn_s_barrier(); asm volatile("" ::: "memory"); }
template <int N> DI void vmw() { asm volatile("s_waitcnt vmcnt(%0)" :: "n"(N) : "memory"); }
template <bool S1> DI void glds16(u32 voff, const void* sbase, u32 ldst) {
    u32 keep;
    if (S1) asm volatile("s_mov_b32 %0, m0\n\ts_mov_b32 m0, %3\n\ts_nop 0\n\tglobal_load_lds_dwordx4 %1, %2 sc1\n\ts_mov_b32 m0, %0" : "=&s"(keep) : "v"(voff), "s"(sbase), "s"(ldst) : "memory");
    else    asm volatile("s_mov_b32 %0, m0\n\ts_mov_b32 m0, %3\n\ts_nop 0\n\tglobal_load_lds_dwordx4 %1, %2\n\ts_mov_b32 m0, %0" : "=&s"(keep) : "v"(voff), "s"(sbase), "s"(ldst) : "memory");
}
DI void glds16v(const void* gaddr, u32 ldst) {
    u32 keep;
    asm volatile("s_mov_b32 %0, m0\n\ts_mov_b32 m0, %2\n\ts_nop 0\n\tglobal_load_lds_dwordx4 %1, off\n\ts_mov_b32 m0, %0" : "=&s"(keep) : "v"(gaddr), "s"(ldst) : "memory");
}
DI float dot2(u32 a, u32 b, float c) { return __builtin_amdgcn_fdot2_f32_bf16(__builtin_bit_cast(bf16x2, a), __builtin_bit_cast(bf16x2, b), c, false); }
DI f2 cmul(f2 a, f2 b) { f2 t = f2{a.x, a.x} * b; return __builtin_elementwise_fma(f2{a.y, a.y}, f2{-b.y, b.x}, t); }
DI f2 cexp_rev(float ar, float ai, float t) {
    const float e = __builtin_amdgcn_exp2f(ar * t), ph = __builtin_amdgcn_fractf(ai * t);
    return f2{e * __builtin_amdgcn_cosf(ph), e * __builtin_amdgcn_sinf(ph)};
}
DI u32 swz_b(u32 row) { return ((row & 3u) << 2) | ((row >> 2) & 3u); }
DI u32 off_b(u32 row, u32 ch) { return 256u * row + 16u * (ch ^ swz_b(row)); }
DI u32 off_h(u32 row, u32 ch) { return 1024u * (row >> 3) + 512u * (ch >> 2) + 64u * (row & 7u) + 16u * ((ch & 3u) ^ ((row >> 2) & 3u)); }
DI u32 ln_pack(float mean, float var) {
    const float m = rbf(mean), sd = rbf(__builtin_amdgcn_sqrtf(rbf(rbf(var) + 1e-5f)));
    return (__float_as_uint(m) >> 16) | (__float_as_uint(sd) & 0xffff0000u);
}

#define SGB(mask, n) __builtin_amdgcn_sched_group_barrier(mask, n, 0)
#define SB0() __builtin_amdgcn_sched_barrier(0)
constexpr int M_MFMA = 0x8, M_VMEM = 0x10, M_DSR = 0x100, M_VALU = 0x2;
DI u32 opaque(u32 v) { asm volatile("" : "+v"(v)); return v; }

__global__ void __launch_bounds__(512, 2)
fused_kernel(const u16* __restrict__ x, const u16* __restrict__ Are, const u16* __restrict__ Aim, const u16* __restrict__ Bre, const u16* __restrict__ Bim,
             const u16* __restrict__ Cre, const u16* __restrict__ Cim, const u16* __restrict__ logdt, const u16* __restrict__ Dskip,
             const u16* __restrict__ wg, const u16* __restrict__ wu, const u16* __restrict__ wd, u16* __restrict__ out, char* __restrict__ ws)
{
    const int tid = threadIdx.x, lane = tid & 63, w = __builtin_amdgcn_readfirstlane(tid >> 6);
    asm volatile("" :: "s"(x), "s"(Are), "s"(Aim), "s"(Bre), "s"(Bim), "s"(Cre), "s"(Cim), "s"(logdt), "s"(Dskip), "s"(wg), "s"(wu), "s"(wd), "s"(out), "s"(ws));
    const int b = blockIdx.x, g = b & 7, j = b >> 3;
    const bool mainb = j < 28;
    const int cm = mainb ? g * 28 + j : 0;
    const int P = 4 * (g >> 1) + j / 7, S = 7 * (g & 1) + j % 7;
    const __amdgpu_buffer_rsrc_t rsW = __builtin_amdgcn_make_buffer_rsrc(ws, 0, WS_END, 0x00020000);
    const __amdgpu_buffer_rsrc_t rsO = __builtin_amdgcn_make_buffer_rsrc((void*)out, 0, L_ * D_ * 2, 0x00020000);
    const u32 trq = (lane & 15) >> 2, trp = lane & 3, trh = lane >> 5, trb = (lane >> 4) & 1;
    {
        u64 pc; asm volatile("s_getpc_b64 %0" : "=s"(pc));
        pc &= ~255ull;
        for (int q = j * 8 + w; q < CODEPF; q += 256) glds16<false>((u32)(q * 1024 + lane * 16), (const void*)pc, 156672);
    }
    v4u xr; u64 ep; u32 p_ld2, p_dk2;
    asm volatile("global_load_dwordx4 %0, %1, %2" : "=v"(xr) : "v"((u32)((8 * b + w) * 896 + min(lane, 55) * 16)), "s"(x) : "memory");
    asm volatile("s_load_dwordx2 %0, %3, 0x0\n\ts_load_dword %1, %4, 0x0\n\ts_load_dword %2, %5, 0x0" : "=&s"(ep), "=&s"(p_ld2), "=&s"(p_dk2)
                 : "s"(ws + WS_EP), "s"((const char*)logdt + cm * 4), "s"((const char*)Dskip + cm * 4) : "memory");
    const int ch = w >> 2, dch = 2 * cm + ch;
    if (mainb) {
        const int ai_ = lane >> 3;
        u64 src_ = (u64)Cim;
        src_ = ai_ == 4 ? (u64)Cre : src_; src_ = ai_ == 3 ? (u64)Bim : src_; src_ = ai_ == 2 ? (u64)Bre : src_; src_ = ai_ == 1 ? (u64)Aim : src_; src_ = ai_ == 0 ? (u64)Are : src_;
        const u16* src = (const u16*)src_;
        glds16v(src + dch * N_ + (w & 3) * 64 + (lane & 7) * 8, PRM + w * 1024);
        const int ew = w < 6 ? w : w - 6;
        glds16<false>((u32)(ew * 1024 + lane * 16), (const void*)&EO, EOL + ew * 1024);
    } else {
        const int hb = (j - 28) * 8 + g;
#pragma unroll
        for (int k = 0; k < 7; ++k) {
            const int gid = min(hb + 32 * k, 195);
            const int dt_ = gid / 28, fg = gid % 28;
            const u32 row = 8 * w + (lane >> 3), chk = (lane & 7) ^ (((row >> 1) & 1) << 2);
            glds16<false>((u32)((64 * fg + row) * (D_ * 2) + dt_ * 128 + chk * 16), (const void*)wd, k * 8192 + w * 1024);
        }
    }
    asm volatile("s_cmp_ge_i32 %1, 28\n\ts_cbranch_scc1 1f\n\ts_waitcnt vmcnt(2)\n\ts_branch 2f\n1:\n\ts_waitcnt vmcnt(7)\n2:" : "+v"(xr) : "s"(__builtin_amdgcn_readfirstlane(j)) : "memory", "scc");
    asm volatile("s_waitcnt lgkmcnt(0)" : "+s"(ep), "+s"(p_ld2), "+s"(p_dk2) :: "memory");
    ep += 1ull;
    const u32 p_ldt = ch ? p_ld2 >> 16 : p_ld2 & 0xffffu, p_dsk = ch ? p_dk2 >> 16 : p_dk2 & 0xffffu;
    if (lane >= 56) xr = v4u{0, 0, 0, 0};
    const u32 tag = (u32)ep, tagx = (u32)(ep >> 32) << 16;
    if (lane < 56) *lds<v4u>(XT + w * 912 + lane * 16) = xr;
    {
        const float v[8] = {bflo(xr.x), bfhi(xr.x), bflo(xr.y), bfhi(xr.y), bflo(xr.z), bfhi(xr.z), bflo(xr.w), bfhi(xr.w)};
        const float mean = wave_sum(((v[0] + v[1]) + (v[2] + v[3])) + ((v[4] + v[5]) + (v[6] + v[7]))) * (1.f / 448.f);
        float q = 0.f;
#pragma unroll
        for (int e = 0; e < 8; ++e) { const float dl = v[e] - mean; q += dl * dl; }
        q = lane < 56 ? q : 0.f;
        const u32 ln1 = ln_pack(mean, wave_sum(q) * (1.f / 448.f));
        if (lane == 0) __hip_atomic_store((GAS u64*)(ws + WS_SG) + (8 * b + w), ((u64)tag << 32) | ln1, RLX_AGENT);
    }
    const u32 km0 = FM + ch * FM_CH + (w & 3) * 256;
    f2 Ga, Gb, Gc, Gd, KX, KY, G4, G4r, E1, E1r, E256, E256r, E2, E2r, E4, E4r;
#define M1(a, bb) (f2{(a).x, (a).x} * (bb))
#define M2(a, br, t) __builtin_elementwise_fma(f2{(a).y, (a).y}, (br), (t))
#define WROW1(r, v) do { u32 keep_; asm volatile("s_mov_b32 %0, m0\n\ts_mov_b32 m0, %2\n\ts_nop 0\n\tds_write_addtid_b32 %1 offset:%3\n\ts_mov_b32 m0, %0" : "=&s"(keep_) : "v"(v), "s"(km0), "n"((r) * FM_ROW) : "memory"); } while (0)
#define WROW6(r0, v0, r1, v1, r2, v2, r3, v3, r4, v4, r5, v5) do { u32 keep_; asm volatile("s_mov_b32 %0, m0\n\ts_mov_b32 m0, %7\n\ts_nop 0\n\t" \
        "ds_write_addtid_b32 %1 offset:%8\n\tds_write_addtid_b32 %2 offset:%9\n\tds_write_addtid_b32 %3 offset:%10\n\tds_write_addtid_b32 %4 offset:%11\n\tds_write_addtid_b32 %5 offset:%12\n\tds_write_addtid_b32 %6 offset:%13\n\ts_mov_b32 m0, %0" \
        : "=&s"(keep_) : "v"(v0), "v"(v1), "v"(v2), "v"(v3), "v"(v4), "v"(v5), "s"(km0), "n"((r0) * FM_ROW), "n"((r1) * FM_ROW), "n"((r2) * FM_ROW), "n"((r3) * FM_ROW), "n"((r4) * FM_ROW), "n"((r5) * FM_ROW) : "memory"); } while (0)
#define WA(r, X) WROW1(r, pk2((X).x, -(X).y))
#define KSTEP(t) do { SB0(); \
        const f2 ta = M1(Ga, G4), tb = M1(Gb, G4), tc = M1(Gc, G4), td_ = M1(Gd, G4), tx = M1(KX, E1), ty = M1(KY, E256); \
        const u32 c0 = pk2(Ga.x, Ga.y), c1 = pk2(Gb.x, Gb.y), c2 = pk2(Gc.x, Gc.y), c3 = pk2(Gd.x, Gd.y), c4 = pk2(KX.x, -KX.y), c5 = pk2(KY.x, -KY.y); \
        f2 ts0, ts1; \
        if ((t) == 0) ts0 = M1(KY, E2); \
        if ((t) == 1 || (t) == 2) ts0 = M1(KY, E4); \
        SB0(); \
        WROW6(21 + 4 * (t), c0, 22 + 4 * (t), c1, 23 + 4 * (t), c2, 24 + 4 * (t), c3, (t), c4, ((t) == 0 ? 8 : (t) == 1 ? 12 : (t) == 2 ? 14 : 13 + (t)), c5); \
        if ((t) == 0) { f2 Z = M2(KY, E2r, ts0); WA(9, Z); ts1 = M1(Z, E2); Z = M2(Z, E2r, ts1); WA(10, Z); ts1 = M1(Z, E2); Z = M2(Z, E2r, ts1); WA(11, Z); } \
        if ((t) == 1) { const f2 V = M2(KY, E4r, ts0); WA(13, V); } \
        if ((t) == 2) { const f2 V = M2(KY, E4r, ts0); WA(15, V); } \
        if ((t) < 7) { Ga = M2(Ga, G4r, ta); Gb = M2(Gb, G4r, tb); Gc = M2(Gc, G4r, tc); Gd = M2(Gd, G4r, td_); KX = M2(KX, E1r, tx); KY = M2(KY, E256r, ty); } \
    } while (0)
    bar();
    auto publish_xg = [&]() {
        u32 x0[4], x1[4];
#pragma unroll
        for (int k = 0; k < 4; ++k) {
            const int id = min(tid + 512 * k, 1791), c = id >> 2, i2 = (id & 3) * 2;
            x0[k] = *lds<u16>(XT + i2 * 912 + c * 2); x1[k] = *lds<u16>(XT + (i2 + 1) * 912 + c * 2);
        }
        SB0();
#pragma unroll
        for (int k = 0; k < 4; ++k)
            __builtin_amdgcn_raw_buffer_store_b128(v4u{x0[k] | tagx, tag, x1[k] | tagx, tag}, rsW, (tid + 512 * k < 1792) ? (u32)(WS_XG + b * 28672 + (tid + 512 * k) * 16) : 0x7ffffff0u, 0, SC1);
    };
    if (mainb) {
    publish_xg();
    {
        vmw<5>();
        {
            const int bb_ = b < 196 ? b : b - 196;
    #pragma unroll
            for (int i = 0; i < 2; ++i) {
                const int id = 2 * bb_ + i, Sp = id / 28, s = id % 28;
                const u32 row = (w & 3) * 4 + (lane >> 4), chk = (lane & 15) ^ swz_b(row);
                glds16<false>((u32)((16 * s + row) * (F_ * 2) + Sp * 256 + chk * 16), (w >> 2) ? (const void*)wu : (const void*)wg, RT + i * 8192 + w * 1024);
            }
        }
        const u32 pa_ = PRM + w * 1024 + lane * 2;
        const u32 p_are = *lds<u16>(pa_), p_aim = *lds<u16>(pa_ + 128), p_bre = *lds<u16>(pa_ + 256), p_bim = *lds<u16>(pa_ + 384), p_cre = *lds<u16>(pa_ + 512), p_cim = *lds<u16>(pa_ + 640);
        const float dt = rbf(expf(bflo(p_ldt)));
        const float zr = dt * bflo(p_are), zi = dt * bflo(p_aim);
        const float ar = zr * 1.4426950408889634f, ai = zi * 0.15915494309189535f;
        const f2 CB = cmul(f2{bflo(p_cre), bflo(p_cim)}, f2{dt * bflo(p_bre), dt * bflo(p_bim)});
        E1 = cexp_rev(ar, ai, 1.f); E256 = cexp_rev(ar, ai, 256.f);
        const f2 G1 = cexp_rev(ar, ai, 8.f);
        E2 = cmul(E1, E1); E4 = cmul(E2, E2);
        const f2 G2 = cmul(G1, G1);
        G4 = cmul(G2, G2);
        E1r = f2{-E1.y, E1.x}; E2r = f2{-E2.y, E2.x}; E4r = f2{-E4.y, E4.x}; E256r = f2{-E256.y, E256.x}; G4r = f2{-G4.y, G4.x};
        Ga = f2{1.f, 0.f}; Gb = G1; Gc = G2; Gd = cmul(G2, G1);
        KX = CB; KY = cmul(CB, E256);
        SB0();
    }
    KSTEP(0); KSTEP(1); KSTEP(2);
    SB0();
    fence();
    {
        KSTEP(3); KSTEP(4); KSTEP(5); KSTEP(6); KSTEP(7);
        SB0();
#undef KSTEP
#undef WA
#undef WROW1
#undef WROW6
#undef M1
#undef M2
        vmw<0>();
        bar();
#pragma unroll
        for (int i = 0; i < 2; ++i) {
            const int id = 2 * b + i, Sp = id / 28, s = id % 28;
            const u32 base = RT + i * 8192 + trb * 4096 + 8 * (trp & 1);
            const v4u f = tr_frag(base + off_b(8 * trh + trq, 2 * w + (trp >> 1)), base + off_b(8 * trh + 4 + trq, 2 * w + (trp >> 1)));
            __builtin_amdgcn_raw_buffer_store_b128(f, rsW, (b < 196 ? (u32)(WS_WG + ((Sp * 8 + w) * 28 + s) * 1024) : 0x7ffffc00u) + lane * 16, 0, SC1);
        }
        const u32 xa = WS_XG + (32 * w + (lane >> 3)) * 28672 + cm * 128 + (lane & 7) * 16, sa = WS_SG + (32 * w + (lane >> 3)) * 64 + (lane & 3) * 16;
        v4u xg[4], sg[4];
        const int ta_ = (w >> 1) & 1, tb_ = w & 1;
        const u32 fa_ = opaque(FM + ch * FM_CH + min(16 * ta_ + (lane & 15), 20) * FM_ROW + (lane >> 4) * 16), fb_ = opaque(FM + ch * FM_CH + (21 + 16 * tb_ + (lane & 15)) * FM_ROW + (lane >> 4) * 16);
        f32x4 f0 = {0.f, 0.f, 0.f, 0.f}, f1 = {0.f, 0.f, 0.f, 0.f};
        v4u A[16], B[16];
#pragma unroll
        for (int ks = 0; ks < 16; ++ks) { A[ks] = *lds<v4u>(fa_ + ks * 64); B[ks] = *lds<v4u>(fb_ + ks * 64); }
        SB0();
#pragma unroll
        for (int k = 0; k < 4; ++k) { xg[k] = __builtin_amdgcn_raw_buffer_load_b128(rsW, xa + k * 8 * 28672, 0, SC1); sg[k] = __builtin_amdgcn_raw_buffer_load_b128(rsW, sa + k * 512, 0, SC1); }
        SB0();
#pragma unroll
        for (int ks = 0; ks < 16; ks += 2) { f0 = mfma16(A[ks], B[ks], f0); f1 = mfma16(A[ks + 1], B[ks + 1], f1); }
        f0 += f1;
#pragma unroll
        for (int r = 0; r < 4; ++r) { const int row = 16 * ta_ + 4 * (lane >> 4) + r; if (row < 24) *lds<float>(FT + ch * FT_CH + (row * 32 + 16 * tb_ + (lane & 15)) * 4) = f0[r]; }
        if ((tid & 255) == 255) *lds<float>(FT + ch * FT_CH + 768 * 4) = 0.f;
        for (u32 spin = 0;; ++spin) {
            u32 bad = 0;
#pragma unroll
            for (int k = 0; k < 4; ++k) bad |= ((sg[k].y ^ tag) | (sg[k].w ^ tag) | (xg[k].y ^ tag)) | ((xg[k].w ^ tag) | (((xg[k].x ^ tagx) | (xg[k].z ^ tagx)) & 0xffff0000u));
            if (__all(bad == 0) || spin > SPIN_CAP) break;
            __builtin_amdgcn_s_sleep(4);
#pragma unroll
            for (int k = 0; k < 4; ++k) { xg[k] = __builtin_amdgcn_raw_buffer_load_b128(rsW, xa + k * 8 * 28672, 0, SC1); sg[k] = __builtin_amdgcn_raw_buffer_load_b128(rsW, sa + k * 512, 0, SC1); }
        }
        const u32 ua = opaque(UU + ((lane >> 2) & 1) * UU_CH + 512 + (32 * w + (lane >> 3)) * 16 + (lane & 3) * 4), xsa = opaque(XS + ((lane >> 2) & 1) * 4096 + (32 * w + (lane >> 3)) * 16 + (lane & 3) * 4);
#pragma unroll
        for (int k = 0; k < 4; ++k) {
            const u32 xm = pk2(bflo(xg[k].x) - bflo(sg[k].x), bflo(xg[k].z) - bflo(sg[k].z));
            const float u0 = bflo(xm) * __builtin_amdgcn_rcpf(bfhi(sg[k].x)), u1 = bfhi(xm) * __builtin_amdgcn_rcpf(bfhi(sg[k].z));
            *lds<u32>(ua + k * 128) = pk2(u0, u1);
            *lds<u32>(xsa + k * 128) = __builtin_amdgcn_perm(xg[k].z, xg[k].x, 0x05040100u);
        }
    }
    if (tid >= 64 && tid < 128) *lds<v4u>(UU + ((tid >> 5) & 1) * UU_CH + (tid & 31) * 16) = v4u{0, 0, 0, 0};
    const u32 eb_ = opaque(EOL + (tid & 255) * 24);
    const v2u ea = *lds<v2u>(eb_), eb = *lds<v2u>(eb_ + 8), ec = *lds<v2u>(eb_ + 16);
    vmw<0>();
    bar();
    if (tid == 0 && b < 196) __hip_atomic_store((GAS u32*)(ws + WS_WR + b * 64), tag, RLX_AGENT);
    const int rq = w & 3;
    const u32 cb = lane & 15, cq = lane >> 4;
    const u32 ubase = opaque(UU + ch * UU_CH + 512 + 32 * (cb - (cq >> 1)) + 16 * (cq & 1) - 32 * 4 * rq - 32 * 2);
    auto ldB = [&](int k, int rr) { return *lds<v4u>(ubase + 32 * (16 * k - 2 * rr + 2)); };
    v4u Bf[8], Bn[8];
#pragma unroll
    for (int p = 0; p < 4; ++p) { Bf[p] = ldB(p, 0); Bn[p] = ldB(p, 1); }
    {
        const u32 fb_ = FT + ch * FT_CH;
        const u32 eo[11] = {ea.x & 0xffffu, ea.x >> 16, ea.y & 0xffffu, ea.y >> 16, eb.x & 0xffffu, eb.x >> 16, eb.y & 0xffffu, eb.y >> 16, ec.x & 0xffffu, ec.x >> 16, ec.y & 0xffffu};
        float v[11];
#pragma unroll
        for (int e = 0; e < 11; ++e) v[e] = *lds<float>(fb_ + eo[e]);
        SB0();
        u32 p[10];
#pragma unroll
        for (int e = 0; e < 10; ++e) p[e] = pk2(v[e], v[e + 1]);
        const u32 ka_ = opaque(KC + ch * KC_CH + (tid & 255) * 16);
#pragma unroll
        for (int s = 0; s < 4; ++s) *lds<v4u>(ka_ + s * KC_COPY) = v4u{p[s], p[s + 2], p[s + 4], p[s + 6]};
    }
    if (tid < 16) *lds<v4u>(KC + (tid >> 3) * KC_CH + ((tid >> 1) & 3) * KC_COPY + 4096 + (tid & 1) * 16) = v4u{0, 0, 0, 0};
    bar();
    v4u Wf[28];
    const u32 l16 = lane * 16;
    const int wf_off = WS_WG + (S * 8 + w) * 28 * 1024;
    u32 f2a;
    const float dsv = bflo(p_dsk);
    const u32 f2_off = WS_F2 + g * 16384 + (tid < 224 ? tid : 0) * 64;
    const u32 bt_src = (8 * w + ((lane >> 2) & 7)) * 4096 + 128 * P + (4 * (lane >> 5) + ((lane & 3) ^ ((2 * w + ((lane >> 4) & 1)) & 3))) * 16;
    auto bt_piece = [&](int part, int k) { glds16<true>(bt_src + part * 2048 + k * 64 * 4096, ws + WS_HT, (part ? BTB : BTA) + (8 * k + w) * 1024); };
    {
        const u32 cs = (3u - cb) & 3u;
        const u32 kbase = opaque(KC + ch * KC_CH + cs * KC_COPY + 2 * (2047 - 16 * (cq >> 1) - cb + 8 * (cq & 1) - cs) - 32 * 4 * rq - 32 * 114);
        const u32 kbase8 = opaque(kbase + 8);
        auto ldA = [&](int p, int rr) { const v2u lo = *lds<volatile v2u>(kbase + 32 * (114 - (16 * p + 2 * rr))), hi = *lds<volatile v2u>(kbase8 + 32 * (114 - (16 * p + 2 * rr))); return v4u{lo.x, lo.y, hi.x, hi.y}; };
        const u32 cxw = opaque(CX + (4 * ch * 4 + rq) * 1024 + lane * 16), cxr = opaque(CX + w * 4096 + lane * 16);
        const u32 fu_ = opaque(UU + ch * UU_CH + 512 + (256 * rq + 16 * cb + 4 * cq) * 2), fx_ = opaque(XS + ch * 4096 + (256 * rq + 16 * cb + 4 * cq) * 2);
        const u32 ht_ = WS_HT + dch * 4096 + (256 * rq + 16 * cb + 4 * cq) * 2;
        auto finish = [&](f32x4 (&acc)[4], int tbase) {
#pragma unroll
            for (int i = 0; i < 4; ++i) *lds<f32x4>(cxw + i * 4096) = acc[i];
            const v2u uu = *lds<v2u>(fu_ + tbase * 512), xx = *lds<v2u>(fx_ + tbase * 512);
            bar();
            if (tbase == 4) f2a = __builtin_amdgcn_raw_buffer_load_b32(rsW, w < 4 ? (tid < 224 ? f2_off : (u32)(WS_WF + (tid - 224) * 64)) : f2_off, 0, SC1);
            const f32x4 p0 = *lds<f32x4>(cxr), p1 = *lds<f32x4>(cxr + 1024), p2 = *lds<f32x4>(cxr + 2048), p3 = *lds<f32x4>(cxr + 3072);
            const float uv[4] = {bflo(uu.x), bfhi(uu.x), bflo(uu.y), bfhi(uu.y)}, xv[4] = {bflo(xx.x), bfhi(xx.x), bflo(xx.y), bfhi(xx.y)};
            const u32 k01 = pk2(uv[0] * dsv, uv[1] * dsv), k23 = pk2(uv[2] * dsv, uv[3] * dsv);
            SB0();
            const f32x4 y = (p0 + p1) + (p2 + p3);
            const u32 y01 = pk2(y[0], y[1]), y23 = pk2(y[2], y[3]);
            const u32 z01 = pk2(bflo(y01) + bflo(k01), bfhi(y01) + bfhi(k01)), z23 = pk2(bflo(y23) + bflo(k23), bfhi(y23) + bfhi(k23));
            const float h[4] = {xv[0] + bflo(z01), xv[1] + bfhi(z01), xv[2] + bflo(z23), xv[3] + bfhi(z23)};
            __builtin_amdgcn_raw_buffer_store_b64(v2u{pk2(h[0], h[1]), pk2(h[2], h[3])}, rsW, ht_ + tbase * 512, 0, SC1);
        };
        f32x4 acc[4];
        v4u Af[8], An[8];
#pragma unroll
        for (int i = 0; i < 4; ++i) acc[i] = f32x4{0.f, 0.f, 0.f, 0.f};
#pragma unroll
        for (int p = 0; p < 4; ++p) { Af[p] = ldA(p, 0); An[p] = ldA(p, 1); }
        SB0();
#pragma unroll
        for (int p = 0; p < 4; ++p)
#pragma unroll
            for (int i = p; i < 4; ++i) acc[i] = mfma16(Af[p], Bf[i - p], acc[i]);
#pragma unroll
        for (int p = 0; p < 4; ++p)
#pragma unroll
            for (int i = p; i < 4; ++i) acc[i] = mfma16(An[p], Bn[i - p], acc[i]);
#pragma unroll
        for (int p = 4; p < 8; ++p) { Af[p] = ldA(p, 0); Bf[p] = ldB(p, 0); }
        SB0();
        finish(acc, 0);
#pragma unroll
        for (int i = 0; i < 4; ++i) acc[i] = f32x4{0.f, 0.f, 0.f, 0.f};
        SB0();
#pragma unroll
        for (int p = 4; p < 8; ++p) { An[p] = ldA(p, 1); Bn[p] = ldB(p, 1); }
        const u32 wr_off = WS_WR + (tid < 196 ? tid : 0) * 64;
        const u32 wr_flag = __builtin_amdgcn_raw_buffer_load_b32(rsW, wr_off, 0, SC1);
#pragma unroll
        for (int p = 0; p < 8; ++p)
#pragma unroll
            for (int i = 0; i < 4; ++i) if (p <= 4 + i) acc[i] = mfma16(Af[p], Bf[4 + i - p], acc[i]);
        SGB(M_VMEM, 1);
#pragma unroll
        for (int i = 0; i < 6; ++i) { SGB(M_MFMA, 2); SGB(M_DSR, 2); SGB(M_MFMA, 2); }
        SB0();
        if (!__all(wr_flag == tag)) {
            for (u32 spin = 0; spin < SPIN_CAP; ++spin) {
                if (__all(__builtin_amdgcn_raw_buffer_load_b32(rsW, wr_off, 0, SC1) == tag)) break;
                __builtin_amdgcn_s_sleep(4);
            }
        }
        vmw<0>();
        bar();
        if (tid < 8) __hip_atomic_store((GAS u32*)(ws + WS_F2 + tid * 16384 + cm * 64), tag, RLX_AGENT);
        SB0();
#pragma unroll
        for (int i = 0; i < 7; ++i) Wf[i] = __builtin_amdgcn_raw_buffer_load_b128(rsW, l16 + (i) * 1024 % 4096, wf_off + (i) * 1024 / 4096 * 4096, SC1);
#pragma unroll
        for (int p = 0; p < 8; ++p)
#pragma unroll
            for (int i = 0; i < 4; ++i) if (p <= 4 + i) acc[i] = mfma16(An[p], Bn[4 + i - p], acc[i]);
#pragma unroll
        for (int i = 0; i < 6; ++i) { SGB(M_MFMA, 4); SGB(M_VMEM, 1); }
        SGB(M_MFMA, 2); SGB(M_VMEM, 1);
        SB0();
#pragma unroll
        for (int i = 7; i < 14; ++i) Wf[i] = __builtin_amdgcn_raw_buffer_load_b128(rsW, l16 + i * 1024 % 4096, wf_off + i * 1024 / 4096 * 4096, SC1);
        finish(acc, 4);
    }
    auto gate_spin = [&](u32 off, bool active) {
        for (u32 spin = 0;; ++spin) {
            const u32 s0 = __builtin_amdgcn_raw_buffer_load_b32(rsW, off, 0, SC1);
            if (__all(s0 == tag || !active) || spin > SPIN_CAP) break;
            __builtin_amdgcn_s_sleep(8);
        }
    };
    if (w < 4 && !__all(f2a == tag)) gate_spin(tid < 224 ? f2_off : (u32)(WS_WF + (tid - 224) * 64), true);
    bar();
    if (b == 0 && tid == 0) __hip_atomic_store((GAS u64*)(ws + WS_EP), ep, RLX_AGENT);
    const float csum = __uint_as_float(__builtin_amdgcn_raw_buffer_load_b32(rsW, WS_CS + (((lane >> 4) & 1) * 1792 + 128 * S + 16 * w + (lane & 15)) * 4, 0, SC1));
    fence();
#pragma unroll
    for (int k = 0; k < 7; ++k) bt_piece(0, k);
    vmw<4>();
    bar();
    if (tid < 8) __hip_atomic_store((GAS u32*)(ws + WS_F2 + (8 + tid) * 16384 + cm * 64), tag, RLX_AGENT);
    v4u Wd[28];
    const int td = w & 1, kq = w >> 1, hp = S / 7, dtile = S % 7;
    const int wd_off = WS_WD + ((dtile * 2 + td) * 112 + 28 * kq) * 1024;
    const u32 trb0 = 1024 * trh + 64 * trq + 16 * ((2 * trb + (trp >> 1)) ^ ((2 * trh) & 3)) + 8 * (trp & 1), trb1 = 1024 * trh + 64 * (4 + trq) + 16 * ((2 * trb + (trp >> 1)) ^ ((2 * trh + 1) & 3)) + 8 * (trp & 1);
    const int hh_out = WS_HH + (P * 112 + 8 * S + w) * 4096;
    u32 hres[2][2]; float rs[2];
    const u32 f3_off = WS_F3 + (P * 16 + (lane < 14 ? lane : 0)) * 64;
    const char* hsrc = ws + WS_HH + (P * 112 + 28 * kq + td) * 4096 + hp * 1024; const u32 hdst = kq * 1024;
    auto hh_dma = [&](int u) { glds16<true>(l16, hsrc + (u % 28) * 4096 + (u >= 28 ? 2048 : 0), (td ? hh_slot(u + 1) : hh_slot(u)) + hdst); };
#define GEMM1_PASS(PART) do { \
        const u32 b0 = opaque(((PART) ? BTB : BTA) + trb0), b1 = opaque(((PART) ? BTB : BTA) + trb1); \
        const u32 s0_ = opaque(b0 + (w >> 1) * 2048 + (w & 1) * 512), s1_ = opaque(b1 + (w >> 1) * 2048 + (w & 1) * 512); \
        f32x16 acc[2]; \
        _Pragma("unroll") for (int i = 0; i < 16; ++i) { acc[0][i] = 0.f; acc[1][i] = 0.f; } \
        v4u Bq[3][2], Of; \
        float sa0 = 0.f, sa1 = 0.f, sq0 = 0.f, sq1 = 0.f; \
        u32 gatev = 0; \
        _Pragma("unroll") for (int t = 0; t < 2; ++t) { Bq[0][t] = tr_frag(b0 + 512 * t, b1 + 512 * t); Bq[1][t] = tr_frag(b0 + 2048 + 512 * t, b1 + 2048 + 512 * t); } \
        _Pragma("unroll") for (int s = 0; s < 28; ++s) { \
            SB0(); \
            if (!(PART)) { \
                if (s == 10) { vmw<10>(); bar(); }        \
                if (s == 19) {     \
                    if (w < 4) { if (!__all(gatev == tag || tid >= 224)) gate_spin(f2_off + 8 * 16384, tid < 224); } \
                    bar(); \
                } \
            } else { \
                if (s == 3) { vmw<2>(); bar(); if (tid == 0) __hip_atomic_store((GAS u32*)(ws + WS_F3 + (P * 16 + S) * 64), tag, RLX_AGENT); }     \
                if (s == 20) {     \
                    if (w == 0) { if (!__all(gatev == tag)) gate_spin(f3_off, true); } \
                    bar(); \
                } \
            } \
            SB0(); \
            if (s + 2 < 28) { _Pragma("unroll") for (int t = 0; t < 2; ++t) Bq[(s + 2) % 3][t] = tr_frag(b0 + (s + 2) * 2048 + 512 * t, b1 + (s + 2) * 2048 + 512 * t); } \
            if ((s & 3) == 0) Of = tr_frag(s0_ + s * 2048, s1_ + s * 2048);        \
            if (!(PART)) { \
                if (s < 14) { Wf[14 + s] = __builtin_amdgcn_raw_buffer_load_b128(rsW, l16 + ((14 + s)) * 1024 % 4096, wf_off + ((14 + s)) * 1024 / 4096 * 4096, SC1); } \
                else if (s == 14) { if (w < 4) gatev = __builtin_amdgcn_raw_buffer_load_b32(rsW, f2_off + 8 * 16384, 0, SC1); } \
                else if (s < 19) { Wd[s - 15] = __builtin_amdgcn_raw_buffer_load_b128(rsW, l16 + ((s - 15)) * 1024 % 4096, wd_off + ((s - 15)) * 1024 / 4096 * 4096, SC1); } \
                else if (s < 26) bt_piece(1, s - 19); \
            } else { \
                if (s >= 1 && s < 25) Wd[3 + s] = __builtin_amdgcn_raw_buffer_load_b128(rsW, l16 + (3 + s) * 1024 % 4096, wd_off + (3 + s) * 1024 / 4096 * 4096, SC1); \
                if (s == 15) { if (w == 0) gatev = __builtin_amdgcn_raw_buffer_load_b32(rsW, f3_off, 0, SC1); } \
                if (s >= 20 && s < 27) { fence(); hh_dma(2 * (s - 20)); } \
                if (s >= 25) { fence(); hh_dma(14 + 2 * (s - 25)); }        \
            } \
            _Pragma("unroll") for (int t = 0; t < 2; ++t) acc[t] = mfma32(Wf[s], Bq[s % 3][t], acc[t]); \
            if ((s & 3) == 2) { \
                _Pragma("unroll") for (int e = 0; e < 4; e += 2) { \
                    const float l0 = bflo(Of[e]), h0 = bfhi(Of[e]), l1 = bflo(Of[e + 1]), h1 = bfhi(Of[e + 1]); \
                    sa0 += l0; sq0 = __builtin_fmaf(l0, l0, sq0); sa1 += l1; sq1 = __builtin_fmaf(l1, l1, sq1); \
                    sa0 += h0; sq0 = __builtin_fmaf(h0, h0, sq0); sa1 += h1; sq1 = __builtin_fmaf(h1, h1, sq1); \
                } \
                asm volatile("" : "+v"(sa0), "+v"(sa1), "+v"(sq0), "+v"(sq1));        \
                if (EXPG == 0) { SGB(M_MFMA, 1); SGB(M_DSR, 2); SGB(M_VALU, 12); SGB(M_VMEM, 1); SGB(M_MFMA, 1); SGB(M_DSR, 2); SGB(M_VALU, 12); } \
                if (EXPG == 1) { SGB(M_VMEM, 1); SGB(M_MFMA, 1); SGB(M_DSR, 4); SGB(M_VALU, 6); SGB(M_MFMA, 1); SGB(M_VALU, 18); } \
                if (EXPG == 2) { SGB(M_MFMA, 2); SGB(M_DSR, 4); SGB(M_VMEM, 1); SGB(M_VALU, 24); } \
            } else { \
                if (EXPG == 0) { SGB(M_MFMA, 1); SGB(M_DSR, 3); SGB(M_VMEM, 1); SGB(M_MFMA, 1); SGB(M_DSR, 3); } \
                if (EXPG == 1) { SGB(M_VMEM, 1); SGB(M_MFMA, 1); SGB(M_DSR, 6); SGB(M_MFMA, 1); } \
                if (EXPG == 2) { SGB(M_MFMA, 2); SGB(M_DSR, 6); SGB(M_VMEM, 1); } \
            } \
        } \
        SB0(); \
        { \
            const float sh = half_sum(sa0 + sa1), sq = half_sum(sq0 + sq1); \
            const u32 wa_ = opaque(SPQ + (PART) * 2048 + (w & 1) * 1024 + (w >> 1) * 4 + lane * 32); \
            if (lane < 32) { *lds<float>(wa_) = sh; *lds<float>(wa_ + 16) = sq; if (!(PART)) *lds<float>(CSL + w * 128 + lane * 4) = csum; } \
        } \
        bar(); \
        {     \
            const u32 ca_ = opaque(CSL + w * 128 + trh * 16), sa_ = opaque(SPQ + (PART) * 2048 + (lane & 31) * 32); \
            const f32x4 csg[2] = {*lds<f32x4>(ca_), *lds<f32x4>(ca_ + 32)}, csu[2] = {*lds<f32x4>(ca_ + 64), *lds<f32x4>(ca_ + 96)}; \
            const f32x4 sm[2] = {*lds<f32x4>(sa_), *lds<f32x4>(sa_ + 1024)}; \
            const f32x4 sq4 = *lds<f32x4>(sa_ + hp * 1024 + 16); \
            const u32 lc_ = 4 * hp + ((lane & 31) >> 3);        \
            const u32 h0_ = opaque(((PART) ? BTB : BTA) + (8 * dtile + 4 * td + kq) * 1024 + (lc_ >> 2) * 512 + trh * 256 + 16 * ((lc_ & 3) ^ ((2 * kq + trh) & 3)) + (lane & 7) * 2); \
            u32 hb_[4]; \
            _Pragma("unroll") for (int e = 0; e < 4; ++e) hb_[e] = *lds<u16>(h0_ + 64 * e); \
            SB0(); \
            _Pragma("unroll") for (int t = 0; t < 2; ++t) { \
                const float m = rbf(((sm[t][0] + sm[t][1]) + (sm[t][2] + sm[t][3])) * (1.f / 448.f)); \
                float hv[8]; \
                _Pragma("unroll") for (int e = 0; e < 8; ++e) hv[e] = (acc[t][e] - m * csg[e >> 2][e & 3]) * (acc[t][e + 8] - m * csu[e >> 2][e & 3]); \
                __builtin_amdgcn_raw_buffer_store_b128(v4u{pk2(hv[0], hv[1]), pk2(hv[2], hv[3]), pk2(hv[4], hv[5]), pk2(hv[6], hv[7])}, rsW, l16 + hh_out + (2 * (PART) + t) * 1024, 0, SC1); \
            } \
            { \
                const f32x4 s4 = hp ? sm[1] : sm[0]; \
                const float mean = ((s4[0] + s4[1]) + (s4[2] + s4[3])) * (1.f / 448.f), var = fmaxf(((sq4[0] + sq4[1]) + (sq4[2] + sq4[3])) * (1.f / 448.f) - mean * mean, 0.f); \
                const float sd = bfhi(ln_pack(mean, var)); \
                rs[PART] = __builtin_amdgcn_rcpf(sd * sd); \
                hres[PART][0] = hb_[0] | (hb_[1] << 16); hres[PART][1] = hb_[2] | (hb_[3] << 16); \
            } \
        } \
    } while (0)
    GEMM1_PASS(0);
    vmw<2>();
    bar();
    GEMM1_PASS(1);
#undef GEMM1_PASS
    vmw<2>();
    bar();
    f32x16 o2[2];
#pragma unroll
    for (int i = 0; i < 16; ++i) { o2[0][i] = 0.f; o2[1][i] = 0.f; }
    const u32 hrdA = opaque(BTA + kq * 1024 + lane * 16), hrdS = opaque(RSP + kq * 1024 + lane * 16), hrdB = opaque(BTB + kq * 1024 + lane * 16);
    auto hh_rd = [&](int u) {
        const u32 sl = hh_slot(u);
        return *lds<v4u>(sl >= RSP ? hrdS + (sl - RSP) : sl >= BTA ? hrdA + (sl - BTA) : hrdB + (sl - BTB));
    };
    const u32 op = (64 * P + 32 * hp + (lane & 31)) * (D_ * 2) + (64 * dtile + 32 * td + 8 * kq + 4 * trh) * 2;
    const u32 red_rd = opaque(td * 12288 + kq * 3072 + lane * 16);
    auto red_give = [&](const f32x16& a, int g4, int part) {
        const u32 sl = g4 == kq ? 24576u : (u32)(td * 12288 + g4 * 3072 + (kq > g4 ? kq - 1 : kq) * 1024);
        *lds<f32x4>(sl + l16 + part * 32768) = f32x4{a[4 * g4], a[4 * g4 + 1], a[4 * g4 + 2], a[4 * g4 + 3]};
    };
    auto red_take = [&](const f32x16& a, int part) {
        const f32x4 p0 = *lds<f32x4>(red_rd + part * 32768), p1 = *lds<f32x4>(red_rd + part * 32768 + 1024), p2 = *lds<f32x4>(red_rd + part * 32768 + 2048);
        f32x4 own;
#pragma unroll
        for (int e = 0; e < 4; ++e) own[e] = kq == 0 ? a[e] : kq == 1 ? a[4 + e] : kq == 2 ? a[8 + e] : a[12 + e];
        const f32x4 v = ((own + p0) + (p1 + p2)) * rs[part];
        const u32 f01 = pk2(v[0], v[1]), f23 = pk2(v[2], v[3]);
        __builtin_amdgcn_raw_buffer_store_b64(v2u{pk2(bflo(hres[part][0]) + bflo(f01), bfhi(hres[part][0]) + bfhi(f01)), pk2(bflo(hres[part][1]) + bflo(f23), bfhi(hres[part][1]) + bfhi(f23))}, rsO, op + part * 1024 * D_ * 2, 0, 0);
    };
    {
        v4u hf[4];
        u32 gatev = 0;
#pragma unroll
        for (int u = 0; u < 3; ++u) hf[u] = hh_rd(u);
#pragma unroll
        for (int u = 0; u < 28; ++u) {
            SB0();
            if (u < 4) hh_dma(20 + 2 * u);
            if (u == 6) { vmw<4>(); bar(); if (tid == 0) __hip_atomic_store((GAS u32*)(ws + WS_F3 + 16384 + (P * 16 + S) * 64), tag, RLX_AGENT); }
            if (u == 17) { vmw<0>(); bar(); }
            SB0();
            if (u + 3 < 28) hf[(u + 3) & 3] = hh_rd(u + 3);
            if (u == 22 && w == 0) gatev = __builtin_amdgcn_raw_buffer_load_b32(rsW, f3_off + 16384, 0, SC1);
            o2[0] = mfma32(Wd[u], hf[u & 3], o2[0]);
        }
        SB0();
        if (w == 0) { if (!__all(gatev == tag)) gate_spin(f3_off + 16384, true); }
        bar();
#pragma unroll
        for (int u = 28; u < 40; u += 2) hh_dma(u);
        vmw<3>(); bar();
#pragma unroll
        for (int u = 0; u < 3; ++u) hf[u] = hh_rd(28 + u);
#pragma unroll
        for (int u = 0; u < 28; ++u) {
            SB0();
            if (u == 3) { vmw<3>(); bar(); }
            if (u == 9) { vmw<5>(); bar(); }
            if (u == 15) { vmw<2>(); bar(); }
            if (u == 21) { vmw<0>(); bar(); }
            if (u < 8) hh_dma(40 + 2 * u);
            SB0();
            if (u + 3 < 28) hf[(u + 3) & 3] = hh_rd(28 + u + 3);
            o2[1] = mfma32(Wd[u], hf[u & 3], o2[1]);
        }
        SB0();
        bar();
    }
#pragma unroll
    for (int g4 = 0; g4 < 4; ++g4) { red_give(o2[0], g4, 0); red_give(o2[1], g4, 1); }
    bar();
    red_take(o2[0], 0);
    red_take(o2[1], 1);
    } else {
        publish_xg();
        const int hb = (j - 28) * 8 + g;
        float cs[8];
        {
            const u16* wsrc = hb < 16 ? wg : wu;
            const int c0 = 112 * (hb & 15) + 8 * (tid % 14), r0 = tid / 14;
#pragma unroll
            for (int e = 0; e < 8; ++e) cs[e] = 0.f;
            v4u t[13];
#pragma unroll
            for (int i = 0; i < 13; ++i) { const int r = r0 + 36 * i; t[i] = (tid < 504 && r < 448) ? *(const v4u*)(wsrc + (size_t)r * F_ + c0) : v4u{0, 0, 0, 0}; }
#pragma unroll
            for (int i = 0; i < 13; ++i) { cs[0] += bflo(t[i].x); cs[1] += bfhi(t[i].x); cs[2] += bflo(t[i].y); cs[3] += bfhi(t[i].y); cs[4] += bflo(t[i].z); cs[5] += bfhi(t[i].z); cs[6] += bflo(t[i].w); cs[7] += bfhi(t[i].w); }
        }
        vmw<0>(); bar();
#pragma unroll
        for (int k = 0; k < 7; ++k) {
            const int gid = hb + 32 * k;
            if (gid < 196) {
                const int dt_ = gid / 28, fg = gid % 28, fsl = w >> 1, td = w & 1;
                u32 ad[2];
#pragma unroll
                for (int t = 0; t < 2; ++t) { const u32 row = 16 * fsl + 8 * t + 4 * trh + trq, chk = 4 * td + 2 * trb + (trp >> 1); ad[t] = k * 8192 + row * 128 + 16 * (chk ^ (((row >> 1) & 1) << 2)) + 8 * (trp & 1); }
                const v4u f = tr_frag(ad[0], ad[1]);
                __builtin_amdgcn_raw_buffer_store_b128(f, rsW, WS_WD + ((dt_ * 2 + td) * 112 + 4 * fg + fsl) * 1024 + lane * 16, 0, SC1);
            }
        }
        if (tid < 504) {
            *lds<f32x4>(65536 + tid * 32) = f32x4{cs[0], cs[1], cs[2], cs[3]};
            *lds<f32x4>(65536 + tid * 32 + 16) = f32x4{cs[4], cs[5], cs[6], cs[7]};
        }
        bar();
        if (tid < 112) {
            float s = 0.f;
            for (int r = 0; r < 36; ++r) s += *lds<float>(65536 + (r * 14 + (tid >> 3)) * 32 + (tid & 7) * 4);
            __hip_atomic_store((GAS u32*)(ws + WS_CS) + ((hb < 16 ? 0 : 1792) + 112 * (hb & 15) + tid), __float_as_uint(s), RLX_AGENT);
        }
        vmw<0>(); bar();
        if (tid == 0) __hip_atomic_store((GAS u32*)(ws + WS_WF + hb * 64), tag, RLX_AGENT);

    }
}

extern "C" void kernel_launch(void* const* d_in, const int* in_sizes, int n_in, void* d_out, int out_size, void* d_ws, size_t ws_size, hipStream_t stream) {
    (void)in_sizes; (void)n_in; (void)out_size; (void)ws_size;
    (void)hipFuncSetAttribute((const void*)fused_kernel, hipFuncAttributeMaxDynamicSharedMemorySize, LDS_BYTES);
    fused_kernel<<<256, 512, LDS_BYTES, stream>>>((const u16*)d_in[0], (const u16*)d_in[1], (const u16*)d_in[2], (const u16*)d_in[3], (const u16*)d_in[4],
        (const u16*)d_in[5], (const u16*)d_in[6], (const u16*)d_in[7], (const u16*)d_in[8], (const u16*)d_in[10], (const u16*)d_in[11], (const u16*)d_in[12],
        (u16*)d_out, (char*)d_ws);
}
```

```cpp
#include <hip/hip_runtime.h>
#include <stdint.h>

typedef unsigned short u16;
typedef unsigned u32;
typedef unsigned long long u64;
typedef u32 v4u __attribute__((ext_vector_type(4)));
typedef u32 v2u __attribute__((ext_vector_type(2)));
typedef float f32x4 __attribute__((ext_vector_type(4)));
typedef float f32x16 __attribute__((ext_vector_type(16)));
typedef float f2 __attribute__((ext_vector_type(2)));
typedef __bf16 bf16x8 __attribute__((ext_vector_type(8)));
typedef __bf16 bf16x2 __attribute__((ext_vector_type(2)));
typedef short s16x4 __attribute__((ext_vector_type(4)));
#define LAS __attribute__((address_space(3)))
#define GAS __attribute__((address_space(1)))
#define DI __device__ __forceinline__
#define RLX_AGENT __ATOMIC_RELAXED, __HIP_MEMORY_SCOPE_AGENT
#define CODEPF 26
#define SPINAT 99
#define SPINX 0
#define SC1 16
#define OUTAUX 0
#define EXPP 0
#define EXPM 0
#define EXPW 0
#define GSLEEP 8
#define EXPG 0
#define EXPS 0
#define BTSC1 true
#define DLY1 0
#define DLY2 35
#define DLY3 35
#define WAUX 16

constexpr int L_ = 2048, D_ = 448, N_ = 256, F_ = 1792;
constexpr u32 WS_EP = 0;
constexpr u32 WS_SG = 4096;
constexpr u32 WS_WF = 32768;
constexpr u32 WS_CS = 40960;
constexpr u32 WS_HT = 65536;
constexpr u32 WS_WR = 1966080;
constexpr u32 WS_HH = 2u << 20;
constexpr u32 WS_WG = 10u << 20;
constexpr u32 WS_WD = 14u << 20;
constexpr u32 WS_XG = 16u << 20;
constexpr u32 WS_F2 = (23u << 20) + (512u << 10);
constexpr u32 WS_F3 = WS_F2 + 2 * 8 * 16384;
constexpr u32 WS_END = 24u << 20;
constexpr u32 WS_DBG = 24u << 20;
constexpr u32 SPIN_CAP = 1u << 16;

constexpr u32 LDS_BYTES = 163840;
constexpr u32 FM = 0, FM_ROW = 1056, FM_CH = 53 * FM_ROW;
constexpr u32 FT = 2 * FM_CH, FT_CH = 772 * 4;
constexpr u32 EOL = FT + 2 * FT_CH;
constexpr u32 RT = 124288;
constexpr u32 PRM = RT + 16384;
constexpr u32 XT = PRM + 8192;
constexpr u32 KC = 0, KC_COPY = 4160, KC_CH = 4 * KC_COPY;
constexpr u32 CX = 2 * KC_CH;
constexpr u32 UU = RT, UU_CH = (256 + 2048) * 2;
constexpr u32 XS = UU + 2 * UU_CH;
static_assert(EOL % 16 == 0 && EOL + 6144 <= RT && XS + 8192 <= XT && XT + 8 * 912 <= 156672, "LDS map");
constexpr u32 BT_PART = 448 * 128;
constexpr u32 BTA = CX + 32768, BTB = 0;
constexpr u32 RSP = BTA + BT_PART;
constexpr u32 SM = 155648;
constexpr u32 SPQ = SM;
constexpr u32 CSL = SPQ + 4096;
static_assert(BTA + BT_PART <= RT && RSP + 7 * 4096 <= SM && CSL + 1024 <= 161280, "LDS map");
constexpr u32 hh_slot(int u) {
    return u < 14 ? BTA + u * 4096 : u < 21 ? RSP + (u - 14) * 4096 : u < 35 ? BTB + (u - 21) * 4096 : u < 49 ? BTA + (u - 35) * 4096 : RSP + (u - 49) * 4096;
}

struct EoTab { u16 v[256 * 12]; };
constexpr u32 foff_c(int j) {
    if (j < 0) return 768u;
    u32 t = (u32)j;
    int s = t >= 1024 ? 3 : t >= 512 ? 2 : t >= 256 ? 1 : 0;
    if (s > 0) t = ((t + (1u << (s - 1)) - 1u + ((t >> s) & 1u)) >> s) << s;
    u32 col = (t >> 3) & 31u, row = 0;
    if (t < 256u) row = t & 7u;
    else if (t < 512u) row = 8u + ((t >> 1) & 3u);
    else if (t < 1024u) row = 12u + 2u * ((t >> 8) & 1u) + ((t >> 2) & 1u);
    else if (t < 2048u) row = 16u + ((t >> 8) & 3u);
    else row = 20u;
    return row * 32u + col;
}
constexpr EoTab make_eo() { EoTab e{}; for (int t = 0; t < 256; ++t) for (int k = 0; k < 12; ++k) e.v[t * 12 + k] = (u16)(k < 11 ? foff_c(2047 - 8 * t - k) * 4u : 0u); return e; }
__device__ const __attribute__((aligned(16))) EoTab EO = make_eo();

DI float bflo(u32 d) { return __uint_as_float(d << 16); }
DI float bfhi(u32 d) { return __uint_as_float(d & 0xffff0000u); }
DI u32 pk2(float lo, float hi) { bf16x2 v = {(__bf16)lo, (__bf16)hi}; return __builtin_bit_cast(u32, v); }
DI float rbf(float f) { return bflo(pk2(f, f)); }
DI bf16x8 as_bf(v4u v) { return __builtin_bit_cast(bf16x8, v); }
DI f32x4 mfma16(v4u a, v4u b, f32x4 c) { return __builtin_amdgcn_mfma_f32_16x16x32_bf16(as_bf(a), as_bf(b), c, 0, 0, 0); }
DI f32x16 mfma32(v4u a, v4u b, f32x16 c) { return __builtin_amdgcn_mfma_f32_32x32x16_bf16(as_bf(a), as_bf(b), c, 0, 0, 0); }
template <class T> DI LAS T* lds(u32 off) { return (LAS T*)(uintptr_t)off; }
DI v2u tr_read(u32 off) { s16x4 r = __builtin_amdgcn_ds_read_tr16_b64_v4i16(lds<s16x4>(off)); return __builtin_bit_cast(v2u, r); }
DI v4u tr_frag(u32 off0, u32 off1) { v2u a = tr_read(off0), b = tr_read(off1); return v4u{a.x, a.y, b.x, b.y}; }
template <int CTRL, int RM> DI float dpp_add(float v) { int t = __builtin_amdgcn_update_dpp(0, __float_as_int(v), CTRL, RM, 0xf, true); return v + __int_as_float(t); }
DI float wave_sum(float v) {
    v = dpp_add<0x111, 0xf>(v); v = dpp_add<0x112, 0xf>(v); v = dpp_add<0x114, 0xf>(v); v = dpp_add<0x118, 0xf>(v);
    v = dpp_add<0x142, 0xa>(v); v = dpp_add<0x143, 0xc>(v);
    return __int_as_float(__builtin_amdgcn_readlane(__float_as_int(v), 63));
}
DI float half_sum(float v) { const u32 u = __float_as_uint(v); const auto r = __builtin_amdgcn_permlane32_swap(u, u, false, false); return __uint_as_float(r[0]) + __uint_as_float(r[1]); }
DI void fence() { asm volatile("" ::: "memory"); }
DI void bar() { asm volatile("s_waitcnt lgkmcnt(0)" ::: "memory"); __builtin_amdgcn_s_barrier(); asm volatile("" ::: "memory"); }
template <int N> DI void vmw() { asm volatile("s_waitcnt vmcnt(%0)" :: "n"(N) : "memory"); }
template <bool S1> DI void glds16(u32 voff, const void* sbase, u32 ldst) {
    u32 keep;
    if (S1) asm volatile("s_mov_b32 %0, m0\n\ts_mov_b32 m0, %3\n\ts_nop 0\n\tglobal_load_lds_dwordx4 %1, %2 sc1\n\ts_mov_b32 m0, %0" : "=&s"(keep) : "v"(voff), "s"(sbase), "s"(ldst) : "memory");
    else    asm volatile("s_mov_b32 %0, m0\n\ts_mov_b32 m0, %3\n\ts_nop 0\n\tglobal_load_lds_dwordx4 %1, %2\n\ts_mov_b32 m0, %0" : "=&s"(keep) : "v"(voff), "s"(sbase), "s"(ldst) : "memory");
}
DI void glds16v(const void* gaddr, u32 ldst) {
    u32 keep;
    asm volatile("s_mov_b32 %0, m0\n\ts_mov_b32 m0, %2\n\ts_nop 0\n\tglobal_load_lds_dwordx4 %1, off\n\ts_mov_b32 m0, %0" : "=&s"(keep) : "v"(gaddr), "s"(ldst) : "memory");
}
DI float dot2(u32 a, u32 b, float c) { return __builtin_amdgcn_fdot2_f32_bf16(__builtin_bit_cast(bf16x2, a), __builtin_bit_cast(bf16x2, b), c, false); }
DI f2 cmul(f2 a, f2 b) { f2 t = f2{a.x, a.x} * b; return __builtin_elementwise_fma(f2{a.y, a.y}, f2{-b.y, b.x}, t); }
DI f2 cexp_rev(float ar, float ai, float t) {
    const float e = __builtin_amdgcn_exp2f(ar * t), ph = __builtin_amdgcn_fractf(ai * t);
    return f2{e * __builtin_amdgcn_cosf(ph), e * __builtin_amdgcn_sinf(ph)};
}
DI u32 swz_b(u32 row) { return ((row & 3u) << 2) | ((row >> 2) & 3u); }
DI u32 off_b(u32 row, u32 ch) { return 256u * row + 16u * (ch ^ swz_b(row)); }
DI u32 off_h(u32 row, u32 ch) { return 1024u * (row >> 3) + 512u * (ch >> 2) + 64u * (row & 7u) + 16u * ((ch & 3u) ^ ((row >> 2) & 3u)); }
DI u32 ln_pack(float mean, float var) {
    const float m = rbf(mean), sd = rbf(__builtin_amdgcn_sqrtf(rbf(rbf(var) + 1e-5f)));
    return (__float_as_uint(m) >> 16) | (__float_as_uint(sd) & 0xffff0000u);
}

#define STAMP(i) do { if ((i) == SPINAT) { while ((u32)(__builtin_amdgcn_s_memrealtime() - tstart0) < SPINX) __builtin_amdgcn_s_sleep(1); } } while (0)
#define CSTAMP(i) do { } while (0)
#define STAMP_DECL const u64 tstart0 = __builtin_amdgcn_s_memrealtime();
#define STAMP_FLUSH do { } while (0)
#define SGB(mask, n) __builtin_amdgcn_sched_group_barrier(mask, n, 0)
#define SB0() __builtin_amdgcn_sched_barrier(0)
constexpr int M_MFMA = 0x8, M_VMEM = 0x10, M_DSR = 0x100, M_VALU = 0x2;
DI void delay_ticks(u32 n) { const u64 t0 = __builtin_amdgcn_s_memrealtime(); while ((u32)(__builtin_amdgcn_s_memrealtime() - t0) < n) __builtin_amdgcn_s_sleep(1); }
DI u32 opaque(u32 v) { asm volatile("" : "+v"(v)); return v; }

__global__ void __launch_bounds__(512, 2)
fused_kernel(const u16* __restrict__ x, const u16* __restrict__ Are, const u16* __restrict__ Aim, const u16* __restrict__ Bre, const u16* __restrict__ Bim,
             const u16* __restrict__ Cre, const u16* __restrict__ Cim, const u16* __restrict__ logdt, const u16* __restrict__ Dskip,
             const u16* __restrict__ wg, const u16* __restrict__ wu, const u16* __restrict__ wd, u16* __restrict__ out, char* __restrict__ ws)
{
    const int tid = threadIdx.x, lane = tid & 63, w = __builtin_amdgcn_readfirstlane(tid >> 6);
    STAMP_DECL
    STAMP(0);
    asm volatile("" :: "s"(x), "s"(Are), "s"(Aim), "s"(Bre), "s"(Bim), "s"(Cre), "s"(Cim), "s"(logdt), "s"(Dskip), "s"(wg), "s"(wu), "s"(wd), "s"(out), "s"(ws));
    const int b = blockIdx.x, g = b & 7, j = b >> 3;
    const bool mainb = j < 28;
    const int cm = mainb ? g * 28 + j : 0;
    const int P = 4 * (g >> 1) + j / 7, S = 7 * (g & 1) + j % 7;
    const __amdgpu_buffer_rsrc_t rsW = __builtin_amdgcn_make_buffer_rsrc(ws, 0, WS_END, 0x00020000);
    const __amdgpu_buffer_rsrc_t rsO = __builtin_amdgcn_make_buffer_rsrc((void*)out, 0, L_ * D_ * 2, 0x00020000);
    const u32 trq = (lane & 15) >> 2, trp = lane & 3, trh = lane >> 5, trb = (lane >> 4) & 1;
    {
        u64 pc; asm volatile("s_getpc_b64 %0" : "=s"(pc));
        pc &= ~255ull;
        for (int q = j * 8 + w; q < CODEPF; q += 256) glds16<false>((u32)(q * 1024 + lane * 16), (const void*)pc, 156672);
    }
    v4u xr; u64 ep; u32 p_ld2, p_dk2;
    asm volatile("global_load_dwordx4 %0, %1, %2" : "=v"(xr) : "v"((u32)((8 * b + w) * 896 + min(lane, 55) * 16)), "s"(x) : "memory");
    asm volatile("s_load_dwordx2 %0, %3, 0x0\n\ts_load_dword %1, %4, 0x0\n\ts_load_dword %2, %5, 0x0" : "=&s"(ep), "=&s"(p_ld2), "=&s"(p_dk2)
                 : "s"(ws + WS_EP), "s"((const char*)logdt + cm * 4), "s"((const char*)Dskip + cm * 4) : "memory");
    const int ch = w >> 2, dch = 2 * cm + ch;
    if (mainb) {
        const int ai_ = lane >> 3;
        u64 src_ = (u64)Cim;
        src_ = ai_ == 4 ? (u64)Cre : src_; src_ = ai_ == 3 ? (u64)Bim : src_; src_ = ai_ == 2 ? (u64)Bre : src_; src_ = ai_ == 1 ? (u64)Aim : src_; src_ = ai_ == 0 ? (u64)Are : src_;
        const u16* src = (const u16*)src_;
        glds16v(src + dch * N_ + (w & 3) * 64 + (lane & 7) * 8, PRM + w * 1024);
        const int ew = w < 6 ? w : w - 6;
        glds16<false>((u32)(ew * 1024 + lane * 16), (const void*)&EO, EOL + ew * 1024);
    } else {
        const int hb = (j - 28) * 8 + g;
#pragma unroll
        for (int k = 0; k < 7; ++k) {
            const int gid = min(hb + 32 * k, 195);
            const int dt_ = gid / 28, fg = gid % 28;
            const u32 row = 8 * w + (lane >> 3), chk = (lane & 7) ^ (((row >> 1) & 1) << 2);
            glds16<false>((u32)((64 * fg + row) * (D_ * 2) + dt_ * 128 + chk * 16), (const void*)wd, k * 8192 + w * 1024);
        }
    }
    asm volatile("s_cmp_ge_i32 %1, 28\n\ts_cbranch_scc1 1f\n\ts_waitcnt vmcnt(2)\n\ts_branch 2f\n1:\n\ts_waitcnt vmcnt(7)\n2:" : "+v"(xr) : "s"(__builtin_amdgcn_readfirstlane(j)) : "memory", "scc");
    asm volatile("s_waitcnt lgkmcnt(0)" : "+s"(ep), "+s"(p_ld2), "+s"(p_dk2) :: "memory");
    ep += 1ull;
    const u32 p_ldt = ch ? p_ld2 >> 16 : p_ld2 & 0xffffu, p_dsk = ch ? p_dk2 >> 16 : p_dk2 & 0xffffu;
    if (lane >= 56) xr = v4u{0, 0, 0, 0};
    const u32 tag = (u32)ep, tagx = (u32)(ep >> 32) << 16;
    if (lane < 56) *lds<v4u>(XT + w * 912 + lane * 16) = xr;
    {
        const float v[8] = {bflo(xr.x), bfhi(xr.x), bflo(xr.y), bfhi(xr.y), bflo(xr.z), bfhi(xr.z), bflo(xr.w), bfhi(xr.w)};
        const float mean = wave_sum(((v[0] + v[1]) + (v[2] + v[3])) + ((v[4] + v[5]) + (v[6] + v[7]))) * (1.f / 448.f);
        float q = 0.f;
#pragma unroll
        for (int e = 0; e < 8; ++e) { const float dl = v[e] - mean; q += dl * dl; }
        q = lane < 56 ? q : 0.f;
        const u32 ln1 = ln_pack(mean, wave_sum(q) * (1.f / 448.f));
        if (lane == 0) __hip_atomic_store((GAS u64*)(ws + WS_SG) + (8 * b + w), ((u64)tag << 32) | ln1, RLX_AGENT);
    }
    STAMP(1);
    const u32 km0 = FM + ch * FM_CH + (w & 3) * 256;
    f2 Ga, Gb, Gc, Gd, KX, KY, G4, G4r, E1, E1r, E256, E256r, E2, E2r, E4, E4r;
#define M1(a, bb) (f2{(a).x, (a).x} * (bb))
#define M2(a, br, t) __builtin_elementwise_fma(f2{(a).y, (a).y}, (br), (t))
#define WROW1(r, v) do { u32 keep_; asm volatile("s_mov_b32 %0, m0\n\ts_mov_b32 m0, %2\n\ts_nop 0\n\tds_write_addtid_b32 %1 offset:%3\n\ts_mov_b32 m0, %0" : "=&s"(keep_) : "v"(v), "s"(km0), "n"((r) * FM_ROW) : "memory"); } while (0)
#define WROW6(r0, v0, r1, v1, r2, v2, r3, v3, r4, v4, r5, v5) do { u32 keep_; asm volatile("s_mov_b32 %0, m0\n\ts_mov_b32 m0, %7\n\ts_nop 0\n\t" \
        "ds_write_addtid_b32 %1 offset:%8\n\tds_write_addtid_b32 %2 offset:%9\n\tds_write_addtid_b32 %3 offset:%10\n\tds_write_addtid_b32 %4 offset:%11\n\tds_write_addtid_b32 %5 offset:%12\n\tds_write_addtid_b32 %6 offset:%13\n\ts_mov_b32 m0, %0" \
        : "=&s"(keep_) : "v"(v0), "v"(v1), "v"(v2), "v"(v3), "v"(v4), "v"(v5), "s"(km0), "n"((r0) * FM_ROW), "n"((r1) * FM_ROW), "n"((r2) * FM_ROW), "n"((r3) * FM_ROW), "n"((r4) * FM_ROW), "n"((r5) * FM_ROW) : "memory"); } while (0)
#define WA(r, X) WROW1(r, pk2((X).x, -(X).y))
#define KSTEP(t) do { SB0(); \
        const f2 ta = M1(Ga, G4), tb = M1(Gb, G4), tc = M1(Gc, G4), td_ = M1(Gd, G4), tx = M1(KX, E1), ty = M1(KY, E256); \
        const u32 c0 = pk2(Ga.x, Ga.y), c1 = pk2(Gb.x, Gb.y), c2 = pk2(Gc.x, Gc.y), c3 = pk2(Gd.x, Gd.y), c4 = pk2(KX.x, -KX.y), c5 = pk2(KY.x, -KY.y); \
        f2 ts0, ts1; \
        if ((t) == 0) ts0 = M1(KY, E2); \
        if ((t) == 1 || (t) == 2) ts0 = M1(KY, E4); \
        SB0(); \
        WROW6(21 + 4 * (t), c0, 22 + 4 * (t), c1, 23 + 4 * (t), c2, 24 + 4 * (t), c3, (t), c4, ((t) == 0 ? 8 : (t) == 1 ? 12 : (t) == 2 ? 14 : 13 + (t)), c5); \
        if ((t) == 0) { f2 Z = M2(KY, E2r, ts0); WA(9, Z); ts1 = M1(Z, E2); Z = M2(Z, E2r, ts1); WA(10, Z); ts1 = M1(Z, E2); Z = M2(Z, E2r, ts1); WA(11, Z); } \
        if ((t) == 1) { const f2 V = M2(KY, E4r, ts0); WA(13, V); } \
        if ((t) == 2) { const f2 V = M2(KY, E4r, ts0); WA(15, V); } \
        if ((t) < 7) { Ga = M2(Ga, G4r, ta); Gb = M2(Gb, G4r, tb); Gc = M2(Gc, G4r, tc); Gd = M2(Gd, G4r, td_); KX = M2(KX, E1r, tx); KY = M2(KY, E256r, ty); } \
    } while (0)
    bar();
    auto publish_xg = [&]() {
        u32 x0[4], x1[4];
#pragma unroll
        for (int k = 0; k < 4; ++k) {
            const int id = min(tid + 512 * k, 1791), c = id >> 2, i2 = (id & 3) * 2;
            x0[k] = *lds<u16>(XT + i2 * 912 + c * 2); x1[k] = *lds<u16>(XT + (i2 + 1) * 912 + c * 2);
        }
        SB0();
#pragma unroll
        for (int k = 0; k < 4; ++k)
            __builtin_amdgcn_raw_buffer_store_b128(v4u{x0[k] | tagx, tag, x1[k] | tagx, tag}, rsW, (tid + 512 * k < 1792) ? (u32)(WS_XG + b * 28672 + (tid + 512 * k) * 16) : 0x7ffffff0u, 0, SC1);
    };
    if (mainb) {
    publish_xg();
    {
        vmw<5>();
        {
            const int bb_ = b < 196 ? b : b - 196;
    #pragma unroll
            for (int i = 0; i < 2; ++i) {
                const int id = 2 * bb_ + i, Sp = id / 28, s = id % 28;
                const u32 row = (w & 3) * 4 + (lane >> 4), chk = (lane & 15) ^ swz_b(row);
                glds16<false>((u32)((16 * s + row) * (F_ * 2) + Sp * 256 + chk * 16), (w >> 2) ? (const void*)wu : (const void*)wg, RT + i * 8192 + w * 1024);
            }
        }
        const u32 pa_ = PRM + w * 1024 + lane * 2;
        const u32 p_are = *lds<u16>(pa_), p_aim = *lds<u16>(pa_ + 128), p_bre = *lds<u16>(pa_ + 256), p_bim = *lds<u16>(pa_ + 384), p_cre = *lds<u16>(pa_ + 512), p_cim = *lds<u16>(pa_ + 640);
        const float dt = rbf(expf(bflo(p_ldt)));
        const float zr = dt * bflo(p_are), zi = dt * bflo(p_aim);
        const float ar = zr * 1.4426950408889634f, ai = zi * 0.15915494309189535f;
        const f2 CB = cmul(f2{bflo(p_cre), bflo(p_cim)}, f2{dt * bflo(p_bre), dt * bflo(p_bim)});
        E1 = cexp_rev(ar, ai, 1.f); E256 = cexp_rev(ar, ai, 256.f);
        const f2 G1 = cexp_rev(ar, ai, 8.f);
        E2 = cmul(E1, E1); E4 = cmul(E2, E2);
        const f2 G2 = cmul(G1, G1);
        G4 = cmul(G2, G2);
        E1r = f2{-E1.y, E1.x}; E2r = f2{-E2.y, E2.x}; E4r = f2{-E4.y, E4.x}; E256r = f2{-E256.y, E256.x}; G4r = f2{-G4.y, G4.x};
        Ga = f2{1.f, 0.f}; Gb = G1; Gc = G2; Gd = cmul(G2, G1);
        KX = CB; KY = cmul(CB, E256);
        SB0();
    }
    KSTEP(0); KSTEP(1); KSTEP(2);
    SB0();
    fence();
    STAMP(2);
    {
        KSTEP(3); KSTEP(4); KSTEP(5); KSTEP(6); KSTEP(7);
        SB0();
#undef KSTEP
#undef WA
#undef WROW1
#undef WROW6
#undef M1
#undef M2
        STAMP(3);
        vmw<0>();
        bar();
#pragma unroll
        for (int i = 0; i < 2; ++i) {
            const int id = 2 * b + i, Sp = id / 28, s = id % 28;
            const u32 base = RT + i * 8192 + trb * 4096 + 8 * (trp & 1);
            const v4u f = tr_frag(base + off_b(8 * trh + trq, 2 * w + (trp >> 1)), base + off_b(8 * trh + 4 + trq, 2 * w + (trp >> 1)));
            __builtin_amdgcn_raw_buffer_store_b128(f, rsW, (b < 196 ? (u32)(WS_WG + ((Sp * 8 + w) * 28 + s) * 1024) : 0x7ffffc00u) + lane * 16, 0, SC1);
        }
        const u32 xa = WS_XG + (32 * w + (lane >> 3)) * 28672 + cm * 128 + (lane & 7) * 16, sa = WS_SG + (32 * w + (lane >> 3)) * 64 + (lane & 3) * 16;
        v4u xg[4], sg[4];
        const int ta_ = (w >> 1) & 1, tb_ = w & 1;
        const u32 fa_ = opaque(FM + ch * FM_CH + (EXPM ? 16 * ta_ + (lane & 15) : min(16 * ta_ + (lane & 15), 20)) * FM_ROW + (lane >> 4) * 16), fb_ = opaque(FM + ch * FM_CH + (21 + 16 * tb_ + (lane & 15)) * FM_ROW + (lane >> 4) * 16);
        f32x4 f0 = {0.f, 0.f, 0.f, 0.f}, f1 = {0.f, 0.f, 0.f, 0.f};
        v4u A[16], B[16];
#pragma unroll
        for (int ks = 0; ks < 16; ++ks) { A[ks] = *lds<v4u>(fa_ + ks * 64); B[ks] = *lds<v4u>(fb_ + ks * 64); }
        SB0();
#pragma unroll
        for (int k = 0; k < 4; ++k) { xg[k] = __builtin_amdgcn_raw_buffer_load_b128(rsW, xa + k * 8 * 28672, 0, SC1); sg[k] = __builtin_amdgcn_raw_buffer_load_b128(rsW, sa + k * 512, 0, SC1); }
        SB0();
#pragma unroll
        for (int ks = 0; ks < 16; ks += 2) { f0 = mfma16(A[ks], B[ks], f0); f1 = mfma16(A[ks + 1], B[ks + 1], f1); }
        f0 += f1;
#pragma unroll
        for (int r = 0; r < 4; ++r) { const int row = 16 * ta_ + 4 * (lane >> 4) + r; if (row < 24) *lds<float>(FT + ch * FT_CH + (row * 32 + 16 * tb_ + (lane & 15)) * 4) = f0[r]; }
        if ((tid & 255) == 255) *lds<float>(FT + ch * FT_CH + 768 * 4) = 0.f;
        STAMP(4);
        for (u32 spin = 0;; ++spin) {
            u32 bad = 0;
#pragma unroll
            for (int k = 0; k < 4; ++k) bad |= ((sg[k].y ^ tag) | (sg[k].w ^ tag) | (xg[k].y ^ tag)) | ((xg[k].w ^ tag) | (((xg[k].x ^ tagx) | (xg[k].z ^ tagx)) & 0xffff0000u));
            if (__all(bad == 0) || spin > SPIN_CAP) break;
            __builtin_amdgcn_s_sleep(4);
#pragma unroll
            for (int k = 0; k < 4; ++k) { xg[k] = __builtin_amdgcn_raw_buffer_load_b128(rsW, xa + k * 8 * 28672, 0, SC1); sg[k] = __builtin_amdgcn_raw_buffer_load_b128(rsW, sa + k * 512, 0, SC1); }
        }
        STAMP(5);
        const u32 ua = opaque(UU + ((lane >> 2) & 1) * UU_CH + 512 + (32 * w + (lane >> 3)) * 16 + (lane & 3) * 4), xsa = opaque(XS + ((lane >> 2) & 1) * 4096 + (32 * w + (lane >> 3)) * 16 + (lane & 3) * 4);
#pragma unroll
        for (int k = 0; k < 4; ++k) {
            const u32 xm = pk2(bflo(xg[k].x) - bflo(sg[k].x), bflo(xg[k].z) - bflo(sg[k].z));
            const float u0 = bflo(xm) * __builtin_amdgcn_rcpf(bfhi(sg[k].x)), u1 = bfhi(xm) * __builtin_amdgcn_rcpf(bfhi(sg[k].z));
            *lds<u32>(ua + k * 128) = pk2(u0, u1);
            *lds<u32>(xsa + k * 128) = __builtin_amdgcn_perm(xg[k].z, xg[k].x, 0x05040100u);
        }
    }
    if (tid >= 64 && tid < 128) *lds<v4u>(UU + ((tid >> 5) & 1) * UU_CH + (tid & 31) * 16) = v4u{0, 0, 0, 0};
    const u32 eb_ = opaque(EOL + (tid & 255) * 24);
    const v2u ea = *lds<v2u>(eb_), eb = *lds<v2u>(eb_ + 8), ec = *lds<v2u>(eb_ + 16);
    vmw<0>();
    bar();
    if (tid == 0 && b < 196) __hip_atomic_store((GAS u32*)(ws + WS_WR + b * 64), tag, RLX_AGENT);
    const int rq = w & 3;
    const u32 cb = lane & 15, cq = lane >> 4;
    const u32 ubase = opaque(UU + ch * UU_CH + 512 + 32 * (cb - (cq >> 1)) + 16 * (cq & 1) - 32 * 4 * rq - 32 * 2);
    auto ldB = [&](int k, int rr) { return *lds<v4u>(ubase + 32 * (16 * k - 2 * rr + 2)); };
    v4u Bf[8], Bn[8];
#pragma unroll
    for (int p = 0; p < 4; ++p) { Bf[p] = ldB(p, 0); Bn[p] = ldB(p, 1); }
    {
        const u32 fb_ = FT + ch * FT_CH;
        const u32 eo[11] = {ea.x & 0xffffu, ea.x >> 16, ea.y & 0xffffu, ea.y >> 16, eb.x & 0xffffu, eb.x >> 16, eb.y & 0xffffu, eb.y >> 16, ec.x & 0xffffu, ec.x >> 16, ec.y & 0xffffu};
        float v[11];
#pragma unroll
        for (int e = 0; e < 11; ++e) v[e] = *lds<float>(fb_ + eo[e]);
        SB0();
        u32 p[10];
#pragma unroll
        for (int e = 0; e < 10; ++e) p[e] = pk2(v[e], v[e + 1]);
        const u32 ka_ = opaque(KC + ch * KC_CH + (tid & 255) * 16);
#pragma unroll
        for (int s = 0; s < 4; ++s) *lds<v4u>(ka_ + s * KC_COPY) = v4u{p[s], p[s + 2], p[s + 4], p[s + 6]};
    }
    if (tid < 16) *lds<v4u>(KC + (tid >> 3) * KC_CH + ((tid >> 1) & 3) * KC_COPY + 4096 + (tid & 1) * 16) = v4u{0, 0, 0, 0};
    bar();
    STAMP(6);
    CSTAMP(26);
    v4u Wf[28];
    const u32 l16 = lane * 16;
    const int wf_off = WS_WG + (S * 8 + w) * 28 * 1024;
    u32 f2a;
    const float dsv = bflo(p_dsk);
    const u32 f2_off = WS_F2 + g * 16384 + (tid < 224 ? tid : 0) * 64;
    const u32 bt_src = (8 * w + ((lane >> 2) & 7)) * 4096 + 128 * P + (4 * (lane >> 5) + ((lane & 3) ^ ((2 * w + ((lane >> 4) & 1)) & 3))) * 16;
    auto bt_piece = [&](int part, int k) { glds16<true>(bt_src + part * 2048 + k * 64 * 4096, ws + WS_HT, (part ? BTB : BTA) + (8 * k + w) * 1024); };
    {
        const u32 cs = (3u - cb) & 3u;
        const u32 kbase = opaque(KC + ch * KC_CH + cs * KC_COPY + 2 * (2047 - 16 * (cq >> 1) - cb + 8 * (cq & 1) - cs) - 32 * 4 * rq - 32 * 114);
        const u32 kbase8 = opaque(kbase + 8);
        auto ldA = [&](int p, int rr) { const v2u lo = *lds<volatile v2u>(kbase + 32 * (114 - (16 * p + 2 * rr))), hi = *lds<volatile v2u>(kbase8 + 32 * (114 - (16 * p + 2 * rr))); return v4u{lo.x, lo.y, hi.x, hi.y}; };
        const u32 cxw = opaque(CX + (4 * ch * 4 + rq) * 1024 + lane * 16), cxr = opaque(CX + w * 4096 + lane * 16);
        const u32 fu_ = opaque(UU + ch * UU_CH + 512 + (256 * rq + 16 * cb + 4 * cq) * 2), fx_ = opaque(XS + ch * 4096 + (256 * rq + 16 * cb + 4 * cq) * 2);
        const u32 ht_ = WS_HT + dch * 4096 + (256 * rq + 16 * cb + 4 * cq) * 2;
        auto finish = [&](f32x4 (&acc)[4], int tbase) {
#pragma unroll
            for (int i = 0; i < 4; ++i) *lds<f32x4>(cxw + i * 4096) = acc[i];
            const v2u uu = *lds<v2u>(fu_ + tbase * 512), xx = *lds<v2u>(fx_ + tbase * 512);
            bar();
            if (tbase == 4) f2a = __builtin_amdgcn_raw_buffer_load_b32(rsW, w < 4 ? (tid < 224 ? f2_off : (u32)(WS_WF + (tid - 224) * 64)) : f2_off, 0, SC1);
            const f32x4 p0 = *lds<f32x4>(cxr), p1 = *lds<f32x4>(cxr + 1024), p2 = *lds<f32x4>(cxr + 2048), p3 = *lds<f32x4>(cxr + 3072);
            const float uv[4] = {bflo(uu.x), bfhi(uu.x), bflo(uu.y), bfhi(uu.y)}, xv[4] = {bflo(xx.x), bfhi(xx.x), bflo(xx.y), bfhi(xx.y)};
            const u32 k01 = pk2(uv[0] * dsv, uv[1] * dsv), k23 = pk2(uv[2] * dsv, uv[3] * dsv);
            SB0();
            const f32x4 y = (p0 + p1) + (p2 + p3);
            const u32 y01 = pk2(y[0], y[1]), y23 = pk2(y[2], y[3]);
            const u32 z01 = pk2(bflo(y01) + bflo(k01), bfhi(y01) + bfhi(k01)), z23 = pk2(bflo(y23) + bflo(k23), bfhi(y23) + bfhi(k23));
            const float h[4] = {xv[0] + bflo(z01), xv[1] + bfhi(z01), xv[2] + bflo(z23), xv[3] + bfhi(z23)};
            __builtin_amdgcn_raw_buffer_store_b64(v2u{pk2(h[0], h[1]), pk2(h[2], h[3])}, rsW, ht_ + tbase * 512, 0, SC1);
        };
        f32x4 acc[4];
        v4u Af[8], An[8];
#pragma unroll
        for (int i = 0; i < 4; ++i) acc[i] = f32x4{0.f, 0.f, 0.f, 0.f};
#pragma unroll
        for (int p = 0; p < 4; ++p) { Af[p] = ldA(p, 0); An[p] = ldA(p, 1); }
        SB0();
#pragma unroll
        for (int p = 0; p < 4; ++p)
#pragma unroll
            for (int i = p; i < 4; ++i) acc[i] = mfma16(Af[p], Bf[i - p], acc[i]);
#pragma unroll
        for (int p = 0; p < 4; ++p)
#pragma unroll
            for (int i = p; i < 4; ++i) acc[i] = mfma16(An[p], Bn[i - p], acc[i]);
#pragma unroll
        for (int p = 4; p < 8; ++p) { Af[p] = ldA(p, 0); Bf[p] = ldB(p, 0); }
        SB0();
        CSTAMP(16);
        finish(acc, 0);
        CSTAMP(17);
#pragma unroll
        for (int i = 0; i < 4; ++i) acc[i] = f32x4{0.f, 0.f, 0.f, 0.f};
        SB0();
#pragma unroll
        for (int p = 4; p < 8; ++p) { An[p] = ldA(p, 1); Bn[p] = ldB(p, 1); }
        const u32 wr_off = WS_WR + (tid < 196 ? tid : 0) * 64;
        const u32 wr_flag = __builtin_amdgcn_raw_buffer_load_b32(rsW, wr_off, 0, SC1);
#pragma unroll
        for (int p = 0; p < 8; ++p)
#pragma unroll
            for (int i = 0; i < 4; ++i) if (p <= 4 + i) acc[i] = mfma16(Af[p], Bf[4 + i - p], acc[i]);
        SGB(M_VMEM, 1);
#pragma unroll
        for (int i = 0; i < 6; ++i) { SGB(M_MFMA, 2); SGB(M_DSR, 2); SGB(M_MFMA, 2); }
        SB0();
        CSTAMP(18);
        if (!__all(wr_flag == tag)) {
            for (u32 spin = 0; spin < SPIN_CAP; ++spin) {
                if (__all(__builtin_amdgcn_raw_buffer_load_b32(rsW, wr_off, 0, SC1) == tag)) break;
                __builtin_amdgcn_s_sleep(4);
            }
        }
        vmw<0>();
        CSTAMP(19);
        bar();
        CSTAMP(20);
        if (tid < 8) __hip_atomic_store((GAS u32*)(ws + WS_F2 + tid * 16384 + cm * 64), tag, RLX_AGENT);
        SB0();
#pragma unroll
        for (int i = 0; i < 7; ++i) Wf[i] = __builtin_amdgcn_raw_buffer_load_b128(rsW, l16 + (i) * 1024 % 4096, wf_off + (i) * 1024 / 4096 * 4096, WAUX);
#pragma unroll
        for (int p = 0; p < 8; ++p)
#pragma unroll
            for (int i = 0; i < 4; ++i) if (p <= 4 + i) acc[i] = mfma16(An[p], Bn[4 + i - p], acc[i]);
#pragma unroll
        for (int i = 0; i < 6; ++i) { SGB(M_MFMA, 4); SGB(M_VMEM, 1); }
        SGB(M_MFMA, 2); SGB(M_VMEM, 1);
        SB0();
        CSTAMP(21);
#pragma unroll
        for (int i = 7; i < 14; ++i) Wf[i] = __builtin_amdgcn_raw_buffer_load_b128(rsW, l16 + i * 1024 % 4096, wf_off + i * 1024 / 4096 * 4096, WAUX);
        finish(acc, 4);
    }
    STAMP(7);
    CSTAMP(27);
    auto gate_spin = [&](u32 off, bool active) {
        for (u32 spin = 0;; ++spin) {
            const u32 s0 = __builtin_amdgcn_raw_buffer_load_b32(rsW, off, 0, SC1);
            if (__all(s0 == tag || !active) || spin > SPIN_CAP) break;
            __builtin_amdgcn_s_sleep(GSLEEP);
        }
    };
    if (w < 4 && !__all(f2a == tag)) gate_spin(tid < 224 ? f2_off : (u32)(WS_WF + (tid - 224) * 64), true);
    bar();
    STAMP(8);
    if (b == 0 && tid == 0) __hip_atomic_store((GAS u64*)(ws + WS_EP), ep, RLX_AGENT);
    const float csum = __uint_as_float(__builtin_amdgcn_raw_buffer_load_b32(rsW, WS_CS + (((lane >> 4) & 1) * 1792 + 128 * S + 16 * w + (lane & 15)) * 4, 0, SC1));
    fence();
#pragma unroll
    for (int k = 0; k < 7; ++k) bt_piece(0, k);
    vmw<4>();
    bar();
    if (tid < 8) __hip_atomic_store((GAS u32*)(ws + WS_F2 + (8 + tid) * 16384 + cm * 64), tag, RLX_AGENT);
    STAMP(9);
    CSTAMP(24);
    v4u Wd[28];
    const int td = w & 1, kq = w >> 1, hp = S / 7, dtile = S % 7;
    const int wd_off = WS_WD + ((dtile * 2 + td) * 112 + 28 * kq) * 1024;
    const u32 trb0 = 1024 * trh + 64 * trq + 16 * ((2 * trb + (trp >> 1)) ^ ((2 * trh) & 3)) + 8 * (trp & 1), trb1 = 1024 * trh + 64 * (4 + trq) + 16 * ((2 * trb + (trp >> 1)) ^ ((2 * trh + 1) & 3)) + 8 * (trp & 1);
    const int hh_out = WS_HH + (P * 112 + 8 * S + w) * 4096;
    u32 hres[2][2]; float rs[2];
    const u32 f3_off = WS_F3 + (P * 16 + (lane < 14 ? lane : 0)) * 64;
    const char* hsrc = ws + WS_HH + (P * 112 + 28 * kq + td) * 4096 + hp * 1024; const u32 hdst = kq * 1024;
    auto hh_dma = [&](int u) { glds16<true>(l16, hsrc + (u % 28) * 4096 + (u >= 28 ? 2048 : 0), (td ? hh_slot(u + 1) : hh_slot(u)) + hdst); };
#define GEMM1_PASS(PART) do { \
        const u32 b0 = opaque(((PART) ? BTB : BTA) + trb0), b1 = opaque(((PART) ? BTB : BTA) + trb1); \
        const u32 s0_ = opaque(b0 + (w >> 1) * 2048 + (w & 1) * 512), s1_ = opaque(b1 + (w >> 1) * 2048 + (w & 1) * 512); \
        f32x16 acc[2]; \
        _Pragma("unroll") for (int i = 0; i < 16; ++i) { acc[0][i] = 0.f; acc[1][i] = 0.f; } \
        v4u Bq[3][2], Of; \
        float sa0 = 0.f, sa1 = 0.f, sq0 = 0.f, sq1 = 0.f; \
        u32 gatev = 0; \
        _Pragma("unroll") for (int t = 0; t < 2; ++t) { Bq[0][t] = tr_frag(b0 + 512 * t, b1 + 512 * t); Bq[1][t] = tr_frag(b0 + 2048 + 512 * t, b1 + 2048 + 512 * t); } \
        _Pragma("unroll") for (int s = 0; s < 28; ++s) { \
            SB0(); \
            if (!(PART)) { \
                if (s == 10) { vmw<10>(); bar(); }        \
                if (s == 19) {     \
                    if (w < 4) { if (!__all(gatev == tag || tid >= 224)) gate_spin(f2_off + 8 * 16384, tid < 224); } \
                    bar(); \
                } \
            } else { \
                if (s == 3) { vmw<2>(); bar(); if (tid == 0) __hip_atomic_store((GAS u32*)(ws + WS_F3 + (P * 16 + S) * 64), tag, RLX_AGENT); }     \
                if (s == 20) {     \
                    if (w == 0) { if (!__all(gatev == tag)) gate_spin(f3_off, true); } \
                    bar(); \
                } \
            } \
            SB0(); \
            if (s + 2 < 28) { _Pragma("unroll") for (int t = 0; t < 2; ++t) Bq[(s + 2) % 3][t] = tr_frag(b0 + (s + 2) * 2048 + 512 * t, b1 + (s + 2) * 2048 + 512 * t); } \
            if ((s & 3) == 0 && !EXPS) Of = tr_frag(s0_ + s * 2048, s1_ + s * 2048);        \
            if (!(PART)) { \
                if (s < 14) { Wf[14 + s] = __builtin_amdgcn_raw_buffer_load_b128(rsW, l16 + ((14 + s)) * 1024 % 4096, wf_off + ((14 + s)) * 1024 / 4096 * 4096, WAUX); } \
                else if (s == 14) { if (w < 4) gatev = __builtin_amdgcn_raw_buffer_load_b32(rsW, f2_off + 8 * 16384, 0, SC1); } \
                else if (s < 19) { if (!EXPW) Wd[s - 15] = __builtin_amdgcn_raw_buffer_load_b128(rsW, l16 + ((s - 15)) * 1024 % 4096, wd_off + ((s - 15)) * 1024 / 4096 * 4096, WAUX); } \
                else if (s < 26) bt_piece(1, s - 19); \
            } else { \
                if (s >= 1 && s < 25) Wd[3 + s] = __builtin_amdgcn_raw_buffer_load_b128(rsW, l16 + (3 + s) * 1024 % 4096, wd_off + (3 + s) * 1024 / 4096 * 4096, WAUX); \
                if (s == 15) { if (w == 0) gatev = __builtin_amdgcn_raw_buffer_load_b32(rsW, f3_off, 0, SC1); } \
                if (s >= 20 && s < 27) { fence(); hh_dma(2 * (s - 20)); } \
                if (s >= 25) { fence(); hh_dma(14 + 2 * (s - 25)); }        \
            } \
            if (EXPP) __builtin_amdgcn_s_setprio(1); \
            _Pragma("unroll") for (int t = 0; t < 2; ++t) acc[t] = mfma32(Wf[s], Bq[s % 3][t], acc[t]); \
            if (EXPP) __builtin_amdgcn_s_setprio(0); \
            if ((s & 3) == 2 && !EXPS) { \
                _Pragma("unroll") for (int e = 0; e < 4; e += 2) { \
                    const float l0 = bflo(Of[e]), h0 = bfhi(Of[e]), l1 = bflo(Of[e + 1]), h1 = bfhi(Of[e + 1]); \
                    sa0 += l0; sq0 = __builtin_fmaf(l0, l0, sq0); sa1 += l1; sq1 = __builtin_fmaf(l1, l1, sq1); \
                    sa0 += h0; sq0 = __builtin_fmaf(h0, h0, sq0); sa1 += h1; sq1 = __builtin_fmaf(h1, h1, sq1); \
                } \
                asm volatile("" : "+v"(sa0), "+v"(sa1), "+v"(sq0), "+v"(sq1));        \
                if (EXPG == 0) { SGB(M_MFMA, 1); SGB(M_DSR, 2); SGB(M_VALU, 12); SGB(M_VMEM, 1); SGB(M_MFMA, 1); SGB(M_DSR, 2); SGB(M_VALU, 12); } \
                if (EXPG == 1) { SGB(M_VMEM, 1); SGB(M_MFMA, 1); SGB(M_DSR, 4); SGB(M_VALU, 6); SGB(M_MFMA, 1); SGB(M_VALU, 18); } \
                if (EXPG == 2) { SGB(M_MFMA, 2); SGB(M_DSR, 4); SGB(M_VMEM, 1); SGB(M_VALU, 24); } \
            } else { \
                if (EXPG == 0) { SGB(M_MFMA, 1); SGB(M_DSR, 3); SGB(M_VMEM, 1); SGB(M_MFMA, 1); SGB(M_DSR, 3); } \
                if (EXPG == 1) { SGB(M_VMEM, 1); SGB(M_MFMA, 1); SGB(M_DSR, 6); SGB(M_MFMA, 1); } \
                if (EXPG == 2) { SGB(M_MFMA, 2); SGB(M_DSR, 6); SGB(M_VMEM, 1); } \
            } \
        } \
        SB0(); \
        { \
            const float sh = half_sum(sa0 + sa1), sq = half_sum(sq0 + sq1); \
            const u32 wa_ = opaque(SPQ + (PART) * 2048 + (w & 1) * 1024 + (w >> 1) * 4 + lane * 32); \
            if (lane < 32) { *lds<float>(wa_) = sh; *lds<float>(wa_ + 16) = sq; if (!(PART)) *lds<float>(CSL + w * 128 + lane * 4) = csum; } \
        } \
        bar(); \
        {     \
            const u32 ca_ = opaque(CSL + w * 128 + trh * 16), sa_ = opaque(SPQ + (PART) * 2048 + (lane & 31) * 32); \
            const f32x4 csg[2] = {*lds<f32x4>(ca_), *lds<f32x4>(ca_ + 32)}, csu[2] = {*lds<f32x4>(ca_ + 64), *lds<f32x4>(ca_ + 96)}; \
            const f32x4 sm[2] = {*lds<f32x4>(sa_), *lds<f32x4>(sa_ + 1024)}; \
            const f32x4 sq4 = *lds<f32x4>(sa_ + hp * 1024 + 16); \
            const u32 lc_ = 4 * hp + ((lane & 31) >> 3);        \
            const u32 h0_ = opaque(((PART) ? BTB : BTA) + (8 * dtile + 4 * td + kq) * 1024 + (lc_ >> 2) * 512 + trh * 256 + 16 * ((lc_ & 3) ^ ((2 * kq + trh) & 3)) + (lane & 7) * 2); \
            u32 hb_[4]; \
            _Pragma("unroll") for (int e = 0; e < 4; ++e) hb_[e] = *lds<u16>(h0_ + 64 * e); \
            if (EXPW && !(PART)) { _Pragma("unroll") for (int i = 0; i < 4; ++i) Wd[i] = __builtin_amdgcn_raw_buffer_load_b128(rsW, l16 + i * 1024, wd_off, WAUX); } \
            SB0(); \
            _Pragma("unroll") for (int t = 0; t < 2; ++t) { \
                const float m = rbf(((sm[t][0] + sm[t][1]) + (sm[t][2] + sm[t][3])) * (1.f / 448.f)); \
                float hv[8]; \
                _Pragma("unroll") for (int e = 0; e < 8; ++e) hv[e] = (acc[t][e] - m * csg[e >> 2][e & 3]) * (acc[t][e + 8] - m * csu[e >> 2][e & 3]); \
                __builtin_amdgcn_raw_buffer_store_b128(v4u{pk2(hv[0], hv[1]), pk2(hv[2], hv[3]), pk2(hv[4], hv[5]), pk2(hv[6], hv[7])}, rsW, l16 + hh_out + (2 * (PART) + t) * 1024, 0, SC1); \
            } \
            { \
                const f32x4 s4 = hp ? sm[1] : sm[0]; \
                const float mean = ((s4[0] + s4[1]) + (s4[2] + s4[3])) * (1.f / 448.f), var = fmaxf(((sq4[0] + sq4[1]) + (sq4[2] + sq4[3])) * (1.f / 448.f) - mean * mean, 0.f); \
                const float sd = bfhi(ln_pack(mean, var)); \
                rs[PART] = __builtin_amdgcn_rcpf(sd * sd); \
                hres[PART][0] = hb_[0] | (hb_[1] << 16); hres[PART][1] = hb_[2] | (hb_[3] << 16); \
            } \
        } \
    } while (0)
    GEMM1_PASS(0);
    CSTAMP(31);
    vmw<2 + 4 * EXPW>();
    bar();
    STAMP(10);
    GEMM1_PASS(1);
    STAMP(11);
    CSTAMP(25);
#undef GEMM1_PASS
    vmw<2>();
    bar();
    STAMP(12);
    CSTAMP(28);
    f32x16 o2[2];
#pragma unroll
    for (int i = 0; i < 16; ++i) { o2[0][i] = 0.f; o2[1][i] = 0.f; }
    const u32 hrdA = opaque(BTA + kq * 1024 + lane * 16), hrdS = opaque(RSP + kq * 1024 + lane * 16), hrdB = opaque(BTB + kq * 1024 + lane * 16);
    auto hh_rd = [&](int u) {
        const u32 sl = hh_slot(u);
        return *lds<v4u>(sl >= RSP ? hrdS + (sl - RSP) : sl >= BTA ? hrdA + (sl - BTA) : hrdB + (sl - BTB));
    };
    const u32 op = (64 * P + 32 * hp + (lane & 31)) * (D_ * 2) + (64 * dtile + 32 * td + 8 * kq + 4 * trh) * 2;
    const u32 red_rd = opaque(td * 12288 + kq * 3072 + lane * 16);
    auto red_give = [&](const f32x16& a, int g4, int part) {
        const u32 sl = g4 == kq ? 24576u : (u32)(td * 12288 + g4 * 3072 + (kq > g4 ? kq - 1 : kq) * 1024);
        *lds<f32x4>(sl + l16 + part * 32768) = f32x4{a[4 * g4], a[4 * g4 + 1], a[4 * g4 + 2], a[4 * g4 + 3]};
    };
    auto red_take = [&](const f32x16& a, int part) {
        const f32x4 p0 = *lds<f32x4>(red_rd + part * 32768), p1 = *lds<f32x4>(red_rd + part * 32768 + 1024), p2 = *lds<f32x4>(red_rd + part * 32768 + 2048);
        f32x4 own;
#pragma unroll
        for (int e = 0; e < 4; ++e) own[e] = kq == 0 ? a[e] : kq == 1 ? a[4 + e] : kq == 2 ? a[8 + e] : a[12 + e];
        const f32x4 v = ((own + p0) + (p1 + p2)) * rs[part];
        const u32 f01 = pk2(v[0], v[1]), f23 = pk2(v[2], v[3]);
        __builtin_amdgcn_raw_buffer_store_b64(v2u{pk2(bflo(hres[part][0]) + bflo(f01), bfhi(hres[part][0]) + bfhi(f01)), pk2(bflo(hres[part][1]) + bflo(f23), bfhi(hres[part][1]) + bfhi(f23))}, rsO, op + part * 1024 * D_ * 2, 0, OUTAUX);
    };
    {
        v4u hf[4];
        u32 gatev = 0;
#pragma unroll
        for (int u = 0; u < 3; ++u) hf[u] = hh_rd(u);
#pragma unroll
        for (int u = 0; u < 28; ++u) {
            SB0();
            if (u < 4) hh_dma(20 + 2 * u);
            if (u == 6) { vmw<4>(); bar(); if (tid == 0) __hip_atomic_store((GAS u32*)(ws + WS_F3 + 16384 + (P * 16 + S) * 64), tag, RLX_AGENT); }
            if (u == 17) { vmw<0>(); bar(); }
            SB0();
            if (u + 3 < 28) hf[(u + 3) & 3] = hh_rd(u + 3);
            if (u == 22 && w == 0) gatev = __builtin_amdgcn_raw_buffer_load_b32(rsW, f3_off + 16384, 0, SC1);
            o2[0] = mfma32(Wd[u], hf[u & 3], o2[0]);
        }
        SB0();
        if (w == 0) { if (!__all(gatev == tag)) gate_spin(f3_off + 16384, true); }
        bar();
        STAMP(13);
#pragma unroll
        for (int u = 28; u < 40; u += 2) hh_dma(u);
        vmw<3>(); bar();
#pragma unroll
        for (int u = 0; u < 3; ++u) hf[u] = hh_rd(28 + u);
#pragma unroll
        for (int u = 0; u < 28; ++u) {
            SB0();
            if (u == 3) { vmw<3>(); bar(); }
            if (u == 9) { vmw<5>(); bar(); }
            if (u == 15) { vmw<2>(); bar(); }
            if (u == 21) { vmw<0>(); bar(); }
            if (u < 8) hh_dma(40 + 2 * u);
            SB0();
            if (u + 3 < 28) hf[(u + 3) & 3] = hh_rd(28 + u + 3);
            o2[1] = mfma32(Wd[u], hf[u & 3], o2[1]);
        }
        SB0();
        bar();
    }
    STAMP(14);
    CSTAMP(29);
#pragma unroll
    for (int g4 = 0; g4 < 4; ++g4) { red_give(o2[0], g4, 0); red_give(o2[1], g4, 1); }
    bar();
    red_take(o2[0], 0);
    red_take(o2[1], 1);
    STAMP(15);
    STAMP_FLUSH;
    } else {
        publish_xg();
        const int hb = (j - 28) * 8 + g;
        float cs[8];
        {
            const u16* wsrc = hb < 16 ? wg : wu;
            const int c0 = 112 * (hb & 15) + 8 * (tid % 14), r0 = tid / 14;
#pragma unroll
            for (int e = 0; e < 8; ++e) cs[e] = 0.f;
            v4u t[13];
#pragma unroll
            for (int i = 0; i < 13; ++i) { const int r = r0 + 36 * i; t[i] = (tid < 504 && r < 448) ? *(const v4u*)(wsrc + (size_t)r * F_ + c0) : v4u{0, 0, 0, 0}; }
#pragma unroll
            for (int i = 0; i < 13; ++i) { cs[0] += bflo(t[i].x); cs[1] += bfhi(t[i].x); cs[2] += bflo(t[i].y); cs[3] += bfhi(t[i].y); cs[4] += bflo(t[i].z); cs[5] += bfhi(t[i].z); cs[6] += bflo(t[i].w); cs[7] += bfhi(t[i].w); }
        }
        vmw<0>(); bar();
#pragma unroll
        for (int k = 0; k < 7; ++k) {
            const int gid = hb + 32 * k;
            if (gid < 196) {
                const int dt_ = gid / 28, fg = gid % 28, fsl = w >> 1, td = w & 1;
                u32 ad[2];
#pragma unroll
                for (int t = 0; t < 2; ++t) { const u32 row = 16 * fsl + 8 * t + 4 * trh + trq, chk = 4 * td + 2 * trb + (trp >> 1); ad[t] = k * 8192 + row * 128 + 16 * (chk ^ (((row >> 1) & 1) << 2)) + 8 * (trp & 1); }
                const v4u f = tr_frag(ad[0], ad[1]);
                __builtin_amdgcn_raw_buffer_store_b128(f, rsW, WS_WD + ((dt_ * 2 + td) * 112 + 4 * fg + fsl) * 1024 + lane * 16, 0, SC1);
            }
        }
        if (tid < 504) {
            *lds<f32x4>(65536 + tid * 32) = f32x4{cs[0], cs[1], cs[2], cs[3]};
            *lds<f32x4>(65536 + tid * 32 + 16) = f32x4{cs[4], cs[5], cs[6], cs[7]};
        }
        bar();
        if (tid < 112) {
            float s = 0.f;
            for (int r = 0; r < 36; ++r) s += *lds<float>(65536 + (r * 14 + (tid >> 3)) * 32 + (tid & 7) * 4);
            __hip_atomic_store((GAS u32*)(ws + WS_CS) + ((hb < 16 ? 0 : 1792) + 112 * (hb & 15) + tid), __float_as_uint(s), RLX_AGENT);
        }
        vmw<0>(); bar();
        if (tid == 0) __hip_atomic_store((GAS u32*)(ws + WS_WF + hb * 64), tag, RLX_AGENT);

    }
}

extern "C" void kernel_launch(void* const* d_in, const int* in_sizes, int n_in, void* d_out, int out_size, void* d_ws, size_t ws_size, hipStream_t stream) {
    (void)in_sizes; (void)n_in; (void)out_size; (void)ws_size;
    (void)hipFuncSetAttribute((const void*)fused_kernel, hipFuncAttributeMaxDynamicSharedMemorySize, LDS_BYTES);
    fused_kernel<<<256, 512, LDS_BYTES, stream>>>((const u16*)d_in[0], (const u16*)d_in[1], (const u16*)d_in[2], (const u16*)d_in[3], (const u16*)d_in[4],
        (const u16*)d_in[5], (const u16*)d_in[6], (const u16*)d_in[7], (const u16*)d_in[8], (const u16*)d_in[10], (const u16*)d_in[11], (const u16*)d_in[12],
        (u16*)d_out, (char*)d_ws);
}
```

```cpp
#include <hip/hip_runtime.h>
#include <stdint.h>

typedef unsigned short u16;
typedef unsigned u32;
typedef unsigned long long u64;
typedef u32 v4u __attribute__((ext_vector_type(4)));
typedef u32 v2u __attribute__((ext_vector_type(2)));
typedef float f32x4 __attribute__((ext_vector_type(4)));
typedef float f32x16 __attribute__((ext_vector_type(16)));
typedef float f2 __attribute__((ext_vector_type(2)));
typedef __bf16 bf16x8 __attribute__((ext_vector_type(8)));
typedef __bf16 bf16x2 __attribute__((ext_vector_type(2)));
typedef short s16x4 __attribute__((ext_vector_type(4)));
#define LAS __attribute__((address_space(3)))
#define GAS __attribute__((address_space(1)))
#define DI __device__ __forceinline__
#define RLX_AGENT __ATOMIC_RELAXED, __HIP_MEMORY_SCOPE_AGENT
#define SC1 16

constexpr int L_ = 2048, D_ = 448, N_ = 256, F_ = 1792;
constexpr u32 WS_EP = 0;
constexpr u32 WS_SG = 4096;
constexpr u32 WS_WF = 32768;
constexpr u32 WS_CS = 40960;
constexpr u32 WS_HT = 65536;
constexpr u32 WS_WR = 1966080;
constexpr u32 WS_HH = 2u << 20;
constexpr u32 WS_WG = 10u << 20;
constexpr u32 WS_WD = 14u << 20;
constexpr u32 WS_XG = 16u << 20;
constexpr u32 WS_F2 = (23u << 20) + (512u << 10);
constexpr u32 WS_F3 = WS_F2 + 2 * 8 * 16384;
constexpr u32 WS_END = 24u << 20;
constexpr int CODEPF = 26;
constexpr u32 SPIN_CAP = 1u << 16;

constexpr u32 LDS_BYTES = 163840;
constexpr u32 FM = 0, FM_ROW = 1056, FM_CH = 53 * FM_ROW;
constexpr u32 FT = 2 * FM_CH, FT_CH = 772 * 4;
constexpr u32 EOL = FT + 2 * FT_CH;
constexpr u32 RT = 124288;
constexpr u32 PRM = RT + 16384;
constexpr u32 XT = PRM + 8192;
constexpr u32 KC = 0, KC_COPY = 4160, KC_CH = 4 * KC_COPY;
constexpr u32 CX = 2 * KC_CH;
constexpr u32 UU = RT, UU_CH = (256 + 2048) * 2;
constexpr u32 XS = UU + 2 * UU_CH;
static_assert(EOL % 16 == 0 && EOL + 6144 <= RT && XS + 8192 <= XT && XT + 8 * 912 <= 156672, "LDS map");
constexpr u32 BT_PART = 448 * 128;
constexpr u32 BTA = CX + 32768, BTB = 0;
constexpr u32 RSP = BTA + BT_PART;
constexpr u32 SM = 155648;
constexpr u32 SPQ = SM;
constexpr u32 CSL = SPQ + 4096;
static_assert(BTA + BT_PART <= RT && RSP + 7 * 4096 <= SM && CSL + 1024 <= 161280, "LDS map");
constexpr u32 hh_slot(int u) {
    return u < 14 ? BTA + u * 4096 : u < 21 ? RSP + (u - 14) * 4096 : u < 35 ? BTB + (u - 21) * 4096 : u < 49 ? BTA + (u - 35) * 4096 : RSP + (u - 49) * 4096;
}

struct EoTab { u16 v[256 * 12]; };
constexpr u32 foff_c(int j) {
    if (j < 0) return 768u;
    u32 t = (u32)j;
    int s = t >= 1024 ? 3 : t >= 512 ? 2 : t >= 256 ? 1 : 0;
    if (s > 0) t = ((t + (1u << (s - 1)) - 1u + ((t >> s) & 1u)) >> s) << s;
    u32 col = (t >> 3) & 31u, row = 0;
    if (t < 256u) row = t & 7u;
    else if (t < 512u) row = 8u + ((t >> 1) & 3u);
    else if (t < 1024u) row = 12u + 2u * ((t >> 8) & 1u) + ((t >> 2) & 1u);
    else if (t < 2048u) row = 16u + ((t >> 8) & 3u);
    else row = 20u;
    return row * 32u + col;
}
constexpr EoTab make_eo() { EoTab e{}; for (int t = 0; t < 256; ++t) for (int k = 0; k < 12; ++k) e.v[t * 12 + k] = (u16)(k < 11 ? foff_c(2047 - 8 * t - k) * 4u : 0u); return e; }
__device__ const __attribute__((aligned(16))) EoTab EO = make_eo();

DI float bflo(u32 d) { return __uint_as_float(d << 16); }
DI float bfhi(u32 d) { return __uint_as_float(d & 0xffff0000u); }
DI u32 pk2(float lo, float hi) { bf16x2 v = {(__bf16)lo, (__bf16)hi}; return __builtin_bit_cast(u32, v); }
DI float rbf(float f) { return bflo(pk2(f, f)); }
DI bf16x8 as_bf(v4u v) { return __builtin_bit_cast(bf16x8, v); }
DI f32x4 mfma16(v4u a, v4u b, f32x4 c) { return __builtin_amdgcn_mfma_f32_16x16x32_bf16(as_bf(a), as_bf(b), c, 0, 0, 0); }
DI f32x16 mfma32(v4u a, v4u b, f32x16 c) { return __builtin_amdgcn_mfma_f32_32x32x16_bf16(as_bf(a), as_bf(b), c, 0, 0, 0); }
template <class T> DI LAS T* lds(u32 off) { return (LAS T*)(uintptr_t)off; }
DI v2u tr_read(u32 off) { s16x4 r = __builtin_amdgcn_ds_read_tr16_b64_v4i16(lds<s16x4>(off)); return __builtin_bit_cast(v2u, r); }
DI v4u tr_frag(u32 off0, u32 off1) { v2u a = tr_read(off0), b = tr_read(off1); return v4u{a.x, a.y, b.x, b.y}; }
template <int CTRL, int RM> DI float dpp_add(float v) { int t = __builtin_amdgcn_update_dpp(0, __float_as_int(v), CTRL, RM, 0xf, true); return v + __int_as_float(t); }
DI float wave_sum(float v) {
    v = dpp_add<0x111, 0xf>(v); v = dpp_add<0x112, 0xf>(v); v = dpp_add<0x114, 0xf>(v); v = dpp_add<0x118, 0xf>(v);
    v = dpp_add<0x142, 0xa>(v); v = dpp_add<0x143, 0xc>(v);
    return __int_as_float(__builtin_amdgcn_readlane(__float_as_int(v), 63));
}
DI float half_sum(float v) { const u32 u = __float_as_uint(v); const auto r = __builtin_amdgcn_permlane32_swap(u, u, false, false); return __uint_as_float(r[0]) + __uint_as_float(r[1]); }
DI void fence() { asm volatile("" ::: "memory"); }
DI void bar() { asm volatile("s_waitcnt lgkmcnt(0)" ::: "memory"); __builtin_amdgcn_s_barrier(); asm volatile("" ::: "memory"); }
template <int N> DI void vmw() { asm volatile("s_waitcnt vmcnt(%0)" :: "n"(N) : "memory"); }
template <bool S1> DI void glds16(u32 voff, const void* sbase, u32 ldst) {
    u32 keep;
    if (S1) asm volatile("s_mov_b32 %0, m0\n\ts_mov_b32 m0, %3\n\ts_nop 0\n\tglobal_load_lds_dwordx4 %1, %2 sc1\n\ts_mov_b32 m0, %0" : "=&s"(keep) : "v"(voff), "s"(sbase), "s"(ldst) : "memory");
    else    asm volatile("s_mov_b32 %0, m0\n\ts_mov_b32 m0, %3\n\ts_nop 0\n\tglobal_load_lds_dwordx4 %1, %2\n\ts_mov_b32 m0, %0" : "=&s"(keep) : "v"(voff), "s"(sbase), "s"(ldst) : "memory");
}
DI void glds16v(const void* gaddr, u32 ldst) {
    u32 keep;
    asm volatile("s_mov_b32 %0, m0\n\ts_mov_b32 m0, %2\n\ts_nop 0\n\tglobal_load_lds_dwordx4 %1, off\n\ts_mov_b32 m0, %0" : "=&s"(keep) : "v"(gaddr), "s"(ldst) : "memory");
}
DI float dot2(u32 a, u32 b, float c) { return __builtin_amdgcn_fdot2_f32_bf16(__builtin_bit_cast(bf16x2, a), __builtin_bit_cast(bf16x2, b), c, false); }
DI f2 cmul(f2 a, f2 b) { f2 t = f2{a.x, a.x} * b; return __builtin_elementwise_fma(f2{a.y, a.y}, f2{-b.y, b.x}, t); }
DI f2 cexp_rev(float ar, float ai, float t) {
    const float e = __builtin_amdgcn_exp2f(ar * t), ph = __builtin_amdgcn_fractf(ai * t);
    return f2{e * __builtin_amdgcn_cosf(ph), e * __builtin_amdgcn_sinf(ph)};
}
DI u32 swz_b(u32 row) { return ((row & 3u) << 2) | ((row >> 2) & 3u); }
DI u32 off_b(u32 row, u32 ch) { return 256u * row + 16u * (ch ^ swz_b(row)); }
DI u32 off_h(u32 row, u32 ch) { return 1024u * (row >> 3) + 512u * (ch >> 2) + 64u * (row & 7u) + 16u * ((ch & 3u) ^ ((row >> 2) & 3u)); }
DI u32 ln_pack(float mean, float var) {
    const float m = rbf(mean), sd = rbf(__builtin_amdgcn_sqrtf(rbf(rbf(var) + 1e-5f)));
    return (__float_as_uint(m) >> 16) | (__float_as_uint(sd) & 0xffff0000u);
}

#define SGB(mask, n) __builtin_amdgcn_sched_group_barrier(mask, n, 0)
#define SB0() __builtin_amdgcn_sched_barrier(0)
constexpr int M_MFMA = 0x8, M_VMEM = 0x10, M_DSR = 0x100, M_VALU = 0x2;
DI u32 opaque(u32 v) { asm volatile("" : "+v"(v)); return v; }

__global__ void __launch_bounds__(512, 2)
fused_kernel(const u16* __restrict__ x, const u16* __restrict__ Are, const u16* __restrict__ Aim, const u16* __restrict__ Bre, const u16* __restrict__ Bim,
             const u16* __restrict__ Cre, const u16* __restrict__ Cim, const u16* __restrict__ logdt, const u16* __restrict__ Dskip,
             const u16* __restrict__ wg, const u16* __restrict__ wu, const u16* __restrict__ wd, u16* __restrict__ out, char* __restrict__ ws)
{
    const int tid = threadIdx.x, lane = tid & 63, w = __builtin_amdgcn_readfirstlane(tid >> 6);
    asm volatile("" :: "s"(x), "s"(Are), "s"(Aim), "s"(Bre), "s"(Bim), "s"(Cre), "s"(Cim), "s"(logdt), "s"(Dskip), "s"(wg), "s"(wu), "s"(wd), "s"(out), "s"(ws));
    const int b = blockIdx.x, g = b & 7, j = b >> 3;
    const bool mainb = j < 28;
    const int cm = mainb ? g * 28 + j : 0;
    const int P = 4 * (g >> 1) + j / 7, S = 7 * (g & 1) + j % 7;
    const __amdgpu_buffer_rsrc_t rsW = __builtin_amdgcn_make_buffer_rsrc(ws, 0, WS_END, 0x00020000);
    const __amdgpu_buffer_rsrc_t rsO = __builtin_amdgcn_make_buffer_rsrc((void*)out, 0, L_ * D_ * 2, 0x00020000);
    const u32 trq = (lane & 15) >> 2, trp = lane & 3, trh = lane >> 5, trb = (lane >> 4) & 1;
    {
        u64 pc; asm volatile("s_getpc_b64 %0" : "=s"(pc));
        pc &= ~255ull;
        for (int q = j * 8 + w; q < CODEPF; q += 256) glds16<false>((u32)(q * 1024 + lane * 16), (const void*)pc, 156672);
    }
    v4u xr; u64 ep; u32 p_ld2, p_dk2;
    asm volatile("global_load_dwordx4 %0, %1, %2" : "=v"(xr) : "v"((u32)((8 * b + w) * 896 + min(lane, 55) * 16)), "s"(x) : "memory");
    asm volatile("s_load_dwordx2 %0, %3, 0x0\n\ts_load_dword %1, %4, 0x0\n\ts_load_dword %2, %5, 0x0" : "=&s"(ep), "=&s"(p_ld2), "=&s"(p_dk2)
                 : "s"(ws + WS_EP), "s"((const char*)logdt + cm * 4), "s"((const char*)Dskip + cm * 4) : "memory");
    const int ch = w >> 2, dch = 2 * cm + ch;
    if (mainb) {
        const int ai_ = lane >> 3;
        u64 src_ = (u64)Cim;
        src_ = ai_ == 4 ? (u64)Cre : src_; src_ = ai_ == 3 ? (u64)Bim : src_; src_ = ai_ == 2 ? (u64)Bre : src_; src_ = ai_ == 1 ? (u64)Aim : src_; src_ = ai_ == 0 ? (u64)Are : src_;
        const u16* src = (const u16*)src_;
        glds16v(src + dch * N_ + (w & 3) * 64 + (lane & 7) * 8, PRM + w * 1024);
        const int ew = w < 6 ? w : w - 6;
        glds16<false>((u32)(ew * 1024 + lane * 16), (const void*)&EO, EOL + ew * 1024);
    } else {
        const int hb = (j - 28) * 8 + g;
#pragma unroll
        for (int k = 0; k < 7; ++k) {
            const int gid = min(hb + 32 * k, 195);
            const int dt_ = gid / 28, fg = gid % 28;
            const u32 row = 8 * w + (lane >> 3), chk = (lane & 7) ^ (((row >> 1) & 1) << 2);
            glds16<false>((u32)((64 * fg + row) * (D_ * 2) + dt_ * 128 + chk * 16), (const void*)wd, k * 8192 + w * 1024);
        }
    }
    asm volatile("s_cmp_ge_i32 %1, 28\n\ts_cbranch_scc1 1f\n\ts_waitcnt vmcnt(2)\n\ts_branch 2f\n1:\n\ts_waitcnt vmcnt(7)\n2:" : "+v"(xr) : "s"(__builtin_amdgcn_readfirstlane(j)) : "memory", "scc");
    asm volatile("s_waitcnt lgkmcnt(0)" : "+s"(ep), "+s"(p_ld2), "+s"(p_dk2) :: "memory");
    ep += 1ull;
    const u32 p_ldt = ch ? p_ld2 >> 16 : p_ld2 & 0xffffu, p_dsk = ch ? p_dk2 >> 16 : p_dk2 & 0xffffu;
    if (lane >= 56) xr = v4u{0, 0, 0, 0};
    const u32 tag = (u32)ep, tagx = (u32)(ep >> 32) << 16;
    if (lane < 56) *lds<v4u>(XT + w * 912 + lane * 16) = xr;
    {
        const float v[8] = {bflo(xr.x), bfhi(xr.x), bflo(xr.y), bfhi(xr.y), bflo(xr.z), bfhi(xr.z), bflo(xr.w), bfhi(xr.w)};
        const float mean = wave_sum(((v[0] + v[1]) + (v[2] + v[3])) + ((v[4] + v[5]) + (v[6] + v[7]))) * (1.f / 448.f);
        float q = 0.f;
#pragma unroll
        for (int e = 0; e < 8; ++e) { const float dl = v[e] - mean; q += dl * dl; }
        q = lane < 56 ? q : 0.f;
        const u32 ln1 = ln_pack(mean, wave_sum(q) * (1.f / 448.f));
        if (lane == 0) __hip_atomic_store((GAS u64*)(ws + WS_SG) + (8 * b + w), ((u64)tag << 32) | ln1, RLX_AGENT);
    }
    const u32 km0 = FM + ch * FM_CH + (w & 3) * 256;
    f2 Ga, Gb, Gc, Gd, KX, KY, G4, G4r, E1, E1r, E256, E256r, E2, E2r, E4, E4r;
#define M1(a, bb) (f2{(a).x, (a).x} * (bb))
#define M2(a, br, t) __builtin_elementwise_fma(f2{(a).y, (a).y}, (br), (t))
#define WROW1(r, v) do { u32 keep_; asm volatile("s_mov_b32 %0, m0\n\ts_mov_b32 m0, %2\n\ts_nop 0\n\tds_write_addtid_b32 %1 offset:%3\n\ts_mov_b32 m0, %0" : "=&s"(keep_) : "v"(v), "s"(km0), "n"((r) * FM_ROW) : "memory"); } while (0)
#define WROW6(r0, v0, r1, v1, r2, v2, r3, v3, r4, v4, r5, v5) do { u32 keep_; asm volatile("s_mov_b32 %0, m0\n\ts_mov_b32 m0, %7\n\ts_nop 0\n\t" \
        "ds_write_addtid_b32 %1 offset:%8\n\tds_write_addtid_b32 %2 offset:%9\n\tds_write_addtid_b32 %3 offset:%10\n\tds_write_addtid_b32 %4 offset:%11\n\tds_write_addtid_b32 %5 offset:%12\n\tds_write_addtid_b32 %6 offset:%13\n\ts_mov_b32 m0, %0" \
        : "=&s"(keep_) : "v"(v0), "v"(v1), "v"(v2), "v"(v3), "v"(v4), "v"(v5), "s"(km0), "n"((r0) * FM_ROW), "n"((r1) * FM_ROW), "n"((r2) * FM_ROW), "n"((r3) * FM_ROW), "n"((r4) * FM_ROW), "n"((r5) * FM_ROW) : "memory"); } while (0)
#define WA(r, X) WROW1(r, pk2((X).x, -(X).y))
#define KSTEP(t) do { SB0(); \
        const f2 ta = M1(Ga, G4), tb = M1(Gb, G4), tc = M1(Gc, G4), td_ = M1(Gd, G4), tx = M1(KX, E1), ty = M1(KY, E256); \
        const u32 c0 = pk2(Ga.x, Ga.y), c1 = pk2(Gb.x, Gb.y), c2 = pk2(Gc.x, Gc.y), c3 = pk2(Gd.x, Gd.y), c4 = pk2(KX.x, -KX.y), c5 = pk2(KY.x, -KY.y); \
        f2 ts0, ts1; \
        if ((t) == 0) ts0 = M1(KY, E2); \
        if ((t) == 1 || (t) == 2) ts0 = M1(KY, E4); \
        SB0(); \
        WROW6(21 + 4 * (t), c0, 22 + 4 * (t), c1, 23 + 4 * (t), c2, 24 + 4 * (t), c3, (t), c4, ((t) == 0 ? 8 : (t) == 1 ? 12 : (t) == 2 ? 14 : 13 + (t)), c5); \
        if ((t) == 0) { f2 Z = M2(KY, E2r, ts0); WA(9, Z); ts1 = M1(Z, E2); Z = M2(Z, E2r, ts1); WA(10, Z); ts1 = M1(Z, E2); Z = M2(Z, E2r, ts1); WA(11, Z); } \
        if ((t) == 1) { const f2 V = M2(KY, E4r, ts0); WA(13, V); } \
        if ((t) == 2) { const f2 V = M2(KY, E4r, ts0); WA(15, V); } \
        if ((t) < 7) { Ga = M2(Ga, G4r, ta); Gb = M2(Gb, G4r, tb); Gc = M2(Gc, G4r, tc); Gd = M2(Gd, G4r, td_); KX = M2(KX, E1r, tx); KY = M2(KY, E256r, ty); } \
    } while (0)
    bar();
    auto publish_xg = [&]() {
        u32 x0[4], x1[4];
#pragma unroll
        for (int k = 0; k < 4; ++k) {
            const int id = min(tid + 512 * k, 1791), c = id >> 2, i2 = (id & 3) * 2;
            x0[k] = *lds<u16>(XT + i2 * 912 + c * 2); x1[k] = *lds<u16>(XT + (i2 + 1) * 912 + c * 2);
        }
        SB0();
#pragma unroll
        for (int k = 0; k < 4; ++k)
            __builtin_amdgcn_raw_buffer_store_b128(v4u{x0[k] | tagx, tag, x1[k] | tagx, tag}, rsW, (tid + 512 * k < 1792) ? (u32)(WS_XG + b * 28672 + (tid + 512 * k) * 16) : 0x7ffffff0u, 0, SC1);
    };
    if (mainb) {
    publish_xg();
    {
        vmw<5>();
        {
            const int bb_ = b < 196 ? b : b - 196;
    #pragma unroll
            for (int i = 0; i < 2; ++i) {
                const int id = 2 * bb_ + i, Sp = id / 28, s = id % 28;
                const u32 row = (w & 3) * 4 + (lane >> 4), chk = (lane & 15) ^ swz_b(row);
                glds16<false>((u32)((16 * s + row) * (F_ * 2) + Sp * 256 + chk * 16), (w >> 2) ? (const void*)wu : (const void*)wg, RT + i * 8192 + w * 1024);
            }
        }
        const u32 pa_ = PRM + w * 1024 + lane * 2;
        const u32 p_are = *lds<u16>(pa_), p_aim = *lds<u16>(pa_ + 128), p_bre = *lds<u16>(pa_ + 256), p_bim = *lds<u16>(pa_ + 384), p_cre = *lds<u16>(pa_ + 512), p_cim = *lds<u16>(pa_ + 640);
        const float dt = rbf(expf(bflo(p_ldt)));
        const float zr = dt * bflo(p_are), zi = dt * bflo(p_aim);
        const float ar = zr * 1.4426950408889634f, ai = zi * 0.15915494309189535f;
        const f2 CB = cmul(f2{bflo(p_cre), bflo(p_cim)}, f2{dt * bflo(p_bre), dt * bflo(p_bim)});
        E1 = cexp_rev(ar, ai, 1.f); E256 = cexp_rev(ar, ai, 256.f);
        const f2 G1 = cexp_rev(ar, ai, 8.f);
        E2 = cmul(E1, E1); E4 = cmul(E2, E2);
        const f2 G2 = cmul(G1, G1);
        G4 = cmul(G2, G2);
        E1r = f2{-E1.y, E1.x}; E2r = f2{-E2.y, E2.x}; E4r = f2{-E4.y, E4.x}; E256r = f2{-E256.y, E256.x}; G4r = f2{-G4.y, G4.x};
        Ga = f2{1.f, 0.f}; Gb = G1; Gc = G2; Gd = cmul(G2, G1);
        KX = CB; KY = cmul(CB, E256);
        SB0();
    }
    KSTEP(0); KSTEP(1); KSTEP(2);
    SB0();
    fence();
    {
        KSTEP(3); KSTEP(4); KSTEP(5); KSTEP(6); KSTEP(7);
        SB0();
#undef KSTEP
#undef WA
#undef WROW1
#undef WROW6
#undef M1
#undef M2
        vmw<0>();
        bar();
#pragma unroll
        for (int i = 0; i < 2; ++i) {
            const int id = 2 * b + i, Sp = id / 28, s = id % 28;
            const u32 base = RT + i * 8192 + trb * 4096 + 8 * (trp & 1);
            const v4u f = tr_frag(base + off_b(8 * trh + trq, 2 * w + (trp >> 1)), base + off_b(8 * trh + 4 + trq, 2 * w + (trp >> 1)));
            __builtin_amdgcn_raw_buffer_store_b128(f, rsW, (b < 196 ? (u32)(WS_WG + ((Sp * 8 + w) * 28 + s) * 1024) : 0x7ffffc00u) + lane * 16, 0, SC1);
        }
        const u32 xa = WS_XG + (32 * w + (lane >> 3)) * 28672 + cm * 128 + (lane & 7) * 16, sa = WS_SG + (32 * w + (lane >> 3)) * 64 + (lane & 3) * 16;
        v4u xg[4], sg[4];
        const int ta_ = (w >> 1) & 1, tb_ = w & 1;
        const u32 fa_ = opaque(FM + ch * FM_CH + min(16 * ta_ + (lane & 15), 20) * FM_ROW + (lane >> 4) * 16), fb_ = opaque(FM + ch * FM_CH + (21 + 16 * tb_ + (lane & 15)) * FM_ROW + (lane >> 4) * 16);
        f32x4 f0 = {0.f, 0.f, 0.f, 0.f}, f1 = {0.f, 0.f, 0.f, 0.f};
        v4u A[16], B[16];
#pragma unroll
        for (int ks = 0; ks < 16; ++ks) { A[ks] = *lds<v4u>(fa_ + ks * 64); B[ks] = *lds<v4u>(fb_ + ks * 64); }
        SB0();
#pragma unroll
        for (int k = 0; k < 4; ++k) { xg[k] = __builtin_amdgcn_raw_buffer_load_b128(rsW, xa + k * 8 * 28672, 0, SC1); sg[k] = __builtin_amdgcn_raw_buffer_load_b128(rsW, sa + k * 512, 0, SC1); }
        SB0();
#pragma unroll
        for (int ks = 0; ks < 16; ks += 2) { f0 = mfma16(A[ks], B[ks], f0); f1 = mfma16(A[ks + 1], B[ks + 1], f1); }
        f0 += f1;
#pragma unroll
        for (int r = 0; r < 4; ++r) { const int row = 16 * ta_ + 4 * (lane >> 4) + r; if (row < 24) *lds<float>(FT + ch * FT_CH + (row * 32 + 16 * tb_ + (lane & 15)) * 4) = f0[r]; }
        if ((tid & 255) == 255) *lds<float>(FT + ch * FT_CH + 768 * 4) = 0.f;
        for (u32 spin = 0;; ++spin) {
            u32 bad = 0;
#pragma unroll
            for (int k = 0; k < 4; ++k) bad |= ((sg[k].y ^ tag) | (sg[k].w ^ tag) | (xg[k].y ^ tag)) | ((xg[k].w ^ tag) | (((xg[k].x ^ tagx) | (xg[k].z ^ tagx)) & 0xffff0000u));
            if (__all(bad == 0) || spin > SPIN_CAP) break;
            __builtin_amdgcn_s_sleep(4);
#pragma unroll
            for (int k = 0; k < 4; ++k) { xg[k] = __builtin_amdgcn_raw_buffer_load_b128(rsW, xa + k * 8 * 28672, 0, SC1); sg[k] = __builtin_amdgcn_raw_buffer_load_b128(rsW, sa + k * 512, 0, SC1); }
        }
        const u32 ua = opaque(UU + ((lane >> 2) & 1) * UU_CH + 512 + (32 * w + (lane >> 3)) * 16 + (lane & 3) * 4), xsa = opaque(XS + ((lane >> 2) & 1) * 4096 + (32 * w + (lane >> 3)) * 16 + (lane & 3) * 4);
#pragma unroll
        for (int k = 0; k < 4; ++k) {
            const u32 xm = pk2(bflo(xg[k].x) - bflo(sg[k].x), bflo(xg[k].z) - bflo(sg[k].z));
            const float u0 = bflo(xm) * __builtin_amdgcn_rcpf(bfhi(sg[k].x)), u1 = bfhi(xm) * __builtin_amdgcn_rcpf(bfhi(sg[k].z));
            *lds<u32>(ua + k * 128) = pk2(u0, u1);
            *lds<u32>(xsa + k * 128) = __builtin_amdgcn_perm(xg[k].z, xg[k].x, 0x05040100u);
        }
    }
    if (tid >= 64 && tid < 128) *lds<v4u>(UU + ((tid >> 5) & 1) * UU_CH + (tid & 31) * 16) = v4u{0, 0, 0, 0};
    const u32 eb_ = opaque(EOL + (tid & 255) * 24);
    const v2u ea = *lds<v2u>(eb_), eb = *lds<v2u>(eb_ + 8), ec = *lds<v2u>(eb_ + 16);
    vmw<0>();
    bar();
    if (tid == 0 && b < 196) __hip_atomic_store((GAS u32*)(ws + WS_WR + b * 64), tag, RLX_AGENT);
    const int rq = w & 3;
    const u32 cb = lane & 15, cq = lane >> 4;
    const u32 ubase = opaque(UU + ch * UU_CH + 512 + 32 * (cb - (cq >> 1)) + 16 * (cq & 1) - 32 * 4 * rq - 32 * 2);
    auto ldB = [&](int k, int rr) { return *lds<v4u>(ubase + 32 * (16 * k - 2 * rr + 2)); };
    v4u Bf[8], Bn[8];
#pragma unroll
    for (int p = 0; p < 4; ++p) { Bf[p] = ldB(p, 0); Bn[p] = ldB(p, 1); }
    {
        const u32 fb_ = FT + ch * FT_CH;
        const u32 eo[11] = {ea.x & 0xffffu, ea.x >> 16, ea.y & 0xffffu, ea.y >> 16, eb.x & 0xffffu, eb.x >> 16, eb.y & 0xffffu, eb.y >> 16, ec.x & 0xffffu, ec.x >> 16, ec.y & 0xffffu};
        float v[11];
#pragma unroll
        for (int e = 0; e < 11; ++e) v[e] = *lds<float>(fb_ + eo[e]);
        SB0();
        u32 p[10];
#pragma unroll
        for (int e = 0; e < 10; ++e) p[e] = pk2(v[e], v[e + 1]);
        const u32 ka_ = opaque(KC + ch * KC_CH + (tid & 255) * 16);
#pragma unroll
        for (int s = 0; s < 4; ++s) *lds<v4u>(ka_ + s * KC_COPY) = v4u{p[s], p[s + 2], p[s + 4], p[s + 6]};
    }
    if (tid < 16) *lds<v4u>(KC + (tid >> 3) * KC_CH + ((tid >> 1) & 3) * KC_COPY + 4096 + (tid & 1) * 16) = v4u{0, 0, 0, 0};
    bar();
    v4u Wf[28];
    const u32 l16 = lane * 16;
    const int wf_off = WS_WG + (S * 8 + w) * 28 * 1024;
    u32 f2a;
    const float dsv = bflo(p_dsk);
    const u32 f2_off = WS_F2 + g * 16384 + (tid < 224 ? tid : 0) * 64;
    const u32 bt_src = (8 * w + ((lane >> 2) & 7)) * 4096 + 128 * P + (4 * (lane >> 5) + ((lane & 3) ^ ((2 * w + ((lane >> 4) & 1)) & 3))) * 16;
    auto bt_piece = [&](int part, int k) { glds16<true>(bt_src + part * 2048 + k * 64 * 4096, ws + WS_HT, (part ? BTB : BTA) + (8 * k + w) * 1024); };
    {
        const u32 cs = (3u - cb) & 3u;
        const u32 kbase = opaque(KC + ch * KC_CH + cs * KC_COPY + 2 * (2047 - 16 * (cq >> 1) - cb + 8 * (cq & 1) - cs) - 32 * 4 * rq - 32 * 114);
        const u32 kbase8 = opaque(kbase + 8);
        auto ldA = [&](int p, int rr) { const v2u lo = *lds<volatile v2u>(kbase + 32 * (114 - (16 * p + 2 * rr))), hi = *lds<volatile v2u>(kbase8 + 32 * (114 - (16 * p + 2 * rr))); return v4u{lo.x, lo.y, hi.x, hi.y}; };
        const u32 cxw = opaque(CX + (4 * ch * 4 + rq) * 1024 + lane * 16), cxr = opaque(CX + w * 4096 + lane * 16);
        const u32 fu_ = opaque(UU + ch * UU_CH + 512 + (256 * rq + 16 * cb + 4 * cq) * 2), fx_ = opaque(XS + ch * 4096 + (256 * rq + 16 * cb + 4 * cq) * 2);
        const u32 ht_ = WS_HT + dch * 4096 + (256 * rq + 16 * cb + 4 * cq) * 2;
        auto finish = [&](f32x4 (&acc)[4], int tbase) {
#pragma unroll
            for (int i = 0; i < 4; ++i) *lds<f32x4>(cxw + i * 4096) = acc[i];
            const v2u uu = *lds<v2u>(fu_ + tbase * 512), xx = *lds<v2u>(fx_ + tbase * 512);
            bar();
            if (tbase == 4) f2a = __builtin_amdgcn_raw_buffer_load_b32(rsW, w < 4 ? (tid < 224 ? f2_off : (u32)(WS_WF + (tid - 224) * 64)) : f2_off, 0, SC1);
            const f32x4 p0 = *lds<f32x4>(cxr), p1 = *lds<f32x4>(cxr + 1024), p2 = *lds<f32x4>(cxr + 2048), p3 = *lds<f32x4>(cxr + 3072);
            const float uv[4] = {bflo(uu.x), bfhi(uu.x), bflo(uu.y), bfhi(uu.y)}, xv[4] = {bflo(xx.x), bfhi(xx.x), bflo(xx.y), bfhi(xx.y)};
            const u32 k01 = pk2(uv[0] * dsv, uv[1] * dsv), k23 = pk2(uv[2] * dsv, uv[3] * dsv);
            SB0();
            const f32x4 y = (p0 + p1) + (p2 + p3);
            const u32 y01 = pk2(y[0], y[1]), y23 = pk2(y[2], y[3]);
            const u32 z01 = pk2(bflo(y01) + bflo(k01), bfhi(y01) + bfhi(k01)), z23 = pk2(bflo(y23) + bflo(k23), bfhi(y23) + bfhi(k23));
            const float h[4] = {xv[0] + bflo(z01), xv[1] + bfhi(z01), xv[2] + bflo(z23), xv[3] + bfhi(z23)};
            __builtin_amdgcn_raw_buffer_store_b64(v2u{pk2(h[0], h[1]), pk2(h[2], h[3])}, rsW, ht_ + tbase * 512, 0, SC1);
        };
        f32x4 acc[4];
        v4u Af[8], An[8];
#pragma unroll
        for (int i = 0; i < 4; ++i) acc[i] = f32x4{0.f, 0.f, 0.f, 0.f};
#pragma unroll
        for (int p = 0; p < 4; ++p) { Af[p] = ldA(p, 0); An[p] = ldA(p, 1); }
        SB0();
#pragma unroll
        for (int p = 0; p < 4; ++p)
#pragma unroll
            for (int i = p; i < 4; ++i) acc[i] = mfma16(Af[p], Bf[i - p], acc[i]);
#pragma unroll
        for (int p = 0; p < 4; ++p)
#pragma unroll
            for (int i = p; i < 4; ++i) acc[i] = mfma16(An[p], Bn[i - p], acc[i]);
#pragma unroll
        for (int p = 4; p < 8; ++p) { Af[p] = ldA(p, 0); Bf[p] = ldB(p, 0); }
        SB0();
        finish(acc, 0);
#pragma unroll
        for (int i = 0; i < 4; ++i) acc[i] = f32x4{0.f, 0.f, 0.f, 0.f};
        SB0();
#pragma unroll
        for (int p = 4; p < 8; ++p) { An[p] = ldA(p, 1); Bn[p] = ldB(p, 1); }
        const u32 wr_off = WS_WR + (tid < 196 ? tid : 0) * 64;
        const u32 wr_flag = __builtin_amdgcn_raw_buffer_load_b32(rsW, wr_off, 0, SC1);
#pragma unroll
        for (int p = 0; p < 8; ++p)
#pragma unroll
            for (int i = 0; i < 4; ++i) if (p <= 4 + i) acc[i] = mfma16(Af[p], Bf[4 + i - p], acc[i]);
        SGB(M_VMEM, 1);
#pragma unroll
        for (int i = 0; i < 6; ++i) { SGB(M_MFMA, 2); SGB(M_DSR, 2); SGB(M_MFMA, 2); }
        SB0();
        if (!__all(wr_flag == tag)) {
            for (u32 spin = 0; spin < SPIN_CAP; ++spin) {
                if (__all(__builtin_amdgcn_raw_buffer_load_b32(rsW, wr_off, 0, SC1) == tag)) break;
                __builtin_amdgcn_s_sleep(4);
            }
        }
        vmw<0>();
        bar();
        if (tid < 8) __hip_atomic_store((GAS u32*)(ws + WS_F2 + tid * 16384 + cm * 64), tag, RLX_AGENT);
        SB0();
#pragma unroll
        for (int i = 0; i < 7; ++i) Wf[i] = __builtin_amdgcn_raw_buffer_load_b128(rsW, l16 + (i) * 1024 % 4096, wf_off + (i) * 1024 / 4096 * 4096, SC1);
#pragma unroll
        for (int p = 0; p < 8; ++p)
#pragma unroll
            for (int i = 0; i < 4; ++i) if (p <= 4 + i) acc[i] = mfma16(An[p], Bn[4 + i - p], acc[i]);
#pragma unroll
        for (int i = 0; i < 6; ++i) { SGB(M_MFMA, 4); SGB(M_VMEM, 1); }
        SGB(M_MFMA, 2); SGB(M_VMEM, 1);
        SB0();
#pragma unroll
        for (int i = 7; i < 14; ++i) Wf[i] = __builtin_amdgcn_raw_buffer_load_b128(rsW, l16 + i * 1024 % 4096, wf_off + i * 1024 / 4096 * 4096, SC1);
        finish(acc, 4);
    }
    auto gate_spin = [&](u32 off, bool active) {
        for (u32 spin = 0;; ++spin) {
            const u32 s0 = __builtin_amdgcn_raw_buffer_load_b32(rsW, off, 0, SC1);
            if (__all(s0 == tag || !active) || spin > SPIN_CAP) break;
            __builtin_amdgcn_s_sleep(8);
        }
    };
    if (w < 4 && !__all(f2a == tag)) gate_spin(tid < 224 ? f2_off : (u32)(WS_WF + (tid - 224) * 64), true);
    bar();
    if (b == 0 && tid == 0) __hip_atomic_store((GAS u64*)(ws + WS_EP), ep, RLX_AGENT);
    const float csum = __uint_as_float(__builtin_amdgcn_raw_buffer_load_b32(rsW, WS_CS + (((lane >> 4) & 1) * 1792 + 128 * S + 16 * w + (lane & 15)) * 4, 0, SC1));
    fence();
#pragma unroll
    for (int k = 0; k < 7; ++k) bt_piece(0, k);
    vmw<4>();
    bar();
    if (tid < 8) __hip_atomic_store((GAS u32*)(ws + WS_F2 + (8 + tid) * 16384 + cm * 64), tag, RLX_AGENT);
    v4u Wd[28];
    const int td = w & 1, kq = w >> 1, hp = S / 7, dtile = S % 7;
    const int wd_off = WS_WD + ((dtile * 2 + td) * 112 + 28 * kq) * 1024;
    const u32 trb0 = 1024 * trh + 64 * trq + 16 * ((2 * trb + (trp >> 1)) ^ ((2 * trh) & 3)) + 8 * (trp & 1), trb1 = 1024 * trh + 64 * (4 + trq) + 16 * ((2 * trb + (trp >> 1)) ^ ((2 * trh + 1) & 3)) + 8 * (trp & 1);
    const int hh_out = WS_HH + (P * 112 + 8 * S + w) * 4096;
    u32 hres[2][2]; float rs[2];
    const u32 f3_off = WS_F3 + (P * 16 + (lane < 14 ? lane : 0)) * 64;
    const char* hsrc = ws + WS_HH + (P * 112 + 28 * kq + td) * 4096 + hp * 1024; const u32 hdst = kq * 1024;
    auto hh_dma = [&](int u) { glds16<true>(l16, hsrc + (u % 28) * 4096 + (u >= 28 ? 2048 : 0), (td ? hh_slot(u + 1) : hh_slot(u)) + hdst); };
#define GEMM1_PASS(PART) do { \
        const u32 b0 = opaque(((PART) ? BTB : BTA) + trb0), b1 = opaque(((PART) ? BTB : BTA) + trb1); \
        const u32 s0_ = opaque(b0 + (w >> 1) * 2048 + (w & 1) * 512), s1_ = opaque(b1 + (w >> 1) * 2048 + (w & 1) * 512); \
        f32x16 acc[2]; \
        _Pragma("unroll") for (int i = 0; i < 16; ++i) { acc[0][i] = 0.f; acc[1][i] = 0.f; } \
        v4u Bq[3][2], Of; \
        float sa0 = 0.f, sa1 = 0.f, sq0 = 0.f, sq1 = 0.f; \
        u32 gatev = 0; \
        _Pragma("unroll") for (int t = 0; t < 2; ++t) { Bq[0][t] = tr_frag(b0 + 512 * t, b1 + 512 * t); Bq[1][t] = tr_frag(b0 + 2048 + 512 * t, b1 + 2048 + 512 * t); } \
        _Pragma("unroll") for (int s = 0; s < 28; ++s) { \
            SB0(); \
            if (!(PART)) { \
                if (s == 10) { vmw<10>(); bar(); }        \
                if (s == 19) {     \
                    if (w < 4) { if (!__all(gatev == tag || tid >= 224)) gate_spin(f2_off + 8 * 16384, tid < 224); } \
                    bar(); \
                } \
            } else { \
                if (s == 3) { vmw<2>(); bar(); if (tid == 0) __hip_atomic_store((GAS u32*)(ws + WS_F3 + (P * 16 + S) * 64), tag, RLX_AGENT); }     \
                if (s == 20) {     \
                    if (w == 0) { if (!__all(gatev == tag)) gate_spin(f3_off, true); } \
                    bar(); \
                } \
            } \
            SB0(); \
            if (s + 2 < 28) { _Pragma("unroll") for (int t = 0; t < 2; ++t) Bq[(s + 2) % 3][t] = tr_frag(b0 + (s + 2) * 2048 + 512 * t, b1 + (s + 2) * 2048 + 512 * t); } \
            if ((s & 3) == 0) Of = tr_frag(s0_ + s * 2048, s1_ + s * 2048);        \
            if (!(PART)) { \
                if (s < 14) { Wf[14 + s] = __builtin_amdgcn_raw_buffer_load_b128(rsW, l16 + ((14 + s)) * 1024 % 4096, wf_off + ((14 + s)) * 1024 / 4096 * 4096, SC1); } \
                else if (s == 14) { if (w < 4) gatev = __builtin_amdgcn_raw_buffer_load_b32(rsW, f2_off + 8 * 16384, 0, SC1); } \
                else if (s < 19) { Wd[s - 15] = __builtin_amdgcn_raw_buffer_load_b128(rsW, l16 + ((s - 15)) * 1024 % 4096, wd_off + ((s - 15)) * 1024 / 4096 * 4096, SC1); } \
                else if (s < 26) bt_piece(1, s - 19); \
            } else { \
                if (s >= 1 && s < 25) Wd[3 + s] = __builtin_amdgcn_raw_buffer_load_b128(rsW, l16 + (3 + s) * 1024 % 4096, wd_off + (3 + s) * 1024 / 4096 * 4096, SC1); \
                if (s == 15) { if (w == 0) gatev = __builtin_amdgcn_raw_buffer_load_b32(rsW, f3_off, 0, SC1); } \
                if (s >= 20 && s < 27) { fence(); hh_dma(2 * (s - 20)); } \
                if (s >= 25) { fence(); hh_dma(14 + 2 * (s - 25)); }        \
            } \
            _Pragma("unroll") for (int t = 0; t < 2; ++t) acc[t] = mfma32(Wf[s], Bq[s % 3][t], acc[t]); \
            if ((s & 3) == 2) { \
                _Pragma("unroll") for (int e = 0; e < 4; e += 2) { \
                    const float l0 = bflo(Of[e]), h0 = bfhi(Of[e]), l1 = bflo(Of[e + 1]), h1 = bfhi(Of[e + 1]); \
                    sa0 += l0; sq0 = __builtin_fmaf(l0, l0, sq0); sa1 += l1; sq1 = __builtin_fmaf(l1, l1, sq1); \
                    sa0 += h0; sq0 = __builtin_fmaf(h0, h0, sq0); sa1 += h1; sq1 = __builtin_fmaf(h1, h1, sq1); \
                } \
                asm volatile("" : "+v"(sa0), "+v"(sa1), "+v"(sq0), "+v"(sq1));        \
                SGB(M_MFMA, 1); SGB(M_DSR, 2); SGB(M_VALU, 12); SGB(M_VMEM, 1); SGB(M_MFMA, 1); SGB(M_DSR, 2); SGB(M_VALU, 12); \
            } else { SGB(M_MFMA, 1); SGB(M_DSR, 3); SGB(M_VMEM, 1); SGB(M_MFMA, 1); SGB(M_DSR, 3); } \
        } \
        SB0(); \
        { \
            const float sh = half_sum(sa0 + sa1), sq = half_sum(sq0 + sq1); \
            const u32 wa_ = opaque(SPQ + (PART) * 2048 + (w & 1) * 1024 + (w >> 1) * 4 + lane * 32); \
            if (lane < 32) { *lds<float>(wa_) = sh; *lds<float>(wa_ + 16) = sq; if (!(PART)) *lds<float>(CSL + w * 128 + lane * 4) = csum; } \
        } \
        bar(); \
        {     \
            const u32 ca_ = opaque(CSL + w * 128 + trh * 16), sa_ = opaque(SPQ + (PART) * 2048 + (lane & 31) * 32); \
            const f32x4 csg[2] = {*lds<f32x4>(ca_), *lds<f32x4>(ca_ + 32)}, csu[2] = {*lds<f32x4>(ca_ + 64), *lds<f32x4>(ca_ + 96)}; \
            const f32x4 sm[2] = {*lds<f32x4>(sa_), *lds<f32x4>(sa_ + 1024)}; \
            const f32x4 sq4 = *lds<f32x4>(sa_ + hp * 1024 + 16); \
            const u32 lc_ = 4 * hp + ((lane & 31) >> 3);        \
            const u32 h0_ = opaque(((PART) ? BTB : BTA) + (8 * dtile + 4 * td + kq) * 1024 + (lc_ >> 2) * 512 + trh * 256 + 16 * ((lc_ & 3) ^ ((2 * kq + trh) & 3)) + (lane & 7) * 2); \
            u32 hb_[4]; \
            _Pragma("unroll") for (int e = 0; e < 4; ++e) hb_[e] = *lds<u16>(h0_ + 64 * e); \
            SB0(); \
            _Pragma("unroll") for (int t = 0; t < 2; ++t) { \
                const float m = rbf(((sm[t][0] + sm[t][1]) + (sm[t][2] + sm[t][3])) * (1.f / 448.f)); \
                float hv[8]; \
                _Pragma("unroll") for (int e = 0; e < 8; ++e) hv[e] = (acc[t][e] - m * csg[e >> 2][e & 3]) * (acc[t][e + 8] - m * csu[e >> 2][e & 3]); \
                __builtin_amdgcn_raw_buffer_store_b128(v4u{pk2(hv[0], hv[1]), pk2(hv[2], hv[3]), pk2(hv[4], hv[5]), pk2(hv[6], hv[7])}, rsW, l16 + hh_out + (2 * (PART) + t) * 1024, 0, SC1); \
            } \
            { \
                const f32x4 s4 = hp ? sm[1] : sm[0]; \
                const float mean = ((s4[0] + s4[1]) + (s4[2] + s4[3])) * (1.f / 448.f), var = fmaxf(((sq4[0] + sq4[1]) + (sq4[2] + sq4[3])) * (1.f / 448.f) - mean * mean, 0.f); \
                const float sd = bfhi(ln_pack(mean, var)); \
                rs[PART] = __builtin_amdgcn_rcpf(sd * sd); \
                hres[PART][0] = hb_[0] | (hb_[1] << 16); hres[PART][1] = hb_[2] | (hb_[3] << 16); \
            } \
        } \
    } while (0)
    GEMM1_PASS(0);
    vmw<2>();
    bar();
    GEMM1_PASS(1);
#undef GEMM1_PASS
    vmw<2>();
    bar();
    f32x16 o2[2];
#pragma unroll
    for (int i = 0; i < 16; ++i) { o2[0][i] = 0.f; o2[1][i] = 0.f; }
    const u32 hrdA = opaque(BTA + kq * 1024 + lane * 16), hrdS = opaque(RSP + kq * 1024 + lane * 16), hrdB = opaque(BTB + kq * 1024 + lane * 16);
    auto hh_rd = [&](int u) {
        const u32 sl = hh_slot(u);
        return *lds<v4u>(sl >= RSP ? hrdS + (sl - RSP) : sl >= BTA ? hrdA + (sl - BTA) : hrdB + (sl - BTB));
    };
    const u32 op = (64 * P + 32 * hp + (lane & 31)) * (D_ * 2) + (64 * dtile + 32 * td + 8 * kq + 4 * trh) * 2;
    const u32 red_rd = opaque(td * 12288 + kq * 3072 + lane * 16);
    auto red_give = [&](const f32x16& a, int g4, int part) {
        const u32 sl = g4 == kq ? 24576u : (u32)(td * 12288 + g4 * 3072 + (kq > g4 ? kq - 1 : kq) * 1024);
        *lds<f32x4>(sl + l16 + part * 32768) = f32x4{a[4 * g4], a[4 * g4 + 1], a[4 * g4 + 2], a[4 * g4 + 3]};
    };
    auto red_take = [&](const f32x16& a, int part) {
        const f32x4 p0 = *lds<f32x4>(red_rd + part * 32768), p1 = *lds<f32x4>(red_rd + part * 32768 + 1024), p2 = *lds<f32x4>(red_rd + part * 32768 + 2048);
        f32x4 own;
#pragma unroll
        for (int e = 0; e < 4; ++e) own[e] = kq == 0 ? a[e] : kq == 1 ? a[4 + e] : kq == 2 ? a[8 + e] : a[12 + e];
        const f32x4 v = ((own + p0) + (p1 + p2)) * rs[part];
        const u32 f01 = pk2(v[0], v[1]), f23 = pk2(v[2], v[3]);
        __builtin_amdgcn_raw_buffer_store_b64(v2u{pk2(bflo(hres[part][0]) + bflo(f01), bfhi(hres[part][0]) + bfhi(f01)), pk2(bflo(hres[part][1]) + bflo(f23), bfhi(hres[part][1]) + bfhi(f23))}, rsO, op + part * 1024 * D_ * 2, 0, 0);
    };
    {
        v4u hf[4];
        u32 gatev = 0;
#pragma unroll
        for (int u = 0; u < 3; ++u) hf[u] = hh_rd(u);
#pragma unroll
        for (int u = 0; u < 28; ++u) {
            SB0();
            if (u < 4) hh_dma(20 + 2 * u);
            if (u == 6) { vmw<4>(); bar(); if (tid == 0) __hip_atomic_store((GAS u32*)(ws + WS_F3 + 16384 + (P * 16 + S) * 64), tag, RLX_AGENT); }
            if (u == 17) { vmw<0>(); bar(); }
            SB0();
            if (u + 3 < 28) hf[(u + 3) & 3] = hh_rd(u + 3);
            if (u == 22 && w == 0) gatev = __builtin_amdgcn_raw_buffer_load_b32(rsW, f3_off + 16384, 0, SC1);
            o2[0] = mfma32(Wd[u], hf[u & 3], o2[0]);
        }
        SB0();
        if (w == 0) { if (!__all(gatev == tag)) gate_spin(f3_off + 16384, true); }
        bar();
#pragma unroll
        for (int u = 28; u < 40; u += 2) hh_dma(u);
        vmw<3>(); bar();
#pragma unroll
        for (int u = 0; u < 3; ++u) hf[u] = hh_rd(28 + u);
#pragma unroll
        for (int u = 0; u < 28; ++u) {
            SB0();
            if (u == 3) { vmw<3>(); bar(); }
            if (u == 9) { vmw<5>(); bar(); }
            if (u == 15) { vmw<2>(); bar(); }
            if (u == 21) { vmw<0>(); bar(); }
            if (u < 8) hh_dma(40 + 2 * u);
            SB0();
            if (u + 3 < 28) hf[(u + 3) & 3] = hh_rd(28 + u + 3);
            o2[1] = mfma32(Wd[u], hf[u & 3], o2[1]);
        }
        SB0();
        bar();
    }
#pragma unroll
    for (int g4 = 0; g4 < 4; ++g4) { red_give(o2[0], g4, 0); red_give(o2[1], g4, 1); }
    bar();
    red_take(o2[0], 0);
    red_take(o2[1], 1);
    } else {
        publish_xg();
        const int hb = (j - 28) * 8 + g;
        float cs[8];
        {
            const u16* wsrc = hb < 16 ? wg : wu;
            const int c0 = 112 * (hb & 15) + 8 * (tid % 14), r0 = tid / 14;
#pragma unroll
            for (int e = 0; e < 8; ++e) cs[e] = 0.f;
            v4u t[13];
#pragma unroll
            for (int i = 0; i < 13; ++i) { const int r = r0 + 36 * i; t[i] = (tid < 504 && r < 448) ? *(const v4u*)(wsrc + (size_t)r * F_ + c0) : v4u{0, 0, 0, 0}; }
#pragma unroll
            for (int i = 0; i < 13; ++i) { cs[0] += bflo(t[i].x); cs[1] += bfhi(t[i].x); cs[2] += bflo(t[i].y); cs[3] += bfhi(t[i].y); cs[4] += bflo(t[i].z); cs[5] += bfhi(t[i].z); cs[6] += bflo(t[i].w); cs[7] += bfhi(t[i].w); }
        }
        vmw<0>(); bar();
#pragma unroll
        for (int k = 0; k < 7; ++k) {
            const int gid = hb + 32 * k;
            if (gid < 196) {
                const int dt_ = gid / 28, fg = gid % 28, fsl = w >> 1, td = w & 1;
                u32 ad[2];
#pragma unroll
                for (int t = 0; t < 2; ++t) { const u32 row = 16 * fsl + 8 * t + 4 * trh + trq, chk = 4 * td + 2 * trb + (trp >> 1); ad[t] = k * 8192 + row * 128 + 16 * (chk ^ (((row >> 1) & 1) << 2)) + 8 * (trp & 1); }
                const v4u f = tr_frag(ad[0], ad[1]);
                __builtin_amdgcn_raw_buffer_store_b128(f, rsW, WS_WD + ((dt_ * 2 + td) * 112 + 4 * fg + fsl) * 1024 + lane * 16, 0, SC1);
            }
        }
        if (tid < 504) {
            *lds<f32x4>(65536 + tid * 32) = f32x4{cs[0], cs[1], cs[2], cs[3]};
            *lds<f32x4>(65536 + tid * 32 + 16) = f32x4{cs[4], cs[5], cs[6], cs[7]};
        }
        bar();
        if (tid < 112) {
            float s = 0.f;
            for (int r = 0; r < 36; ++r) s += *lds<float>(65536 + (r * 14 + (tid >> 3)) * 32 + (tid & 7) * 4);
            __hip_atomic_store((GAS u32*)(ws + WS_CS) + ((hb < 16 ? 0 : 1792) + 112 * (hb & 15) + tid), __float_as_uint(s), RLX_AGENT);
        }
        vmw<0>(); bar();
        if (tid == 0) __hip_atomic_store((GAS u32*)(ws + WS_WF + hb * 64), tag, RLX_AGENT);

    }
}

extern "C" void kernel_launch(void* const* d_in, const int* in_sizes, int n_in, void* d_out, int out_size, void* d_ws, size_t ws_size, hipStream_t stream) {
    (void)in_sizes; (void)n_in; (void)out_size; (void)ws_size;
    (void)hipFuncSetAttribute((const void*)fused_kernel, hipFuncAttributeMaxDynamicSharedMemorySize, LDS_BYTES);
    fused_kernel<<<256, 512, LDS_BYTES, stream>>>((const u16*)d_in[0], (const u16*)d_in[1], (const u16*)d_in[2], (const u16*)d_in[3], (const u16*)d_in[4],
        (const u16*)d_in[5], (const u16*)d_in[6], (const u16*)d_in[7], (const u16*)d_in[8], (const u16*)d_in[10], (const u16*)d_in[11], (const u16*)d_in[12],
        (u16*)d_out, (char*)d_ws);
}
```
